```python
import torch

def _softmax(x, axis=-1):
    return torch.softmax(x, dim=axis)
def _gelu(x):
    return torch.nn.functional.gelu(x, approximate="tanh")
def _silu(x):
    return torch.nn.functional.silu(x)
def _sigmoid(x):
    return torch.sigmoid(x)
def _layernorm(x, axis=-1, eps=1e-5):
    m = x.mean(dim=axis, keepdim=True)
    v = x.var(dim=axis, keepdim=True, unbiased=False)
    return (x - m) / torch.sqrt(v + eps)
def _scatter_add(tgt, idx, src, axis=0):
    return tgt.clone().index_add_(axis, idx, src)
def _segment_sum(src, seg, n):
    out = torch.zeros((n,) + src.shape[1:], dtype=src.dtype)
    return out.index_add_(0, seg, src)
def _pad(x, pw):
    flat = tuple(v for pair in reversed(pw) for v in pair)
    return torch.nn.functional.pad(x, flat)

kernel_name = 'standalone__gqa_softmax__near_ridge'

def setup_inputs():

    _w = (lambda a: (lambda t: t.to(torch.bfloat16) if t.is_floating_point() else t)(torch.as_tensor(a)))
    B, Hq, Hkv, Sq, Sk, D = 16, 32, 32, 384, 384, 192
    return {
        "q": _w((lambda *s: torch.randn(*s, dtype=torch.bfloat16) * 0.8)(B, Hq, Sq, D)),
        "k": _w((lambda *s: torch.randn(*s, dtype=torch.bfloat16) * 0.8)(B, Hkv, Sk, D)),
        "v": _w((lambda *s: torch.randn(*s, dtype=torch.bfloat16) * 0.05)(B, Hkv, Sk, D)),
    }

def reference(q, k, v):
    B, Hq, Sq, D = q.shape
    Hkv = k.shape[1]
    rep = Hq // Hkv
    k = (lambda x, n, axis: torch.repeat_interleave(x, n, dim=axis))(k, rep, axis=1)
    v = (lambda x, n, axis: torch.repeat_interleave(x, n, dim=axis))(v, rep, axis=1)
    scale = D ** -0.5
    scores = torch.einsum("bhqd,bhkd->bhqk", q, k) * scale
    p = _softmax(scores, axis=-1)
    return torch.einsum("bhqk,bhkd->bhqd", p, v)
```

```cpp
#include <hip/hip_runtime.h>
#include <hip/hip_bf16.h>
#include <stdint.h>

typedef float    f32x16 __attribute__((ext_vector_type(16)));
typedef float    f32x4  __attribute__((ext_vector_type(4)));
typedef float    f32x2  __attribute__((ext_vector_type(2)));
typedef __bf16   bf16x8 __attribute__((ext_vector_type(8)));
typedef __bf16   bf16x4 __attribute__((ext_vector_type(4)));
typedef __bf16   bf16x2 __attribute__((ext_vector_type(2)));
typedef short    s16x4  __attribute__((ext_vector_type(4)));
typedef unsigned u32x4  __attribute__((ext_vector_type(4)));
typedef unsigned u32x2  __attribute__((ext_vector_type(2)));
#define DI __device__ __forceinline__
#define LDSP __attribute__((address_space(3)))

constexpr int NB = 16, NH = 32, SQ = 384, SK = 384, D = 192;
constexpr int NHEADS = NB * NH;
constexpr unsigned ROWB = D * 2;
constexpr unsigned HEADB = SK * ROWB;
constexpr int KT = 32;
constexpr int NT = SK / KT;
constexpr unsigned TILEB = KT * ROWB;
constexpr unsigned SLOTB = 2 * TILEB;
constexpr int NSLOT = 3;
constexpr unsigned LDSB = NSLOT * SLOTB;
constexpr float CSC = 0.07216878364870322f * 1.4426950408889634f;
constexpr float LTHR = 1073741824.f;

DI unsigned rfl(unsigned x) { return __builtin_amdgcn_readfirstlane(x); }
DI unsigned cvtpk(float lo, float hi) { f32x2 v = {lo, hi}; bf16x2 b = __builtin_convertvector(v, bf16x2); return __builtin_bit_cast(unsigned, b); }
#define MFMA32(a, b, c) __builtin_amdgcn_mfma_f32_32x32x16_bf16((a), (b), (c), 0, 0, 0)
#define MFMA16(a, b, c) __builtin_amdgcn_mfma_f32_16x16x32_bf16((a), (b), (c), 0, 0, 0)

DI void dma16(unsigned m0v, unsigned voff, __amdgpu_buffer_rsrc_t srd, unsigned soff) {
  asm volatile("s_mov_b32 m0, %0\n\ts_nop 0\n\tbuffer_load_dwordx4 %1, %2, %3 offen lds" :: "s"(m0v), "v"(voff), "s"(srd), "s"(soff) : "memory");
}

__device__ __forceinline__ void slow_block(const bf16x8 (&qf)[12], const __bf16* __restrict__ Kh, const __bf16* __restrict__ Vh,
                                        f32x16 (&o)[6], float& lsum, int lane) {
  const int r = lane & 31, h = lane >> 5;
  float m = -INFINITY, l = 0.f;
  #pragma unroll
  for (int db = 0; db < 6; ++db) o[db] = f32x16{};
  for (int t = 0; t < NT; ++t) {
    f32x16 s = {};
    unsigned ko = (unsigned)((t * KT + r) * D + 8 * h);
    #pragma unroll
    for (int st = 0; st < 12; ++st) {
      asm volatile("" : "+v"(ko));
      bf16x8 kf = *(const bf16x8*)(Kh + ko + 16 * st);
      s = MFMA32(kf, qf[st], s);
      __builtin_amdgcn_sched_barrier(0);
    }
    float mx = s[0];
    #pragma unroll
    for (int i = 1; i < 16; ++i) mx = fmaxf(mx, s[i]);
    mx = fmaxf(mx, __shfl_xor(mx, 32));
    const float mn = fmaxf(m, mx);
    const float al = (m == mn) ? 1.f : __builtin_amdgcn_exp2f((m - mn) * CSC);
    m = mn;
    float ps = 0.f;
    #pragma unroll
    for (int i = 0; i < 16; ++i) { s[i] = __builtin_amdgcn_exp2f((s[i] - mn) * CSC); }
    u32x4 pw[2];
    #pragma unroll
    for (int i = 0; i < 8; ++i) { unsigned w = cvtpk(s[2 * i], s[2 * i + 1]); pw[i >> 2][i & 3] = w;
      ps += __uint_as_float(w << 16) + __uint_as_float(w & 0xffff0000u); }
    ps += __shfl_xor(ps, 32);
    l = l * al + ps;
    #pragma unroll
    for (int db = 0; db < 6; ++db) {
      #pragma unroll
      for (int i = 0; i < 16; ++i) o[db][i] *= al;
      #pragma unroll
      for (int ks = 0; ks < 2; ++ks) {
        unsigned vo = (unsigned)((t * KT + 16 * ks + 4 * h) * D + 32 * db + r);
        asm volatile("" : "+v"(vo));
        bf16x8 vf;
        #pragma unroll
        for (int j = 0; j < 8; ++j) vf[j] = Vh[vo + (8 * (j >> 2) + (j & 3)) * D];
        o[db] = MFMA32(vf, __builtin_bit_cast(bf16x8, pw[ks]), o[db]);
        __builtin_amdgcn_sched_barrier(0);
      }
    }
  }
  lsum = l;
}

__global__ __launch_bounds__(256, 2)
void attn_v0(const __bf16* __restrict__ Q, const __bf16* __restrict__ K, const __bf16* __restrict__ V, __bf16* __restrict__ O) {
  extern __shared__ __attribute__((aligned(16))) char lds[];
  const unsigned tid = threadIdx.x, lane = tid & 63u, r32 = lane & 31u, hi = lane >> 5;
  const unsigned wid = rfl(tid >> 6);
  const unsigned bid = blockIdx.x, xcd = bid & 7u, idx = bid >> 3;
  const unsigned hl = idx / 3u, part = idx - hl * 3u, head = hl * 8u + xcd;
  const unsigned ldsb = (unsigned)(size_t)lds;
  const __bf16* Kh = K + (size_t)head * SK * D;
  const __bf16* Vh = V + (size_t)head * SK * D;
  const unsigned qrow = part * 128u + wid * 32u + r32;
  const __bf16* Qr = Q + ((size_t)head * SQ + qrow) * D;
  __amdgpu_buffer_rsrc_t srdK = __builtin_amdgcn_make_buffer_rsrc((void*)Kh, (short)0, (int)HEADB, 0x00020000);
  __amdgpu_buffer_rsrc_t srdV = __builtin_amdgcn_make_buffer_rsrc((void*)Vh, (short)0, (int)HEADB, 0x00020000);
  unsigned vk[3], vv[3];
  #pragma unroll
  for (int j = 0; j < 3; ++j) {
    const unsigned g = 64u * (3u * wid + j) + lane;
    vk[j] = 16u * ((g & ~7u) | ((g & 7u) ^ ((g / 48u) & 7u)));
    vv[j] = 16u * ((g & ~7u) | ((g & 7u) ^ (((g / 48u) & 1u) << 2)));
  }
  const unsigned m0base = ldsb + 3072u * wid;
  auto issue_tile = [&](int t, unsigned slotoff) {
    const unsigned so = (unsigned)t * TILEB;
    #pragma unroll
    for (int j = 0; j < 3; ++j) dma16(rfl(m0base + slotoff + 1024u * j), vk[j], srdK, so);
    #pragma unroll
    for (int j = 0; j < 3; ++j) dma16(rfl(m0base + slotoff + TILEB + 1024u * j), vv[j], srdV, so);
  };
  issue_tile(0, 0);
  bf16x8 qf[12];
  #pragma unroll
  for (int s = 0; s < 12; ++s) qf[s] = *(const bf16x8*)(Qr + 16 * s + 8 * hi);
  issue_tile(1, SLOTB);
  unsigned kb[4];
  #pragma unroll
  for (int j = 0; j < 4; ++j) kb[j] = r32 * ROWB + 16u * ((2u * j + hi) ^ ((r32 >> 1) & 7u));
  const unsigned qp = (lane >> 2) & 3u, xx = (qp >> 1) & 1u, blk = (lane >> 4) & 1u, pp = lane & 3u;
  const unsigned vbe = TILEB + (4u * hi + qp) * ROWB + 64u * xx + 32u * blk + 8u * pp;
  const unsigned vbo = vbe - 128u * xx;
  const unsigned row16 = lane & 15u, grp = lane >> 4;
  const unsigned selw = (((row16 & 3u) == 0u) && (((row16 >> 2) & 1u) == (grp & 1u))) ? 0x3F803F80u : 0u;
  const u32x4 selv = {selw, selw, selw, selw};
  const bf16x8 sel = __builtin_bit_cast(bf16x8, selv);

  f32x16 o[6];
  #pragma unroll
  for (int db = 0; db < 6; ++db) o[db] = f32x16{};
  f32x4 lacc = {};
  float nmc = 0.f;
  unsigned slot = 0;
  for (int t = 0; t < NT; ++t) {
    if (t + 1 < NT) asm volatile("s_waitcnt vmcnt(6) lgkmcnt(0)\n\ts_barrier" ::: "memory");
    else            asm volatile("s_waitcnt vmcnt(0) lgkmcnt(0)\n\ts_barrier" ::: "memory");
    if (t + 2 < NT) { unsigned ns = slot + 2 * SLOTB; if (ns >= LDSB) ns -= LDSB; issue_tile(t + 2, ns); }
    const LDSP char* sb = (const LDSP char*)lds + slot;
    f32x16 s = {};
    #pragma unroll
    for (int st = 0; st < 12; ++st) {
      bf16x8 kf = *(const LDSP bf16x8*)(sb + kb[st & 3] + 128 * (st >> 2));
      s = MFMA32(kf, qf[st], s);
    }
    if (t == 0) {
      float mx = s[0];
      #pragma unroll
      for (int i = 1; i < 16; ++i) mx = fmaxf(mx, s[i]);
      auto rr = __builtin_amdgcn_permlane32_swap(__float_as_uint(mx), __float_as_uint(mx), false, false);
      mx = fmaxf(__uint_as_float(rr[0]), __uint_as_float(rr[1]));
      nmc = -mx * CSC;
    }
    u32x4 pw[2];
    #pragma unroll
    for (int i = 0; i < 8; ++i) {
      const float p0 = __builtin_amdgcn_exp2f(__builtin_fmaf(s[2 * i], CSC, nmc));
      const float p1 = __builtin_amdgcn_exp2f(__builtin_fmaf(s[2 * i + 1], CSC, nmc));
      pw[i >> 2][i & 3] = cvtpk(p0, p1);
    }
    #pragma unroll
    for (int ks = 0; ks < 2; ++ks) {
      const bf16x8 pf = __builtin_bit_cast(bf16x8, pw[ks]);
      lacc = MFMA16(sel, pf, lacc);
      #pragma unroll
      for (int db = 0; db < 6; ++db) {
        const LDSP char* vp = sb + ((db & 1) ? vbo : vbe) + (16 * ks) * ROWB + 64 * db;
        s16x4 lo = __builtin_bit_cast(s16x4, __builtin_amdgcn_ds_read_tr16_b64_v4i16((LDSP s16x4*)(vp)));
        s16x4 up = __builtin_bit_cast(s16x4, __builtin_amdgcn_ds_read_tr16_b64_v4i16((LDSP s16x4*)(vp + 8 * ROWB)));
        const bf16x8 vf = __builtin_bit_cast(bf16x8, __builtin_shufflevector(lo, up, 0, 1, 2, 3, 4, 5, 6, 7));
        o[db] = MFMA32(vf, pf, o[db]);
      }
    }
    slot += SLOTB; if (slot >= LDSB) slot -= LDSB;
  }
  float l = lacc[0];
  if (__builtin_expect(__any(!(l <= LTHR)), 0)) {
    slow_block(qf, Kh, Vh, o, l, lane);
  }
  const float il = 1.f / l;
  char* Or = (char*)(O + ((size_t)head * SQ + qrow) * D) + 16u * hi;
  #pragma unroll
  for (int db = 0; db < 6; ++db) {
    #pragma unroll
    for (int gp = 0; gp < 2; ++gp) {
      unsigned a0 = cvtpk(o[db][8 * gp + 0] * il, o[db][8 * gp + 1] * il), a1 = cvtpk(o[db][8 * gp + 2] * il, o[db][8 * gp + 3] * il);
      unsigned b0 = cvtpk(o[db][8 * gp + 4] * il, o[db][8 * gp + 5] * il), b1 = cvtpk(o[db][8 * gp + 6] * il, o[db][8 * gp + 7] * il);
      auto r0 = __builtin_amdgcn_permlane32_swap(a0, b0, false, false);
      auto r1 = __builtin_amdgcn_permlane32_swap(a1, b1, false, false);
      u32x4 w = {r0[0], r1[0], r0[1], r1[1]};
      *(u32x4*)(Or + 64 * db + 32 * gp) = w;
    }
  }
}

extern "C" void kernel_launch(void* const* d_in, const int* in_sizes, int n_in, void* d_out, int out_size, void* d_ws, size_t ws_size, hipStream_t stream) {
  (void)in_sizes; (void)n_in; (void)out_size; (void)d_ws; (void)ws_size;
  (void)hipFuncSetAttribute((const void*)attn_v0, hipFuncAttributeMaxDynamicSharedMemorySize, (int)LDSB);
  hipLaunchKernelGGL(attn_v0, dim3(NHEADS * 3), dim3(256), LDSB, stream,
                     (const __bf16*)d_in[0], (const __bf16*)d_in[1], (const __bf16*)d_in[2], (__bf16*)d_out);
}
```

```cpp
#include <hip/hip_runtime.h>
#include <hip/hip_bf16.h>
#include <stdint.h>

typedef float    f32x16 __attribute__((ext_vector_type(16)));
typedef float    f32x2  __attribute__((ext_vector_type(2)));
typedef __bf16   bf16x8 __attribute__((ext_vector_type(8)));
typedef __bf16   bf16x2 __attribute__((ext_vector_type(2)));
typedef unsigned u32x4  __attribute__((ext_vector_type(4)));
#define DI __device__ __forceinline__
#define SBAR() __builtin_amdgcn_sched_barrier(0)

constexpr int NB = 16, NH = 32, SQ = 384, SK = 384, D = 192;
constexpr int NHEADS = NB * NH;
constexpr unsigned ROWB = D * 2;
constexpr unsigned HEADB = SK * ROWB;
constexpr int KT = 32, NT = SK / KT;
constexpr unsigned TILEB = KT * ROWB;
constexpr unsigned VRING = 6 * TILEB;
constexpr unsigned STG = 10 * TILEB, STGW = 8448;
constexpr unsigned LDSB = STG + 4 * STGW;
constexpr float CSC = 0.07216878364870322f * 1.4426950408889634f;

DI unsigned rfl(unsigned x) { return __builtin_amdgcn_readfirstlane(x); }
DI unsigned cvtpk(float lo, float hi) { f32x2 v = {lo, hi}; bf16x2 b = __builtin_convertvector(v, bf16x2); return __builtin_bit_cast(unsigned, b); }
#define MFMA32(a, b, c) __builtin_amdgcn_mfma_f32_32x32x16_bf16((a), (b), (c), 0, 0, 0)

DI void slow_block_impl(const __bf16* __restrict__ Qr, const __bf16* __restrict__ Kh, const __bf16* __restrict__ Vh, __bf16* __restrict__ Or, int lane) {
  const int r = lane & 31, h = lane >> 5;
  float m = -INFINITY, l = 0.f;
  f32x16 o[6];
  #pragma unroll
  for (int db = 0; db < 6; ++db) o[db] = f32x16{};
  for (int t = 0; t < NT; ++t) {
    f32x16 s = {};
    unsigned ko = (unsigned)((t * KT + r) * D + 8 * h), qo = 8 * h;
    #pragma unroll
    for (int st = 0; st < 12; ++st) {
      asm volatile("" : "+v"(ko), "+v"(qo));
      bf16x8 kf = *(const bf16x8*)(Kh + ko + 16 * st);
      bf16x8 qf = *(const bf16x8*)(Qr + qo + 16 * st);
      s = MFMA32(kf, qf, s);
      SBAR();
    }
    float mx = s[0];
    #pragma unroll
    for (int i = 1; i < 16; ++i) mx = fmaxf(mx, s[i]);
    mx = fmaxf(mx, __shfl_xor(mx, 32));
    const float mn = fmaxf(m, mx);
    const float al = (m == mn) ? 1.f : __builtin_amdgcn_exp2f((m - mn) * CSC);
    m = mn;
    float ps = 0.f;
    #pragma unroll
    for (int i = 0; i < 16; ++i) { s[i] = __builtin_amdgcn_exp2f((s[i] - mn) * CSC); }
    u32x4 pw[2];
    #pragma unroll
    for (int i = 0; i < 8; ++i) { unsigned w = cvtpk(s[2 * i], s[2 * i + 1]); pw[i >> 2][i & 3] = w;
      ps += __uint_as_float(w << 16) + __uint_as_float(w & 0xffff0000u); }
    ps += __shfl_xor(ps, 32);
    l = l * al + ps;
    #pragma unroll
    for (int db = 0; db < 6; ++db) {
      #pragma unroll
      for (int i = 0; i < 16; ++i) o[db][i] *= al;
      #pragma unroll
      for (int ks = 0; ks < 2; ++ks) {
        unsigned vo = (unsigned)((t * KT + 16 * ks + 4 * h) * D + 32 * db + r);
        asm volatile("" : "+v"(vo));
        bf16x8 vf;
        #pragma unroll
        for (int j = 0; j < 8; ++j) vf[j] = Vh[vo + (8 * (j >> 2) + (j & 3)) * D];
        o[db] = MFMA32(vf, __builtin_bit_cast(bf16x8, pw[ks]), o[db]);
        SBAR();
      }
    }
  }
  const float il = 1.f / l;
  #pragma unroll
  for (int db = 0; db < 6; ++db)
    #pragma unroll
    for (int g = 0; g < 4; ++g) {
      uint2 w; w.x = cvtpk(o[db][4 * g] * il, o[db][4 * g + 1] * il); w.y = cvtpk(o[db][4 * g + 2] * il, o[db][4 * g + 3] * il);
      *(uint2*)(Or + 32 * db + 8 * g + 4 * h) = w;
    }
}

__global__ __launch_bounds__(256, 1)
void gqa_softmax_attn_kernel(const __bf16* __restrict__ Q, const __bf16* __restrict__ K, const __bf16* __restrict__ V, __bf16* __restrict__ O) {
  extern __shared__ __attribute__((aligned(16))) char lds[];
  const unsigned tid = threadIdx.x, lane = tid & 63u, r16 = lane & 15u, g4 = lane >> 4;
  const unsigned wid = rfl(tid >> 6);
  const unsigned bid = blockIdx.x, xcd = bid & 7u, jj = bid >> 3;
  const unsigned pair = jj >> 1, half = jj & 1u, head0 = pair * 8u + xcd;
  const unsigned ldsb = (unsigned)(size_t)lds;
  unsigned vk[3], vv[3], vq[2], kb[2][2], vb[4];
  #pragma unroll
  for (int j = 0; j < 3; ++j) {
    const unsigned g = 64u * (3u * wid + j) + lane;
    vk[j] = 16u * ((g & ~7u) | ((g & 7u) ^ ((g / 48u) & 7u)));
    vv[j] = 16u * ((g & ~7u) | ((g & 7u) ^ (((g / 48u) & 3u) << 1)));
  }
  #pragma unroll
  for (int h = 0; h < 2; ++h) { const unsigned row = 8u * h + (lane >> 3); vq[h] = row * ROWB + 16u * ((lane & 7u) ^ ((row >> 1) & 7u)); }
  const unsigned m0b = rfl(ldsb + 3072u * wid);
  const unsigned stg = ldsb + STG + wid * STGW;
  const unsigned m0q = rfl(stg + 2304u);
  const unsigned m0p = rfl(ldsb + 3u * TILEB + wid * 12288u);
  const unsigned hb = rfl(head0 * HEADB);
  const unsigned qb0 = rfl(head0 * HEADB + (half * 192u + wid * 48u) * ROWB);
  asm volatile(
      "s_mov_b64 s[40:41], %[kp]\n"
      "s_and_b32 s41, s41, 0xffff\n"
      "s_mov_b32 s42, 75497472\n"
      "s_mov_b32 s43, 0x20000\n"
      "s_mov_b64 s[48:49], %[qp]\n"
      "s_and_b32 s49, s49, 0xffff\n"
      "s_mov_b32 s50, 75497472\n"
      "s_mov_b32 s51, 0x20000\n"
      "s_mov_b64 s[44:45], %[vp]\n"
      "s_and_b32 s45, s45, 0xffff\n"
      "s_mov_b32 s46, 75497472\n"
      "s_mov_b32 s47, 0x20000\n"
      "s_mov_b32 s68, %[hb]\n"
      "s_mov_b32 s69, %[qb0]\n"
      "s_add_u32 s70, s68, 0\n"
      "s_add_u32 m0, %[m0b], 0\n"
      "s_nop 0\n"
      "buffer_load_dwordx4 %[vk0], s[40:43], s70 offen nt lds\n"
      "s_add_u32 m0, %[m0b], 1024\n"
      "s_nop 0\n"
      "buffer_load_dwordx4 %[vk1], s[40:43], s70 offen nt lds\n"
      "s_add_u32 m0, %[m0b], 2048\n"
      "s_nop 0\n"
      "buffer_load_dwordx4 %[vk2], s[40:43], s70 offen nt lds\n"
      "s_add_u32 s72, s69, 0\n"
      "s_add_u32 m0, %[m0q], 0\n"
      "s_nop 0\n"
      "buffer_load_dwordx4 %[vq0], s[48:51], s72 offen nt lds\n"
      "s_add_u32 m0, %[m0q], 1024\n"
      "s_nop 0\n"
      "buffer_load_dwordx4 %[vq1], s[48:51], s72 offen nt lds\n"
      "s_add_u32 s72, s69, 6144\n"
      "s_add_u32 m0, %[m0q], 2048\n"
      "s_nop 0\n"
      "buffer_load_dwordx4 %[vq0], s[48:51], s72 offen nt lds\n"
      "s_add_u32 m0, %[m0q], 3072\n"
      "s_nop 0\n"
      "buffer_load_dwordx4 %[vq1], s[48:51], s72 offen nt lds\n"
      "s_add_u32 s72, s69, 12288\n"
      "s_add_u32 m0, %[m0q], 4096\n"
      "s_nop 0\n"
      "buffer_load_dwordx4 %[vq0], s[48:51], s72 offen nt lds\n"
      "s_add_u32 m0, %[m0q], 5120\n"
      "s_nop 0\n"
      "buffer_load_dwordx4 %[vq1], s[48:51], s72 offen nt lds\n"
      "s_add_u32 s72, s69, 128\n"
      "s_add_u32 m0, %[m0p], 0\n"
      "s_nop 0\n"
      "buffer_load_dwordx4 %[vq0], s[48:51], s72 offen nt lds\n"
      "s_add_u32 m0, %[m0p], 1024\n"
      "s_nop 0\n"
      "buffer_load_dwordx4 %[vq1], s[48:51], s72 offen nt lds\n"
      "s_add_u32 s72, s69, 6272\n"
      "s_add_u32 m0, %[m0p], 2048\n"
      "s_nop 0\n"
      "buffer_load_dwordx4 %[vq0], s[48:51], s72 offen nt lds\n"
      "s_add_u32 m0, %[m0p], 3072\n"
      "s_nop 0\n"
      "buffer_load_dwordx4 %[vq1], s[48:51], s72 offen nt lds\n"
      "s_add_u32 s72, s69, 12416\n"
      "s_add_u32 m0, %[m0p], 4096\n"
      "s_nop 0\n"
      "buffer_load_dwordx4 %[vq0], s[48:51], s72 offen nt lds\n"
      "s_add_u32 m0, %[m0p], 5120\n"
      "s_nop 0\n"
      "buffer_load_dwordx4 %[vq1], s[48:51], s72 offen nt lds\n"
      "s_add_u32 s72, s69, 256\n"
      "s_add_u32 m0, %[m0p], 6144\n"
      "s_nop 0\n"
      "buffer_load_dwordx4 %[vq0], s[48:51], s72 offen nt lds\n"
      "s_add_u32 m0, %[m0p], 7168\n"
      "s_nop 0\n"
      "buffer_load_dwordx4 %[vq1], s[48:51], s72 offen nt lds\n"
      "s_add_u32 s72, s69, 6400\n"
      "s_add_u32 m0, %[m0p], 8192\n"
      "s_nop 0\n"
      "buffer_load_dwordx4 %[vq0], s[48:51], s72 offen nt lds\n"
      "s_add_u32 m0, %[m0p], 9216\n"
      "s_nop 0\n"
      "buffer_load_dwordx4 %[vq1], s[48:51], s72 offen nt lds\n"
      "s_add_u32 s72, s69, 12544\n"
      "s_add_u32 m0, %[m0p], 10240\n"
      "s_nop 0\n"
      "buffer_load_dwordx4 %[vq0], s[48:51], s72 offen nt lds\n"
      "s_add_u32 m0, %[m0p], 11264\n"
      "s_nop 0\n"
      "buffer_load_dwordx4 %[vq1], s[48:51], s72 offen nt lds\n"
      "s_add_u32 s70, s68, 12288\n"
      "s_add_u32 m0, %[m0b], 12288\n"
      "s_nop 0\n"
      "buffer_load_dwordx4 %[vk0], s[40:43], s70 offen nt lds\n"
      "s_add_u32 m0, %[m0b], 13312\n"
      "s_nop 0\n"
      "buffer_load_dwordx4 %[vk1], s[40:43], s70 offen nt lds\n"
      "s_add_u32 m0, %[m0b], 14336\n"
      "s_nop 0\n"
      "buffer_load_dwordx4 %[vk2], s[40:43], s70 offen nt lds\n"
      "s_add_u32 s70, s68, 24576\n"
      "s_add_u32 m0, %[m0b], 24576\n"
      "s_nop 0\n"
      "buffer_load_dwordx4 %[vk0], s[40:43], s70 offen nt lds\n"
      "s_add_u32 m0, %[m0b], 25600\n"
      "s_nop 0\n"
      "buffer_load_dwordx4 %[vk1], s[40:43], s70 offen nt lds\n"
      "s_add_u32 m0, %[m0b], 26624\n"
      "s_nop 0\n"
      "buffer_load_dwordx4 %[vk2], s[40:43], s70 offen nt lds\n"
      "s_add_u32 s71, s68, 0\n"
      "s_add_u32 m0, %[m0b], 98304\n"
      "s_nop 0\n"
      "buffer_load_dwordx4 %[vv0], s[44:47], s71 offen nt lds\n"
      "s_add_u32 m0, %[m0b], 99328\n"
      "s_nop 0\n"
      "buffer_load_dwordx4 %[vv1], s[44:47], s71 offen nt lds\n"
      "s_add_u32 m0, %[m0b], 100352\n"
      "s_nop 0\n"
      "buffer_load_dwordx4 %[vv2], s[44:47], s71 offen nt lds\n"
      "s_add_u32 s71, s68, 12288\n"
      "s_add_u32 m0, %[m0b], 110592\n"
      "s_nop 0\n"
      "buffer_load_dwordx4 %[vv0], s[44:47], s71 offen nt lds\n"
      "s_add_u32 m0, %[m0b], 111616\n"
      "s_nop 0\n"
      "buffer_load_dwordx4 %[vv1], s[44:47], s71 offen nt lds\n"
      "s_add_u32 m0, %[m0b], 112640\n"
      "s_nop 0\n"
      "buffer_load_dwordx4 %[vv2], s[44:47], s71 offen nt lds\n"
      :: [kp] "s"(K), [qp] "s"(Q), [vp] "s"(V), [hb] "s"(hb), [qb0] "s"(qb0), [m0b] "s"(m0b), [m0q] "s"(m0q), [m0p] "s"(m0p),
         [vk0] "v"(vk[0]), [vk1] "v"(vk[1]), [vk2] "v"(vk[2]), [vv0] "v"(vv[0]), [vv1] "v"(vv[1]), [vv2] "v"(vv[2]), [vq0] "v"(vq[0]), [vq1] "v"(vq[1])
      : "memory", "scc", "m0", "s40", "s41", "s42", "s43", "s44", "s45", "s46", "s47", "s48", "s49", "s50", "s51", "s68", "s69", "s70", "s71", "s72");
  __builtin_amdgcn_sched_barrier(0);
  const unsigned spq = rfl(m0p - m0q);
  const unsigned fk = (r16 >> 1) & 7u;
  kb[0][0] = ldsb + r16 * ROWB + 16u * (g4 ^ fk);
  kb[1][0] = ldsb + r16 * ROWB + 16u * ((4u + g4) ^ fk);
  kb[0][1] = kb[0][0] + 3u * TILEB; kb[1][1] = kb[1][0] + 3u * TILEB;
  const unsigned qs0 = m0q + r16 * 128u + 16u * (g4 ^ fk), qs1 = m0q + r16 * 128u + 16u * ((4u + g4) ^ fk);
  const unsigned qp = (lane >> 2) & 3u, pp = lane & 3u, hv = (2u * g4 + (qp >> 1)) & 3u;
  #pragma unroll
  for (int u = 0; u < 4; ++u) vb[u] = ldsb + VRING + (4u * g4 + qp) * ROWB + 32u * ((unsigned)u ^ hv) + 8u * pp;
  const unsigned ow = stg + r16 * 144u + 8u * g4;
  const unsigned orb = stg + (lane >> 3) * 144u + (lane & 7u) * 16u;
  const unsigned ost = (lane >> 3) * ROWB + (lane & 7u) * 16u;
  unsigned long long flagA, flagB;
  asm volatile(
      "L_start%=:\n"
      "s_load_dwordx2 s[48:49], %[ka], 0\n"
      "s_load_dwordx2 s[40:41], %[ka], 8\n"
      "s_load_dwordx2 s[44:45], %[ka], 16\n"
      "s_load_dwordx2 s[52:53], %[ka], 24\n"
      "s_load_dwordx2 s[64:65], %[ka], 0\n"
      "s_load_dwordx2 s[56:57], %[ka], 8\n"
      "s_load_dwordx2 s[60:61], %[ka], 16\n"
      "s_load_dwordx2 s[80:81], %[ka], 0\n"
      "s_load_dwordx2 s[76:77], %[ka], 24\n"
      "s_load_dwordx2 s[84:85], %[ka], 24\n"
      "s_waitcnt lgkmcnt(0)\n"
      "s_and_b32 s49, s49, 0xffff\n"
      "s_mov_b32 s50, 75497472\n"
      "s_mov_b32 s51, 0x20000\n"
      "s_and_b32 s41, s41, 0xffff\n"
      "s_mov_b32 s42, 75497472\n"
      "s_mov_b32 s43, 0x20000\n"
      "s_and_b32 s45, s45, 0xffff\n"
      "s_mov_b32 s46, 75497472\n"
      "s_mov_b32 s47, 0x20000\n"
      "s_and_b32 s53, s53, 0xffff\n"
      "s_mov_b32 s54, 75497472\n"
      "s_mov_b32 s55, 0x20000\n"
      "s_and_b32 s65, s65, 0xffff\n"
      "s_mov_b32 s66, 75497472\n"
      "s_mov_b32 s67, 0x20000\n"
      "s_and_b32 s57, s57, 0xffff\n"
      "s_mov_b32 s58, 75497472\n"
      "s_mov_b32 s59, 0x20000\n"
      "s_and_b32 s61, s61, 0xffff\n"
      "s_mov_b32 s62, 75497472\n"
      "s_mov_b32 s63, 0x20000\n"
      "s_and_b32 s81, s81, 0xffff\n"
      "s_mov_b32 s82, 75497472\n"
      "s_mov_b32 s83, 0x20000\n"
      "s_and_b32 s77, s77, 0xffff\n"
      "s_mov_b32 s78, 0\n"
      "s_mov_b32 s79, 0x20000\n"
      "s_and_b32 s85, s85, 0xffff\n"
      "s_mov_b32 s86, 75497472\n"
      "s_mov_b32 s87, 0x20000\n"
      "s_mov_b32 s68, %[hb]\n"
      "s_mov_b32 s69, %[qb0]\n"
      "s_mov_b32 s74, 0\n"
      "s_mov_b32 s75, 0\n"
      "s_mov_b64 %[flagA], 0\n"
      "s_mov_b64 %[flagB], 0\n"
      "v_mov_b32 v222, 0x3f803f80\n"
      "v_accvgpr_write_b32 a220, v222\n"
      "v_accvgpr_write_b32 a221, v222\n"
      "v_accvgpr_write_b32 a222, v222\n"
      "v_accvgpr_write_b32 a223, v222\n"
      "v_add_u32 v224, %[spq], %[qs0]\n"
      "v_add_u32 v225, %[spq], %[qs1]\n"
      "s_waitcnt vmcnt(12) lgkmcnt(0)\n"
      "s_barrier\n"
      "ds_read_b128 a[0:3], %[qs0] offset:0\n"
      "ds_read_b128 a[4:7], %[qs1] offset:0\n"
      "ds_read_b128 a[24:27], %[qs0] offset:2048\n"
      "ds_read_b128 a[28:31], %[qs1] offset:2048\n"
      "ds_read_b128 a[48:51], %[qs0] offset:4096\n"
      "ds_read_b128 a[52:55], %[qs1] offset:4096\n"
      "ds_read_b128 a[8:11], v224 offset:0\n"
      "ds_read_b128 a[12:15], v225 offset:0\n"
      "ds_read_b128 a[32:35], v224 offset:2048\n"
      "ds_read_b128 a[36:39], v225 offset:2048\n"
      "ds_read_b128 a[56:59], v224 offset:4096\n"
      "ds_read_b128 a[60:63], v225 offset:4096\n"
      "ds_read_b128 a[16:19], v224 offset:6144\n"
      "ds_read_b128 a[20:23], v225 offset:6144\n"
      "ds_read_b128 a[40:43], v224 offset:8192\n"
      "ds_read_b128 a[44:47], v225 offset:8192\n"
      "ds_read_b128 a[64:67], v224 offset:10240\n"
      "ds_read_b128 a[68:71], v225 offset:10240\n"
      "ds_read_b128 a[144:147], %[kb00] offset:0\n"
      "ds_read_b128 a[148:151], %[kb00] offset:6144\n"
      "ds_read_b128 a[152:155], %[kb10] offset:0\n"
      "ds_read_b128 a[156:159], %[kb10] offset:6144\n"
      "ds_read_b128 a[160:163], %[kb00] offset:128\n"
      "ds_read_b128 a[164:167], %[kb00] offset:6272\n"
      "ds_read_b128 a[168:171], %[kb10] offset:128\n"
      "ds_read_b128 a[172:175], %[kb10] offset:6272\n"
      "ds_read_b128 a[176:179], %[kb00] offset:256\n"
      "ds_read_b128 a[180:183], %[kb00] offset:6400\n"
      "ds_read_b128 a[184:187], %[kb10] offset:256\n"
      "ds_read_b128 a[188:191], %[kb10] offset:6400\n"
      "s_waitcnt lgkmcnt(15)\n"
      "s_add_u32 s72, s69, 18874368\n"
      "s_add_u32 m0, %[m0q], 0\n"
      "s_nop 0\n"
      "buffer_load_dwordx4 %[vq0], s[64:67], s72 offen nt lds\n"
      "s_add_u32 m0, %[m0q], 1024\n"
      "s_nop 0\n"
      "buffer_load_dwordx4 %[vq1], s[64:67], s72 offen nt lds\n"
      "s_waitcnt lgkmcnt(11)\n"
      "v_mfma_f32_16x16x32_bf16 v[144:147], a[144:147], a[0:3], 0\n"
      "v_mfma_f32_16x16x32_bf16 v[152:155], a[144:147], a[24:27], 0\n"
      "v_mfma_f32_16x16x32_bf16 v[160:163], a[144:147], a[48:51], 0\n"
      "s_waitcnt lgkmcnt(10)\n"
      "v_mfma_f32_16x16x32_bf16 v[148:151], a[148:151], a[0:3], 0\n"
      "v_mfma_f32_16x16x32_bf16 v[156:159], a[148:151], a[24:27], 0\n"
      "s_waitcnt vmcnt(8) lgkmcnt(0)\n"
      "s_barrier\n"
      "v_mfma_f32_16x16x32_bf16 v[164:167], a[148:151], a[48:51], 0\n"
      "s_add_u32 s70, s68, 36864\n"
      "ds_read_b128 a[192:195], %[kb00] offset:12288\n"
      "ds_read_b128 a[196:199], %[kb00] offset:18432\n"
      "v_mfma_f32_16x16x32_bf16 v[144:147], a[152:155], a[4:7], v[144:147]\n"
      "s_add_u32 m0, %[m0b], 36864\n"
      "ds_read_b128 a[200:203], %[kb10] offset:12288\n"
      "ds_read_b128 a[204:207], %[kb10] offset:18432\n"
      "v_mfma_f32_16x16x32_bf16 v[152:155], a[152:155], a[28:31], v[152:155]\n"
      "buffer_load_dwordx4 %[vk0], s[40:43], s70 offen nt lds\n"
      "ds_read_b128 a[144:147], %[kb00] offset:12416\n"
      "v_mfma_f32_16x16x32_bf16 v[160:163], a[152:155], a[52:55], v[160:163]\n"
      "s_add_u32 s72, s69, 18880512\n"
      "ds_read_b128 a[148:151], %[kb00] offset:18560\n"
      "ds_read_b128 a[152:155], %[kb10] offset:12416\n"
      "v_mfma_f32_16x16x32_bf16 v[148:151], a[156:159], a[4:7], v[148:151]\n"
      "s_add_u32 m0, %[m0b], 37888\n"
      "v_mfma_f32_16x16x32_bf16 v[156:159], a[156:159], a[28:31], v[156:159]\n"
      "buffer_load_dwordx4 %[vk1], s[40:43], s70 offen nt lds\n"
      "v_mfma_f32_16x16x32_bf16 v[164:167], a[156:159], a[52:55], v[164:167]\n"
      "s_add_u32 m0, %[m0q], 2048\n"
      "ds_read_b128 a[156:159], %[kb10] offset:18560\n"
      "v_mfma_f32_16x16x32_bf16 v[144:147], a[160:163], a[8:11], v[144:147]\n"
      "buffer_load_dwordx4 %[vq0], s[64:67], s72 offen nt lds\n"
      "v_mfma_f32_16x16x32_bf16 v[152:155], a[160:163], a[32:35], v[152:155]\n"
      "s_add_u32 m0, %[m0b], 38912\n"
      "v_mfma_f32_16x16x32_bf16 v[160:163], a[160:163], a[56:59], v[160:163]\n"
      "buffer_load_dwordx4 %[vk2], s[40:43], s70 offen nt lds\n"
      "ds_read_b128 a[160:163], %[kb00] offset:12544\n"
      "v_mfma_f32_16x16x32_bf16 v[148:151], a[164:167], a[8:11], v[148:151]\n"
      "s_add_u32 m0, %[m0q], 3072\n"
      "v_mfma_f32_16x16x32_bf16 v[156:159], a[164:167], a[32:35], v[156:159]\n"
      "buffer_load_dwordx4 %[vq1], s[64:67], s72 offen nt lds\n"
      "v_mfma_f32_16x16x32_bf16 v[164:167], a[164:167], a[56:59], v[164:167]\n"
      "s_add_u32 s71, s68, 24576\n"
      "ds_read_b128 a[164:167], %[kb00] offset:18688\n"
      "v_mfma_f32_16x16x32_bf16 v[144:147], a[168:171], a[12:15], v[144:147]\n"
      "s_add_u32 m0, %[m0b], 73728\n"
      "v_mfma_f32_16x16x32_bf16 v[152:155], a[168:171], a[36:39], v[152:155]\n"
      "buffer_load_dwordx4 %[vv0], s[44:47], s71 offen nt lds\n"
      "v_mfma_f32_16x16x32_bf16 v[160:163], a[168:171], a[60:63], v[160:163]\n"
      "ds_read_b128 a[168:171], %[kb10] offset:12544\n"
      "v_mfma_f32_16x16x32_bf16 v[148:151], a[172:175], a[12:15], v[148:151]\n"
      "s_add_u32 m0, %[m0b], 74752\n"
      "v_mfma_f32_16x16x32_bf16 v[156:159], a[172:175], a[36:39], v[156:159]\n"
      "buffer_load_dwordx4 %[vv1], s[44:47], s71 offen nt lds\n"
      "v_mfma_f32_16x16x32_bf16 v[164:167], a[172:175], a[60:63], v[164:167]\n"
      "ds_read_b128 a[172:175], %[kb10] offset:18688\n"
      "v_mfma_f32_16x16x32_bf16 v[144:147], a[176:179], a[16:19], v[144:147]\n"
      "v_mfma_f32_16x16x32_bf16 v[152:155], a[176:179], a[40:43], v[152:155]\n"
      "s_add_u32 m0, %[m0b], 75776\n"
      "v_mfma_f32_16x16x32_bf16 v[160:163], a[176:179], a[64:67], v[160:163]\n"
      "buffer_load_dwordx4 %[vv2], s[44:47], s71 offen nt lds\n"
      "ds_read_b128 a[176:179], %[kb00] offset:24576\n"
      "v_mfma_f32_16x16x32_bf16 v[148:151], a[180:183], a[16:19], v[148:151]\n"
      "v_mfma_f32_16x16x32_bf16 v[156:159], a[180:183], a[40:43], v[156:159]\n"
      "v_mfma_f32_16x16x32_bf16 v[164:167], a[180:183], a[64:67], v[164:167]\n"
      "s_add_u32 s71, s68, 36864\n"
      "ds_read_b128 a[180:183], %[kb00] offset:30720\n"
      "v_mfma_f32_16x16x32_bf16 v[144:147], a[184:187], a[20:23], v[144:147]\n"
      "s_add_u32 m0, %[m0b], 86016\n"
      "v_mfma_f32_16x16x32_bf16 v[152:155], a[184:187], a[44:47], v[152:155]\n"
      "buffer_load_dwordx4 %[vv0], s[44:47], s71 offen nt lds\n"
      "v_mfma_f32_16x16x32_bf16 v[160:163], a[184:187], a[68:71], v[160:163]\n"
      "ds_read_b128 a[184:187], %[kb10] offset:24576\n"
      "v_mfma_f32_16x16x32_bf16 v[148:151], a[188:191], a[20:23], v[148:151]\n"
      "s_add_u32 m0, %[m0b], 87040\n"
      "v_mfma_f32_16x16x32_bf16 v[156:159], a[188:191], a[44:47], v[156:159]\n"
      "buffer_load_dwordx4 %[vv1], s[44:47], s71 offen nt lds\n"
      "v_mfma_f32_16x16x32_bf16 v[164:167], a[188:191], a[68:71], v[164:167]\n"
      "s_waitcnt lgkmcnt(11)\n"
      "ds_read_b128 a[188:191], %[kb10] offset:30720\n"
      "v_mfma_f32_16x16x32_bf16 v[168:171], a[192:195], a[0:3], 0\n"
      "v_mfma_f32_16x16x32_bf16 v[176:179], a[192:195], a[24:27], 0\n"
      "s_add_u32 m0, %[m0b], 88064\n"
      "v_mfma_f32_16x16x32_bf16 v[184:187], a[192:195], a[48:51], 0\n"
      "buffer_load_dwordx4 %[vv2], s[44:47], s71 offen nt lds\n"
      "ds_read_b128 a[192:195], %[kb00] offset:24704\n"
      "v_mfma_f32_16x16x32_bf16 v[172:175], a[196:199], a[0:3], 0\n"
      "v_max3_f32 v222, v144, v145, v146\n"
      "v_max3_f32 v223, v147, v148, v149\n"
      "v_mfma_f32_16x16x32_bf16 v[180:183], a[196:199], a[24:27], 0\n"
      "v_max3_f32 v222, v222, v150, v151\n"
      "v_max_f32 v222, v222, v223\n"
      "v_mfma_f32_16x16x32_bf16 v[188:191], a[196:199], a[48:51], 0\n"
      "s_add_u32 s70, s68, 49152\n"
      "ds_read_b128 a[196:199], %[kb00] offset:30848\n"
      "v_mov_b32 v223, v222\n"
      "v_mfma_f32_16x16x32_bf16 v[168:171], a[200:203], a[4:7], v[168:171]\n"
      "s_add_u32 m0, %[m0b], 0\n"
      "v_permlane16_swap_b32 v222, v223\n"
      "v_max_f32 v222, v222, v223\n"
      "v_mfma_f32_16x16x32_bf16 v[176:179], a[200:203], a[28:31], v[176:179]\n"
      "buffer_load_dwordx4 %[vk0], s[40:43], s70 offen nt lds\n"
      "v_mov_b32 v223, v222\n"
      "v_mfma_f32_16x16x32_bf16 v[184:187], a[200:203], a[52:55], v[184:187]\n"
      "ds_read_b128 a[200:203], %[kb10] offset:24704\n"
      "v_permlane32_swap_b32 v222, v223\n"
      "v_mfma_f32_16x16x32_bf16 v[172:175], a[204:207], a[4:7], v[172:175]\n"
      "s_add_u32 m0, %[m0b], 1024\n"
      "v_max_f32 v222, v222, v223\n"
      "v_mul_f32_e64 v216, -%[csc], v222\n"
      "v_mfma_f32_16x16x32_bf16 v[180:183], a[204:207], a[28:31], v[180:183]\n"
      "buffer_load_dwordx4 %[vk1], s[40:43], s70 offen nt lds\n"
      "v_max3_f32 v222, v152, v153, v154\n"
      "v_mfma_f32_16x16x32_bf16 v[188:191], a[204:207], a[52:55], v[188:191]\n"
      "s_waitcnt lgkmcnt(11)\n"
      "ds_read_b128 a[204:207], %[kb10] offset:30848\n"
      "v_max3_f32 v223, v155, v156, v157\n"
      "v_mfma_f32_16x16x32_bf16 v[168:171], a[144:147], a[8:11], v[168:171]\n"
      "v_max3_f32 v222, v222, v158, v159\n"
      "v_max_f32 v222, v222, v223\n"
      "v_mfma_f32_16x16x32_bf16 v[176:179], a[144:147], a[32:35], v[176:179]\n"
      "s_add_u32 m0, %[m0b], 2048\n"
      "v_mov_b32 v223, v222\n"
      "v_mfma_f32_16x16x32_bf16 v[184:187], a[144:147], a[56:59], v[184:187]\n"
      "buffer_load_dwordx4 %[vk2], s[40:43], s70 offen nt lds\n"
      "ds_read_b128 a[144:147], %[kb00] offset:24832\n"
      "v_mfma_f32_16x16x32_bf16 v[172:175], a[148:151], a[8:11], v[172:175]\n"
      "v_permlane16_swap_b32 v222, v223\n"
      "v_max_f32 v222, v222, v223\n"
      "v_mfma_f32_16x16x32_bf16 v[180:183], a[148:151], a[32:35], v[180:183]\n"
      "v_mov_b32 v223, v222\n"
      "v_mfma_f32_16x16x32_bf16 v[188:191], a[148:151], a[56:59], v[188:191]\n"
      "ds_read_b128 a[148:151], %[kb00] offset:30976\n"
      "v_permlane32_swap_b32 v222, v223\n"
      "v_mfma_f32_16x16x32_bf16 v[168:171], a[152:155], a[12:15], v[168:171]\n"
      "v_max_f32 v222, v222, v223\n"
      "v_mul_f32_e64 v217, -%[csc], v222\n"
      "v_mfma_f32_16x16x32_bf16 v[176:179], a[152:155], a[36:39], v[176:179]\n"
      "v_max3_f32 v222, v160, v161, v162\n"
      "v_max3_f32 v223, v163, v164, v165\n"
      "v_mfma_f32_16x16x32_bf16 v[184:187], a[152:155], a[60:63], v[184:187]\n"
      "ds_read_b128 a[152:155], %[kb10] offset:24832\n"
      "v_max3_f32 v222, v222, v166, v167\n"
      "v_mfma_f32_16x16x32_bf16 v[172:175], a[156:159], a[12:15], v[172:175]\n"
      "v_max_f32 v222, v222, v223\n"
      "v_mov_b32 v223, v222\n"
      "v_mfma_f32_16x16x32_bf16 v[180:183], a[156:159], a[36:39], v[180:183]\n"
      "s_nop 0\n"
      "v_permlane16_swap_b32 v222, v223\n"
      "v_max_f32 v222, v222, v223\n"
      "v_mfma_f32_16x16x32_bf16 v[188:191], a[156:159], a[60:63], v[188:191]\n"
      "s_waitcnt lgkmcnt(11)\n"
      "ds_read_b128 a[156:159], %[kb10] offset:30976\n"
      "v_mov_b32 v223, v222\n"
      "v_mfma_f32_16x16x32_bf16 v[168:171], a[160:163], a[16:19], v[168:171]\n"
      "s_nop 0\n"
      "v_permlane32_swap_b32 v222, v223\n"
      "v_max_f32 v222, v222, v223\n"
      "v_mfma_f32_16x16x32_bf16 v[176:179], a[160:163], a[40:43], v[176:179]\n"
      "v_mul_f32_e64 v218, -%[csc], v222\n"
      "v_fma_f32 v144, v144, %[csc], v216\n"
      "v_mfma_f32_16x16x32_bf16 v[184:187], a[160:163], a[64:67], v[184:187]\n"
      "v_fma_f32 v145, v145, %[csc], v216\n"
      "v_mfma_f32_16x16x32_bf16 v[172:175], a[164:167], a[16:19], v[172:175]\n"
      "v_exp_f32 v144, v144\n"
      "v_mfma_f32_16x16x32_bf16 v[180:183], a[164:167], a[40:43], v[180:183]\n"
      "v_fma_f32 v146, v146, %[csc], v216\n"
      "s_waitcnt vmcnt(16) lgkmcnt(0)\n"
      "s_barrier\n"
      "v_mfma_f32_16x16x32_bf16 v[188:191], a[164:167], a[64:67], v[188:191]\n"
      "s_add_u32 s70, s68, 61440\n"
      "ds_read_b64_tr_b16 a[160:161], %[vb0] offset:24576\n"
      "ds_read_b64_tr_b16 a[162:163], %[vb0] offset:30720\n"
      "v_mfma_f32_16x16x32_bf16 v[168:171], a[168:171], a[20:23], v[168:171]\n"
      "s_add_u32 m0, %[m0b], 12288\n"
      "ds_read_b64_tr_b16 a[164:165], %[vb1] offset:24576\n"
      "ds_read_b64_tr_b16 a[166:167], %[vb1] offset:30720\n"
      "v_mfma_f32_16x16x32_bf16 v[176:179], a[168:171], a[44:47], v[176:179]\n"
      "buffer_load_dwordx4 %[vk0], s[40:43], s70 offen nt lds\n"
      "v_mfma_f32_16x16x32_bf16 v[184:187], a[168:171], a[68:71], v[184:187]\n"
      "ds_read_b64_tr_b16 a[168:169], %[vb2] offset:24576\n"
      "ds_read_b64_tr_b16 a[170:171], %[vb2] offset:30720\n"
      "v_mfma_f32_16x16x32_bf16 v[172:175], a[172:175], a[20:23], v[172:175]\n"
      "v_exp_f32 v145, v145\n"
      "v_mfma_f32_16x16x32_bf16 v[180:183], a[172:175], a[44:47], v[180:183]\n"
      "v_fma_f32 v147, v147, %[csc], v216\n"
      "v_mfma_f32_16x16x32_bf16 v[188:191], a[172:175], a[68:71], v[188:191]\n"
      "v_exp_f32 v146, v146\n"
      "v_mfma_f32_16x16x32_bf16 v[192:195], a[176:179], a[0:3], 0\n"
      "v_fma_f32 v148, v148, %[csc], v216\n"
      "v_mfma_f32_16x16x32_bf16 v[200:203], a[176:179], a[24:27], 0\n"
      "v_exp_f32 v147, v147\n"
      "v_mfma_f32_16x16x32_bf16 v[208:211], a[176:179], a[48:51], 0\n"
      "v_cvt_pk_bf16_f32 v144, v144, v145\n"
      "v_fma_f32 v149, v149, %[csc], v216\n"
      "v_mfma_f32_16x16x32_bf16 v[196:199], a[180:183], a[0:3], 0\n"
      "s_add_u32 m0, %[m0b], 13312\n"
      "v_exp_f32 v148, v148\n"
      "v_mfma_f32_16x16x32_bf16 v[204:207], a[180:183], a[24:27], 0\n"
      "buffer_load_dwordx4 %[vk1], s[40:43], s70 offen nt lds\n"
      "v_fma_f32 v150, v150, %[csc], v216\n"
      "v_mfma_f32_16x16x32_bf16 v[212:215], a[180:183], a[48:51], 0\n"
      "v_exp_f32 v149, v149\n"
      "v_mfma_f32_16x16x32_bf16 v[192:195], a[184:187], a[4:7], v[192:195]\n"
      "v_cvt_pk_bf16_f32 v145, v146, v147\n"
      "v_fma_f32 v151, v151, %[csc], v216\n"
      "v_mfma_f32_16x16x32_bf16 v[200:203], a[184:187], a[28:31], v[200:203]\n"
      "v_exp_f32 v150, v150\n"
      "v_mfma_f32_16x16x32_bf16 v[208:211], a[184:187], a[52:55], v[208:211]\n"
      "v_fma_f32 v152, v152, %[csc], v217\n"
      "v_mfma_f32_16x16x32_bf16 v[196:199], a[188:191], a[4:7], v[196:199]\n"
      "v_exp_f32 v151, v151\n"
      "v_mfma_f32_16x16x32_bf16 v[204:207], a[188:191], a[28:31], v[204:207]\n"
      "v_cvt_pk_bf16_f32 v146, v148, v149\n"
      "v_fma_f32 v153, v153, %[csc], v217\n"
      "v_mfma_f32_16x16x32_bf16 v[212:215], a[188:191], a[52:55], v[212:215]\n"
      "v_exp_f32 v152, v152\n"
      "v_mfma_f32_16x16x32_bf16 v[192:195], a[192:195], a[8:11], v[192:195]\n"
      "v_fma_f32 v154, v154, %[csc], v217\n"
      "v_mfma_f32_16x16x32_bf16 v[200:203], a[192:195], a[32:35], v[200:203]\n"
      "s_add_u32 m0, %[m0b], 14336\n"
      "v_exp_f32 v153, v153\n"
      "v_mfma_f32_16x16x32_bf16 v[208:211], a[192:195], a[56:59], v[208:211]\n"
      "buffer_load_dwordx4 %[vk2], s[40:43], s70 offen nt lds\n"
      "v_cvt_pk_bf16_f32 v147, v150, v151\n"
      "v_mfma_f32_16x16x32_bf16 v[196:199], a[196:199], a[8:11], v[196:199]\n"
      "v_fma_f32 v155, v155, %[csc], v217\n"
      "v_mfma_f32_16x16x32_bf16 v[204:207], a[196:199], a[32:35], v[204:207]\n"
      "v_exp_f32 v154, v154\n"
      "v_mfma_f32_16x16x32_bf16 v[212:215], a[196:199], a[56:59], v[212:215]\n"
      "v_fma_f32 v156, v156, %[csc], v217\n"
      "v_mfma_f32_16x16x32_bf16 v[192:195], a[200:203], a[12:15], v[192:195]\n"
      "v_exp_f32 v155, v155\n"
      "v_mfma_f32_16x16x32_bf16 v[200:203], a[200:203], a[36:39], v[200:203]\n"
      "v_cvt_pk_bf16_f32 v152, v152, v153\n"
      "v_fma_f32 v157, v157, %[csc], v217\n"
      "v_mfma_f32_16x16x32_bf16 v[208:211], a[200:203], a[60:63], v[208:211]\n"
      "s_add_u32 s72, s69, 18886656\n"
      "v_exp_f32 v156, v156\n"
      "v_mfma_f32_16x16x32_bf16 v[196:199], a[204:207], a[12:15], v[196:199]\n"
      "s_add_u32 m0, %[m0q], 4096\n"
      "v_fma_f32 v158, v158, %[csc], v217\n"
      "v_mfma_f32_16x16x32_bf16 v[204:207], a[204:207], a[36:39], v[204:207]\n"
      "v_exp_f32 v157, v157\n"
      "v_mfma_f32_16x16x32_bf16 v[212:215], a[204:207], a[60:63], v[212:215]\n"
      "s_add_u32 s70, s68, 73728\n"
      "v_cvt_pk_bf16_f32 v153, v154, v155\n"
      "v_fma_f32 v159, v159, %[csc], v217\n"
      "v_mfma_f32_16x16x32_bf16 v[192:195], a[144:147], a[16:19], v[192:195]\n"
      "v_exp_f32 v158, v158\n"
      "v_mfma_f32_16x16x32_bf16 v[200:203], a[144:147], a[40:43], v[200:203]\n"
      "buffer_load_dwordx4 %[vq0], s[64:67], s72 offen nt lds\n"
      "v_fma_f32 v160, v160, %[csc], v218\n"
      "v_mfma_f32_16x16x32_bf16 v[208:211], a[144:147], a[64:67], v[208:211]\n"
      "s_add_u32 m0, %[m0b], 24576\n"
      "v_exp_f32 v159, v159\n"
      "v_mfma_f32_16x16x32_bf16 v[196:199], a[148:151], a[16:19], v[196:199]\n"
      "v_cvt_pk_bf16_f32 v154, v156, v157\n"
      "v_fma_f32 v161, v161, %[csc], v218\n"
      "v_mfma_f32_16x16x32_bf16 v[204:207], a[148:151], a[40:43], v[204:207]\n"
      "v_exp_f32 v160, v160\n"
      "v_mfma_f32_16x16x32_bf16 v[212:215], a[148:151], a[64:67], v[212:215]\n"
      "buffer_load_dwordx4 %[vk0], s[40:43], s70 offen nt lds\n"
      "v_fma_f32 v162, v162, %[csc], v218\n"
      "v_mfma_f32_16x16x32_bf16 v[192:195], a[152:155], a[20:23], v[192:195]\n"
      "s_add_u32 m0, %[m0q], 5120\n"
      "v_exp_f32 v161, v161\n"
      "v_mfma_f32_16x16x32_bf16 v[200:203], a[152:155], a[44:47], v[200:203]\n"
      "v_cvt_pk_bf16_f32 v155, v158, v159\n"
      "v_fma_f32 v163, v163, %[csc], v218\n"
      "v_mfma_f32_16x16x32_bf16 v[208:211], a[152:155], a[68:71], v[208:211]\n"
      "v_exp_f32 v162, v162\n"
      "v_mfma_f32_16x16x32_bf16 v[196:199], a[156:159], a[20:23], v[196:199]\n"
      "buffer_load_dwordx4 %[vq1], s[64:67], s72 offen nt lds\n"
      "v_fma_f32 v164, v164, %[csc], v218\n"
      "v_mfma_f32_16x16x32_bf16 v[204:207], a[156:159], a[44:47], v[204:207]\n"
      "s_add_u32 m0, %[m0b], 25600\n"
      "v_exp_f32 v163, v163\n"
      "v_mfma_f32_16x16x32_bf16 v[212:215], a[156:159], a[68:71], v[212:215]\n"
      "v_cvt_pk_bf16_f32 v160, v160, v161\n"
      "v_fma_f32 v165, v165, %[csc], v218\n"
      "v_exp_f32 v164, v164\n"
      "v_fma_f32 v166, v166, %[csc], v218\n"
      "v_exp_f32 v165, v165\n"
      "v_cvt_pk_bf16_f32 v161, v162, v163\n"
      "v_fma_f32 v167, v167, %[csc], v218\n"
      "v_exp_f32 v166, v166\n"
      "v_exp_f32 v167, v167\n"
      "v_cvt_pk_bf16_f32 v162, v164, v165\n"
      "v_cvt_pk_bf16_f32 v163, v166, v167\n"
      "v_mfma_f32_16x16x32_bf16 a[208:211], a[220:223], v[144:147], 0\n"
      "ds_read_b64_tr_b16 a[172:173], %[vb3] offset:24576\n"
      "ds_read_b64_tr_b16 a[174:175], %[vb3] offset:30720\n"
      "v_mfma_f32_16x16x32_bf16 a[212:215], a[220:223], v[152:155], 0\n"
      "ds_read_b64_tr_b16 a[176:177], %[vb0] offset:24704\n"
      "ds_read_b64_tr_b16 a[178:179], %[vb0] offset:30848\n"
      "v_mfma_f32_16x16x32_bf16 a[216:219], a[220:223], v[160:163], 0\n"
      "s_waitcnt lgkmcnt(4)\n"
      "ds_read_b64_tr_b16 a[180:181], %[vb1] offset:24704\n"
      "ds_read_b64_tr_b16 a[182:183], %[vb1] offset:30848\n"
      "v_mfma_f32_16x16x32_bf16 v[0:3], a[160:163], v[144:147], 0\n"
      "ds_read_b64_tr_b16 a[184:185], %[vb2] offset:24704\n"
      "ds_read_b64_tr_b16 a[186:187], %[vb2] offset:30848\n"
      "v_mfma_f32_16x16x32_bf16 v[48:51], a[160:163], v[152:155], 0\n"
      "buffer_load_dwordx4 %[vk1], s[40:43], s70 offen nt lds\n"
      "v_fma_f32 v168, v168, %[csc], v216\n"
      "v_mfma_f32_16x16x32_bf16 v[96:99], a[160:163], v[160:163], 0\n"
      "ds_read_b64_tr_b16 a[188:189], %[vb3] offset:24704\n"
      "ds_read_b64_tr_b16 a[190:191], %[vb3] offset:30848\n"
      "v_mfma_f32_16x16x32_bf16 v[4:7], a[164:167], v[144:147], 0\n"
      "ds_read_b64_tr_b16 a[192:193], %[vb0] offset:24832\n"
      "ds_read_b64_tr_b16 a[194:195], %[vb0] offset:30976\n"
      "v_mfma_f32_16x16x32_bf16 v[52:55], a[164:167], v[152:155], 0\n"
      "s_add_u32 m0, %[m0b], 26624\n"
      "ds_read_b64_tr_b16 a[196:197], %[vb1] offset:24832\n"
      "ds_read_b64_tr_b16 a[198:199], %[vb1] offset:30976\n"
      "v_mfma_f32_16x16x32_bf16 v[100:103], a[164:167], v[160:163], 0\n"
      "ds_read_b64_tr_b16 a[200:201], %[vb2] offset:24832\n"
      "ds_read_b64_tr_b16 a[202:203], %[vb2] offset:30976\n"
      "v_mfma_f32_16x16x32_bf16 v[8:11], a[168:171], v[144:147], 0\n"
      "buffer_load_dwordx4 %[vk2], s[40:43], s70 offen nt lds\n"
      "v_fma_f32 v169, v169, %[csc], v216\n"
      "v_mfma_f32_16x16x32_bf16 v[56:59], a[168:171], v[152:155], 0\n"
      "ds_read_b64_tr_b16 a[204:205], %[vb3] offset:24832\n"
      "ds_read_b64_tr_b16 a[206:207], %[vb3] offset:30976\n"
      "v_mfma_f32_16x16x32_bf16 v[104:107], a[168:171], v[160:163], 0\n"
      "s_waitcnt lgkmcnt(12)\n"
      "ds_read_b64_tr_b16 a[144:145], %[vb0] offset:36864\n"
      "ds_read_b64_tr_b16 a[146:147], %[vb0] offset:43008\n"
      "v_mfma_f32_16x16x32_bf16 v[12:15], a[172:175], v[144:147], 0\n"
      "ds_read_b64_tr_b16 a[148:149], %[vb1] offset:36864\n"
      "ds_read_b64_tr_b16 a[150:151], %[vb1] offset:43008\n"
      "v_mfma_f32_16x16x32_bf16 v[60:63], a[172:175], v[152:155], 0\n"
      "ds_read_b64_tr_b16 a[152:153], %[vb2] offset:36864\n"
      "ds_read_b64_tr_b16 a[154:155], %[vb2] offset:43008\n"
      "v_mfma_f32_16x16x32_bf16 v[108:111], a[172:175], v[160:163], 0\n"
      "v_exp_f32 v168, v168\n"
      "v_mfma_f32_16x16x32_bf16 v[16:19], a[176:179], v[144:147], 0\n"
      "v_fma_f32 v170, v170, %[csc], v216\n"
      "v_mfma_f32_16x16x32_bf16 v[64:67], a[176:179], v[152:155], 0\n"
      "v_exp_f32 v169, v169\n"
      "v_mfma_f32_16x16x32_bf16 v[112:115], a[176:179], v[160:163], 0\n"
      "v_fma_f32 v171, v171, %[csc], v216\n"
      "v_mfma_f32_16x16x32_bf16 v[20:23], a[180:183], v[144:147], 0\n"
      "v_exp_f32 v170, v170\n"
      "v_mfma_f32_16x16x32_bf16 v[68:71], a[180:183], v[152:155], 0\n"
      "v_fma_f32 v172, v172, %[csc], v216\n"
      "v_mfma_f32_16x16x32_bf16 v[116:119], a[180:183], v[160:163], 0\n"
      "s_waitcnt lgkmcnt(10)\n"
      "v_exp_f32 v171, v171\n"
      "v_mfma_f32_16x16x32_bf16 v[24:27], a[184:187], v[144:147], 0\n"
      "v_cvt_pk_bf16_f32 v168, v168, v169\n"
      "v_fma_f32 v173, v173, %[csc], v216\n"
      "v_mfma_f32_16x16x32_bf16 v[72:75], a[184:187], v[152:155], 0\n"
      "v_exp_f32 v172, v172\n"
      "v_mfma_f32_16x16x32_bf16 v[120:123], a[184:187], v[160:163], 0\n"
      "v_fma_f32 v174, v174, %[csc], v216\n"
      "v_mfma_f32_16x16x32_bf16 v[28:31], a[188:191], v[144:147], 0\n"
      "v_exp_f32 v173, v173\n"
      "v_mfma_f32_16x16x32_bf16 v[76:79], a[188:191], v[152:155], 0\n"
      "v_cvt_pk_bf16_f32 v169, v170, v171\n"
      "v_fma_f32 v175, v175, %[csc], v216\n"
      "v_mfma_f32_16x16x32_bf16 v[124:127], a[188:191], v[160:163], 0\n"
      "v_exp_f32 v174, v174\n"
      "v_mfma_f32_16x16x32_bf16 v[32:35], a[192:195], v[144:147], 0\n"
      "v_fma_f32 v176, v176, %[csc], v217\n"
      "v_mfma_f32_16x16x32_bf16 v[80:83], a[192:195], v[152:155], 0\n"
      "v_exp_f32 v175, v175\n"
      "v_mfma_f32_16x16x32_bf16 v[128:131], a[192:195], v[160:163], 0\n"
      "v_cvt_pk_bf16_f32 v170, v172, v173\n"
      "v_fma_f32 v177, v177, %[csc], v217\n"
      "v_mfma_f32_16x16x32_bf16 v[36:39], a[196:199], v[144:147], 0\n"
      "v_exp_f32 v176, v176\n"
      "v_mfma_f32_16x16x32_bf16 v[84:87], a[196:199], v[152:155], 0\n"
      "v_fma_f32 v178, v178, %[csc], v217\n"
      "v_mfma_f32_16x16x32_bf16 v[132:135], a[196:199], v[160:163], 0\n"
      "s_waitcnt lgkmcnt(2)\n"
      "v_exp_f32 v177, v177\n"
      "v_mfma_f32_16x16x32_bf16 v[40:43], a[200:203], v[144:147], 0\n"
      "v_cvt_pk_bf16_f32 v171, v174, v175\n"
      "v_fma_f32 v179, v179, %[csc], v217\n"
      "v_mfma_f32_16x16x32_bf16 v[88:91], a[200:203], v[152:155], 0\n"
      "v_exp_f32 v178, v178\n"
      "v_mfma_f32_16x16x32_bf16 v[136:139], a[200:203], v[160:163], 0\n"
      "v_fma_f32 v180, v180, %[csc], v217\n"
      "v_mfma_f32_16x16x32_bf16 v[44:47], a[204:207], v[144:147], 0\n"
      "v_exp_f32 v179, v179\n"
      "v_mfma_f32_16x16x32_bf16 v[92:95], a[204:207], v[152:155], 0\n"
      "v_cvt_pk_bf16_f32 v176, v176, v177\n"
      "v_fma_f32 v181, v181, %[csc], v217\n"
      "v_mfma_f32_16x16x32_bf16 v[140:143], a[204:207], v[160:163], 0\n"
      "v_exp_f32 v180, v180\n"
      "v_fma_f32 v182, v182, %[csc], v217\n"
      "v_exp_f32 v181, v181\n"
      "v_cvt_pk_bf16_f32 v177, v178, v179\n"
      "v_fma_f32 v183, v183, %[csc], v217\n"
      "v_exp_f32 v182, v182\n"
      "v_fma_f32 v184, v184, %[csc], v218\n"
      "v_exp_f32 v183, v183\n"
      "v_cvt_pk_bf16_f32 v178, v180, v181\n"
      "v_fma_f32 v185, v185, %[csc], v218\n"
      "v_exp_f32 v184, v184\n"
      "v_fma_f32 v186, v186, %[csc], v218\n"
      "v_exp_f32 v185, v185\n"
      "v_cvt_pk_bf16_f32 v179, v182, v183\n"
      "v_fma_f32 v187, v187, %[csc], v218\n"
      "v_exp_f32 v186, v186\n"
      "v_fma_f32 v188, v188, %[csc], v218\n"
      "v_exp_f32 v187, v187\n"
      "v_cvt_pk_bf16_f32 v184, v184, v185\n"
      "v_fma_f32 v189, v189, %[csc], v218\n"
      "v_exp_f32 v188, v188\n"
      "v_fma_f32 v190, v190, %[csc], v218\n"
      "v_exp_f32 v189, v189\n"
      "v_cvt_pk_bf16_f32 v185, v186, v187\n"
      "v_fma_f32 v191, v191, %[csc], v218\n"
      "v_exp_f32 v190, v190\n"
      "v_exp_f32 v191, v191\n"
      "v_cvt_pk_bf16_f32 v186, v188, v189\n"
      "v_cvt_pk_bf16_f32 v187, v190, v191\n"
      "v_mfma_f32_16x16x32_bf16 a[208:211], a[220:223], v[168:171], a[208:211]\n"
      "ds_read_b64_tr_b16 a[156:157], %[vb3] offset:36864\n"
      "ds_read_b64_tr_b16 a[158:159], %[vb3] offset:43008\n"
      "v_mfma_f32_16x16x32_bf16 a[212:215], a[220:223], v[176:179], a[212:215]\n"
      "ds_read_b64_tr_b16 a[160:161], %[vb0] offset:36992\n"
      "ds_read_b64_tr_b16 a[162:163], %[vb0] offset:43136\n"
      "v_mfma_f32_16x16x32_bf16 a[216:219], a[220:223], v[184:187], a[216:219]\n"
      "ds_read_b64_tr_b16 a[164:165], %[vb1] offset:36992\n"
      "ds_read_b64_tr_b16 a[166:167], %[vb1] offset:43136\n"
      "v_mfma_f32_16x16x32_bf16 v[0:3], a[144:147], v[168:171], v[0:3]\n"
      "ds_read_b64_tr_b16 a[168:169], %[vb2] offset:36992\n"
      "ds_read_b64_tr_b16 a[170:171], %[vb2] offset:43136\n"
      "v_mfma_f32_16x16x32_bf16 v[48:51], a[144:147], v[176:179], v[48:51]\n"
      "s_waitcnt vmcnt(22)\n"
      "ds_read_b64_tr_b16 a[172:173], %[vb3] offset:36992\n"
      "ds_read_b64_tr_b16 a[174:175], %[vb3] offset:43136\n"
      "v_mfma_f32_16x16x32_bf16 v[96:99], a[144:147], v[184:187], v[96:99]\n"
      "ds_read_b64_tr_b16 a[176:177], %[vb0] offset:37120\n"
      "ds_read_b64_tr_b16 a[178:179], %[vb0] offset:43264\n"
      "v_mfma_f32_16x16x32_bf16 v[4:7], a[148:151], v[168:171], v[4:7]\n"
      "ds_read_b64_tr_b16 a[180:181], %[vb1] offset:37120\n"
      "ds_read_b64_tr_b16 a[182:183], %[vb1] offset:43264\n"
      "v_mfma_f32_16x16x32_bf16 v[52:55], a[148:151], v[176:179], v[52:55]\n"
      "ds_read_b64_tr_b16 a[184:185], %[vb2] offset:37120\n"
      "ds_read_b64_tr_b16 a[186:187], %[vb2] offset:43264\n"
      "v_mfma_f32_16x16x32_bf16 v[100:103], a[148:151], v[184:187], v[100:103]\n"
      "s_waitcnt lgkmcnt(15)\n"
      "ds_read_b64_tr_b16 a[188:189], %[vb3] offset:37120\n"
      "ds_read_b64_tr_b16 a[190:191], %[vb3] offset:43264\n"
      "v_mfma_f32_16x16x32_bf16 v[8:11], a[152:155], v[168:171], v[8:11]\n"
      "ds_read_b128 a[72:75], %[qs0] offset:0\n"
      "ds_read_b128 a[76:79], %[qs1] offset:0\n"
      "v_mfma_f32_16x16x32_bf16 v[56:59], a[152:155], v[176:179], v[56:59]\n"
      "v_fma_f32 v192, v192, %[csc], v216\n"
      "v_fma_f32 v193, v193, %[csc], v216\n"
      "v_mfma_f32_16x16x32_bf16 v[104:107], a[152:155], v[184:187], v[104:107]\n"
      "s_waitcnt lgkmcnt(14)\n"
      "v_exp_f32 v192, v192\n"
      "v_mfma_f32_16x16x32_bf16 v[12:15], a[156:159], v[168:171], v[12:15]\n"
      "v_fma_f32 v194, v194, %[csc], v216\n"
      "v_mfma_f32_16x16x32_bf16 v[60:63], a[156:159], v[176:179], v[60:63]\n"
      "v_exp_f32 v193, v193\n"
      "s_waitcnt vmcnt(14) lgkmcnt(0)\n"
      "s_barrier\n"
      "v_mfma_f32_16x16x32_bf16 v[108:111], a[156:159], v[184:187], v[108:111]\n"
      "s_add_u32 s71, s68, 49152\n"
      "ds_read_b128 a[192:195], %[kb01] offset:0\n"
      "ds_read_b128 a[196:199], %[kb01] offset:6144\n"
      "v_mfma_f32_16x16x32_bf16 v[16:19], a[160:163], v[168:171], v[16:19]\n"
      "s_add_u32 m0, %[m0b], 98304\n"
      "ds_read_b128 a[200:203], %[kb11] offset:0\n"
      "ds_read_b128 a[204:207], %[kb11] offset:6144\n"
      "v_mfma_f32_16x16x32_bf16 v[64:67], a[160:163], v[176:179], v[64:67]\n"
      "buffer_load_dwordx4 %[vv0], s[44:47], s71 offen nt lds\n"
      "ds_read_b128 a[144:147], %[kb01] offset:128\n"
      "v_mfma_f32_16x16x32_bf16 v[112:115], a[160:163], v[184:187], v[112:115]\n"
      "ds_read_b128 a[148:151], %[kb01] offset:6272\n"
      "ds_read_b128 a[152:155], %[kb11] offset:128\n"
      "v_mfma_f32_16x16x32_bf16 v[20:23], a[164:167], v[168:171], v[20:23]\n"
      "ds_read_b128 a[156:159], %[kb11] offset:6272\n"
      "ds_read_b128 a[160:163], %[kb01] offset:256\n"
      "v_mfma_f32_16x16x32_bf16 v[68:71], a[164:167], v[176:179], v[68:71]\n"
      "v_fma_f32 v195, v195, %[csc], v216\n"
      "v_mfma_f32_16x16x32_bf16 v[116:119], a[164:167], v[184:187], v[116:119]\n"
      "ds_read_b128 a[164:167], %[kb01] offset:6400\n"
      "v_mfma_f32_16x16x32_bf16 v[24:27], a[168:171], v[168:171], v[24:27]\n"
      "v_exp_f32 v194, v194\n"
      "v_mfma_f32_16x16x32_bf16 v[72:75], a[168:171], v[176:179], v[72:75]\n"
      "v_fma_f32 v196, v196, %[csc], v216\n"
      "v_mfma_f32_16x16x32_bf16 v[120:123], a[168:171], v[184:187], v[120:123]\n"
      "ds_read_b128 a[168:171], %[kb11] offset:256\n"
      "v_mfma_f32_16x16x32_bf16 v[28:31], a[172:175], v[168:171], v[28:31]\n"
      "s_add_u32 m0, %[m0b], 99328\n"
      "v_exp_f32 v195, v195\n"
      "v_mfma_f32_16x16x32_bf16 v[76:79], a[172:175], v[176:179], v[76:79]\n"
      "buffer_load_dwordx4 %[vv1], s[44:47], s71 offen nt lds\n"
      "v_cvt_pk_bf16_f32 v192, v192, v193\n"
      "v_mfma_f32_16x16x32_bf16 v[124:127], a[172:175], v[184:187], v[124:127]\n"
      "ds_read_b128 a[172:175], %[kb11] offset:6400\n"
      "v_fma_f32 v197, v197, %[csc], v216\n"
      "v_mfma_f32_16x16x32_bf16 v[32:35], a[176:179], v[168:171], v[32:35]\n"
      "v_exp_f32 v196, v196\n"
      "v_mfma_f32_16x16x32_bf16 v[80:83], a[176:179], v[176:179], v[80:83]\n"
      "v_fma_f32 v198, v198, %[csc], v216\n"
      "v_mfma_f32_16x16x32_bf16 v[128:131], a[176:179], v[184:187], v[128:131]\n"
      "ds_read_b64_tr_b16 a[176:177], %[vb0] offset:0\n"
      "ds_read_b64_tr_b16 a[178:179], %[vb0] offset:6144\n"
      "v_mfma_f32_16x16x32_bf16 v[36:39], a[180:183], v[168:171], v[36:39]\n"
      "s_add_u32 s72, s69, 18874496\n"
      "v_exp_f32 v197, v197\n"
      "v_mfma_f32_16x16x32_bf16 v[84:87], a[180:183], v[176:179], v[84:87]\n"
      "s_add_u32 m0, %[m0q], 0\n"
      "v_cvt_pk_bf16_f32 v193, v194, v195\n"
      "v_fma_f32 v199, v199, %[csc], v216\n"
      "v_mfma_f32_16x16x32_bf16 v[132:135], a[180:183], v[184:187], v[132:135]\n"
      "buffer_load_dwordx4 %[vq0], s[64:67], s72 offen nt lds\n"
      "v_mfma_f32_16x16x32_bf16 v[40:43], a[184:187], v[168:171], v[40:43]\n"
      "s_add_u32 m0, %[m0q], 1024\n"
      "ds_read_b64_tr_b16 a[180:181], %[vb1] offset:0\n"
      "ds_read_b64_tr_b16 a[182:183], %[vb1] offset:6144\n"
      "v_mfma_f32_16x16x32_bf16 v[88:91], a[184:187], v[176:179], v[88:91]\n"
      "buffer_load_dwordx4 %[vq1], s[64:67], s72 offen nt lds\n"
      "v_mfma_f32_16x16x32_bf16 v[136:139], a[184:187], v[184:187], v[136:139]\n"
      "s_add_u32 m0, %[m0b], 100352\n"
      "ds_read_b64_tr_b16 a[184:185], %[vb2] offset:0\n"
      "ds_read_b64_tr_b16 a[186:187], %[vb2] offset:6144\n"
      "v_mfma_f32_16x16x32_bf16 v[44:47], a[188:191], v[168:171], v[44:47]\n"
      "buffer_load_dwordx4 %[vv2], s[44:47], s71 offen nt lds\n"
      "v_mfma_f32_16x16x32_bf16 v[92:95], a[188:191], v[176:179], v[92:95]\n"
      "v_exp_f32 v198, v198\n"
      "v_mfma_f32_16x16x32_bf16 v[140:143], a[188:191], v[184:187], v[140:143]\n"
      "s_waitcnt lgkmcnt(14)\n"
      "v_fma_f32 v200, v200, %[csc], v217\n"
      "s_add_u32 s71, s68, 61440\n"
      "s_add_u32 m0, %[m0b], 110592\n"
      "s_nop 0\n"
      "buffer_load_dwordx4 %[vv0], s[44:47], s71 offen nt lds\n"
      "s_add_u32 m0, %[m0b], 111616\n"
      "s_nop 0\n"
      "buffer_load_dwordx4 %[vv1], s[44:47], s71 offen nt lds\n"
      "s_add_u32 m0, %[m0b], 112640\n"
      "s_nop 0\n"
      "buffer_load_dwordx4 %[vv2], s[44:47], s71 offen nt lds\n"
      "buffer_store_dword v222, %[ost], s[76:79], 0 offen\n"
      "buffer_store_dword v222, %[ost], s[76:79], 0 offen\n"
      "buffer_store_dword v222, %[ost], s[76:79], 0 offen\n"
      "s_waitcnt lgkmcnt(0)\n"
      "L_loop%=:\n"
      "s_mov_b32 s58, 75497472\n"
      "s_mov_b32 s62, 75497472\n"
      "s_mov_b32 s66, 75497472\n"
      "s_cmp_eq_u32 s74, 0\n"
      "s_cselect_b32 s82, 75497472, 0\n"
      "s_cselect_b32 s86, 0, 75497472\n"
      "v_mfma_f32_16x16x32_bf16 v[144:147], a[192:195], a[0:3], 0\n"
      "v_exp_f32 v199, v199\n"
      "v_mfma_f32_16x16x32_bf16 v[152:155], a[192:195], a[24:27], 0\n"
      "v_cvt_pk_bf16_f32 v194, v196, v197\n"
      "v_fma_f32 v201, v201, %[csc], v217\n"
      "v_mfma_f32_16x16x32_bf16 v[160:163], a[192:195], a[48:51], 0\n"
      "v_exp_f32 v200, v200\n"
      "v_mfma_f32_16x16x32_bf16 v[148:151], a[196:199], a[0:3], 0\n"
      "ds_write_b64 %[ow], a[96:97] offset:0\n"
      "v_fma_f32 v202, v202, %[csc], v217\n"
      "v_mfma_f32_16x16x32_bf16 v[156:159], a[196:199], a[24:27], 0\n"
      "v_exp_f32 v201, v201\n"
      "v_mfma_f32_16x16x32_bf16 v[164:167], a[196:199], a[48:51], 0\n"
      "v_cvt_pk_bf16_f32 v195, v198, v199\n"
      "v_fma_f32 v203, v203, %[csc], v217\n"
      "v_mfma_f32_16x16x32_bf16 v[144:147], a[200:203], a[4:7], v[144:147]\n"
      "v_exp_f32 v202, v202\n"
      "v_mfma_f32_16x16x32_bf16 v[152:155], a[200:203], a[28:31], v[152:155]\n"
      "ds_write_b64 %[ow], a[98:99] offset:32\n"
      "v_fma_f32 v204, v204, %[csc], v217\n"
      "v_mfma_f32_16x16x32_bf16 v[160:163], a[200:203], a[52:55], v[160:163]\n"
      "v_exp_f32 v203, v203\n"
      "v_mfma_f32_16x16x32_bf16 v[148:151], a[204:207], a[4:7], v[148:151]\n"
      "v_cvt_pk_bf16_f32 v200, v200, v201\n"
      "v_fma_f32 v205, v205, %[csc], v217\n"
      "v_mfma_f32_16x16x32_bf16 v[156:159], a[204:207], a[28:31], v[156:159]\n"
      "v_exp_f32 v204, v204\n"
      "v_mfma_f32_16x16x32_bf16 v[164:167], a[204:207], a[52:55], v[164:167]\n"
      "ds_write_b64 %[ow], a[100:101] offset:64\n"
      "v_fma_f32 v206, v206, %[csc], v217\n"
      "v_mfma_f32_16x16x32_bf16 v[144:147], a[144:147], a[8:11], v[144:147]\n"
      "v_exp_f32 v205, v205\n"
      "v_mfma_f32_16x16x32_bf16 v[152:155], a[144:147], a[32:35], v[152:155]\n"
      "v_cvt_pk_bf16_f32 v201, v202, v203\n"
      "v_fma_f32 v207, v207, %[csc], v217\n"
      "v_mfma_f32_16x16x32_bf16 v[160:163], a[144:147], a[56:59], v[160:163]\n"
      "v_exp_f32 v206, v206\n"
      "v_mfma_f32_16x16x32_bf16 v[148:151], a[148:151], a[8:11], v[148:151]\n"
      "ds_write_b64 %[ow], a[102:103] offset:96\n"
      "v_fma_f32 v208, v208, %[csc], v218\n"
      "v_mfma_f32_16x16x32_bf16 v[156:159], a[148:151], a[32:35], v[156:159]\n"
      "v_exp_f32 v207, v207\n"
      "v_mfma_f32_16x16x32_bf16 v[164:167], a[148:151], a[56:59], v[164:167]\n"
      "v_cvt_pk_bf16_f32 v202, v204, v205\n"
      "v_fma_f32 v209, v209, %[csc], v218\n"
      "v_mfma_f32_16x16x32_bf16 v[144:147], a[152:155], a[12:15], v[144:147]\n"
      "v_exp_f32 v208, v208\n"
      "v_mfma_f32_16x16x32_bf16 v[152:155], a[152:155], a[36:39], v[152:155]\n"
      "ds_read_b128 a[232:235], %[orb] offset:0\n"
      "v_fma_f32 v210, v210, %[csc], v218\n"
      "v_mfma_f32_16x16x32_bf16 v[160:163], a[152:155], a[60:63], v[160:163]\n"
      "v_exp_f32 v209, v209\n"
      "v_mfma_f32_16x16x32_bf16 v[148:151], a[156:159], a[12:15], v[148:151]\n"
      "v_cvt_pk_bf16_f32 v203, v206, v207\n"
      "v_fma_f32 v211, v211, %[csc], v218\n"
      "v_mfma_f32_16x16x32_bf16 v[156:159], a[156:159], a[36:39], v[156:159]\n"
      "v_exp_f32 v210, v210\n"
      "v_mfma_f32_16x16x32_bf16 v[164:167], a[156:159], a[60:63], v[164:167]\n"
      "ds_read_b128 a[236:239], %[orb] offset:1152\n"
      "v_fma_f32 v212, v212, %[csc], v218\n"
      "v_mfma_f32_16x16x32_bf16 v[144:147], a[160:163], a[16:19], v[144:147]\n"
      "v_exp_f32 v211, v211\n"
      "v_mfma_f32_16x16x32_bf16 v[152:155], a[160:163], a[40:43], v[152:155]\n"
      "v_cvt_pk_bf16_f32 v208, v208, v209\n"
      "v_fma_f32 v213, v213, %[csc], v218\n"
      "v_mfma_f32_16x16x32_bf16 v[160:163], a[160:163], a[64:67], v[160:163]\n"
      "s_waitcnt lgkmcnt(1)\n"
      "v_exp_f32 v212, v212\n"
      "v_mfma_f32_16x16x32_bf16 v[148:151], a[164:167], a[16:19], v[148:151]\n"
      "s_add_u32 s73, s69, 4276099072\n"
      "buffer_store_dwordx4 a[232:235], %[ost], s[84:87], s73 offen offset:0 sc1\n"
      "v_fma_f32 v214, v214, %[csc], v218\n"
      "v_mfma_f32_16x16x32_bf16 v[156:159], a[164:167], a[40:43], v[156:159]\n"
      "v_exp_f32 v213, v213\n"
      "v_mfma_f32_16x16x32_bf16 v[164:167], a[164:167], a[64:67], v[164:167]\n"
      "v_cvt_pk_bf16_f32 v209, v210, v211\n"
      "v_fma_f32 v215, v215, %[csc], v218\n"
      "v_mfma_f32_16x16x32_bf16 v[144:147], a[168:171], a[20:23], v[144:147]\n"
      "v_exp_f32 v214, v214\n"
      "v_mfma_f32_16x16x32_bf16 v[152:155], a[168:171], a[44:47], v[152:155]\n"
      "v_exp_f32 v215, v215\n"
      "v_mfma_f32_16x16x32_bf16 v[160:163], a[168:171], a[68:71], v[160:163]\n"
      "v_cvt_pk_bf16_f32 v210, v212, v213\n"
      "v_mfma_f32_16x16x32_bf16 v[148:151], a[172:175], a[20:23], v[148:151]\n"
      "v_cvt_pk_bf16_f32 v211, v214, v215\n"
      "v_mfma_f32_16x16x32_bf16 v[156:159], a[172:175], a[44:47], v[156:159]\n"
      "ds_read_b64_tr_b16 a[188:189], %[vb3] offset:0\n"
      "ds_read_b64_tr_b16 a[190:191], %[vb3] offset:6144\n"
      "v_mfma_f32_16x16x32_bf16 v[164:167], a[172:175], a[68:71], v[164:167]\n"
      "ds_read_b64_tr_b16 a[192:193], %[vb0] offset:128\n"
      "ds_read_b64_tr_b16 a[194:195], %[vb0] offset:6272\n"
      "v_mfma_f32_16x16x32_bf16 a[208:211], a[220:223], v[192:195], a[208:211]\n"
      "ds_read_b64_tr_b16 a[196:197], %[vb1] offset:128\n"
      "ds_read_b64_tr_b16 a[198:199], %[vb1] offset:6272\n"
      "v_mfma_f32_16x16x32_bf16 a[212:215], a[220:223], v[200:203], a[212:215]\n"
      "ds_read_b64_tr_b16 a[200:201], %[vb2] offset:128\n"
      "ds_read_b64_tr_b16 a[202:203], %[vb2] offset:6272\n"
      "v_mfma_f32_16x16x32_bf16 a[216:219], a[220:223], v[208:211], a[216:219]\n"
      "s_waitcnt lgkmcnt(8)\n"
      "ds_read_b64_tr_b16 a[204:205], %[vb3] offset:128\n"
      "ds_read_b64_tr_b16 a[206:207], %[vb3] offset:6272\n"
      "v_mfma_f32_16x16x32_bf16 v[0:3], a[176:179], v[192:195], v[0:3]\n"
      "s_add_u32 s73, s69, 4276102144\n"
      "ds_read_b64_tr_b16 a[144:145], %[vb0] offset:256\n"
      "ds_read_b64_tr_b16 a[146:147], %[vb0] offset:6400\n"
      "v_mfma_f32_16x16x32_bf16 v[48:51], a[176:179], v[200:203], v[48:51]\n"
      "ds_read_b64_tr_b16 a[148:149], %[vb1] offset:256\n"
      "ds_read_b64_tr_b16 a[150:151], %[vb1] offset:6400\n"
      "v_mfma_f32_16x16x32_bf16 v[96:99], a[176:179], v[208:211], v[96:99]\n"
      "ds_read_b64_tr_b16 a[152:153], %[vb2] offset:256\n"
      "ds_read_b64_tr_b16 a[154:155], %[vb2] offset:6400\n"
      "v_mfma_f32_16x16x32_bf16 v[4:7], a[180:183], v[192:195], v[4:7]\n"
      "buffer_store_dwordx4 a[236:239], %[ost], s[84:87], s73 offen offset:0 sc1\n"
      "v_fma_f32 v144, v144, %[csc], v216\n"
      "v_mfma_f32_16x16x32_bf16 v[52:55], a[180:183], v[200:203], v[52:55]\n"
      "ds_read_b64_tr_b16 a[156:157], %[vb3] offset:256\n"
      "ds_read_b64_tr_b16 a[158:159], %[vb3] offset:6400\n"
      "v_mfma_f32_16x16x32_bf16 v[100:103], a[180:183], v[208:211], v[100:103]\n"
      "v_fma_f32 v145, v145, %[csc], v216\n"
      "v_mfma_f32_16x16x32_bf16 v[8:11], a[184:187], v[192:195], v[8:11]\n"
      "v_exp_f32 v144, v144\n"
      "v_mfma_f32_16x16x32_bf16 v[56:59], a[184:187], v[200:203], v[56:59]\n"
      "v_fma_f32 v146, v146, %[csc], v216\n"
      "v_mfma_f32_16x16x32_bf16 v[104:107], a[184:187], v[208:211], v[104:107]\n"
      "s_waitcnt lgkmcnt(10)\n"
      "v_exp_f32 v145, v145\n"
      "v_mfma_f32_16x16x32_bf16 v[12:15], a[188:191], v[192:195], v[12:15]\n"
      "v_fma_f32 v147, v147, %[csc], v216\n"
      "s_waitcnt vmcnt(20) lgkmcnt(0)\n"
      "s_barrier\n"
      "v_mfma_f32_16x16x32_bf16 v[60:63], a[188:191], v[200:203], v[60:63]\n"
      "s_add_u32 s70, s68, 86016\n"
      "ds_read_b128 a[160:163], %[kb00] offset:0\n"
      "ds_read_b128 a[164:167], %[kb00] offset:6144\n"
      "v_mfma_f32_16x16x32_bf16 v[108:111], a[188:191], v[208:211], v[108:111]\n"
      "s_add_u32 m0, %[m0b], 36864\n"
      "ds_read_b128 a[168:171], %[kb10] offset:0\n"
      "ds_read_b128 a[172:175], %[kb10] offset:6144\n"
      "v_mfma_f32_16x16x32_bf16 v[16:19], a[192:195], v[192:195], v[16:19]\n"
      "s_waitcnt vmcnt(25)\n"
      "buffer_load_dwordx4 %[vk0], s[40:43], s70 offen nt lds\n"
      "ds_read_b128 a[96:99], %[qs0] offset:2048\n"
      "v_mfma_f32_16x16x32_bf16 v[64:67], a[192:195], v[200:203], v[64:67]\n"
      "ds_read_b128 a[100:103], %[qs1] offset:2048\n"
      "ds_read_b128 a[176:179], %[kb00] offset:128\n"
      "v_mfma_f32_16x16x32_bf16 v[112:115], a[192:195], v[208:211], v[112:115]\n"
      "ds_read_b128 a[180:183], %[kb00] offset:6272\n"
      "ds_read_b128 a[184:187], %[kb10] offset:128\n"
      "v_mfma_f32_16x16x32_bf16 v[20:23], a[196:199], v[192:195], v[20:23]\n"
      "ds_read_b128 a[188:191], %[kb10] offset:6272\n"
      "ds_read_b128 a[192:195], %[kb00] offset:256\n"
      "v_mfma_f32_16x16x32_bf16 v[68:71], a[196:199], v[200:203], v[68:71]\n"
      "v_exp_f32 v146, v146\n"
      "v_mfma_f32_16x16x32_bf16 v[116:119], a[196:199], v[208:211], v[116:119]\n"
      "ds_read_b128 a[196:199], %[kb00] offset:6400\n"
      "v_fma_f32 v148, v148, %[csc], v216\n"
      "v_mfma_f32_16x16x32_bf16 v[24:27], a[200:203], v[192:195], v[24:27]\n"
      "v_exp_f32 v147, v147\n"
      "v_mfma_f32_16x16x32_bf16 v[72:75], a[200:203], v[200:203], v[72:75]\n"
      "v_cvt_pk_bf16_f32 v144, v144, v145\n"
      "v_fma_f32 v149, v149, %[csc], v216\n"
      "v_mfma_f32_16x16x32_bf16 v[120:123], a[200:203], v[208:211], v[120:123]\n"
      "s_add_u32 m0, %[m0b], 37888\n"
      "ds_read_b128 a[200:203], %[kb10] offset:256\n"
      "v_mfma_f32_16x16x32_bf16 v[28:31], a[204:207], v[192:195], v[28:31]\n"
      "buffer_load_dwordx4 %[vk1], s[40:43], s70 offen nt lds\n"
      "v_mfma_f32_16x16x32_bf16 v[76:79], a[204:207], v[200:203], v[76:79]\n"
      "v_exp_f32 v148, v148\n"
      "v_mfma_f32_16x16x32_bf16 v[124:127], a[204:207], v[208:211], v[124:127]\n"
      "ds_read_b128 a[204:207], %[kb10] offset:6400\n"
      "v_fma_f32 v150, v150, %[csc], v216\n"
      "v_mfma_f32_16x16x32_bf16 v[32:35], a[144:147], v[192:195], v[32:35]\n"
      "v_exp_f32 v149, v149\n"
      "v_mfma_f32_16x16x32_bf16 v[80:83], a[144:147], v[200:203], v[80:83]\n"
      "v_cvt_pk_bf16_f32 v145, v146, v147\n"
      "v_fma_f32 v151, v151, %[csc], v216\n"
      "v_mfma_f32_16x16x32_bf16 v[128:131], a[144:147], v[208:211], v[128:131]\n"
      "ds_read_b64_tr_b16 a[144:145], %[vb0] offset:12288\n"
      "ds_read_b64_tr_b16 a[146:147], %[vb0] offset:18432\n"
      "v_mfma_f32_16x16x32_bf16 v[36:39], a[148:151], v[192:195], v[36:39]\n"
      "v_exp_f32 v150, v150\n"
      "v_mfma_f32_16x16x32_bf16 v[84:87], a[148:151], v[200:203], v[84:87]\n"
      "v_fma_f32 v152, v152, %[csc], v217\n"
      "v_mfma_f32_16x16x32_bf16 v[132:135], a[148:151], v[208:211], v[132:135]\n"
      "ds_read_b64_tr_b16 a[148:149], %[vb1] offset:12288\n"
      "ds_read_b64_tr_b16 a[150:151], %[vb1] offset:18432\n"
      "v_mfma_f32_16x16x32_bf16 v[40:43], a[152:155], v[192:195], v[40:43]\n"
      "s_add_u32 m0, %[m0b], 38912\n"
      "v_exp_f32 v151, v151\n"
      "v_mfma_f32_16x16x32_bf16 v[88:91], a[152:155], v[200:203], v[88:91]\n"
      "buffer_load_dwordx4 %[vk2], s[40:43], s70 offen nt lds\n"
      "v_cvt_pk_bf16_f32 v146, v148, v149\n"
      "v_mfma_f32_16x16x32_bf16 v[136:139], a[152:155], v[208:211], v[136:139]\n"
      "ds_read_b64_tr_b16 a[152:153], %[vb2] offset:12288\n"
      "ds_read_b64_tr_b16 a[154:155], %[vb2] offset:18432\n"
      "v_mfma_f32_16x16x32_bf16 v[44:47], a[156:159], v[192:195], v[44:47]\n"
      "v_fma_f32 v153, v153, %[csc], v217\n"
      "v_mfma_f32_16x16x32_bf16 v[92:95], a[156:159], v[200:203], v[92:95]\n"
      "v_exp_f32 v152, v152\n"
      "v_mfma_f32_16x16x32_bf16 v[140:143], a[156:159], v[208:211], v[140:143]\n"
      "s_waitcnt lgkmcnt(15)\n"
      "v_fma_f32 v154, v154, %[csc], v217\n"
      "v_mfma_f32_16x16x32_bf16 v[168:171], a[160:163], a[0:3], 0\n"
      "s_waitcnt lgkmcnt(14)\n"
      "v_exp_f32 v153, v153\n"
      "v_mfma_f32_16x16x32_bf16 v[176:179], a[160:163], a[24:27], 0\n"
      "s_add_u32 s72, s69, 18880640\n"
      "v_cvt_pk_bf16_f32 v147, v150, v151\n"
      "v_fma_f32 v155, v155, %[csc], v217\n"
      "v_mfma_f32_16x16x32_bf16 v[184:187], a[160:163], a[48:51], 0\n"
      "s_add_u32 m0, %[m0q], 2048\n"
      "v_exp_f32 v154, v154\n"
      "v_mfma_f32_16x16x32_bf16 v[172:175], a[164:167], a[0:3], 0\n"
      "buffer_load_dwordx4 %[vq0], s[64:67], s72 offen nt lds\n"
      "v_fma_f32 v156, v156, %[csc], v217\n"
      "v_mfma_f32_16x16x32_bf16 v[180:183], a[164:167], a[24:27], 0\n"
      "s_add_u32 s71, s68, 73728\n"
      "v_exp_f32 v155, v155\n"
      "v_mfma_f32_16x16x32_bf16 v[188:191], a[164:167], a[48:51], 0\n"
      "s_add_u32 m0, %[m0b], 73728\n"
      "v_cvt_pk_bf16_f32 v152, v152, v153\n"
      "v_fma_f32 v157, v157, %[csc], v217\n"
      "v_mfma_f32_16x16x32_bf16 v[168:171], a[168:171], a[4:7], v[168:171]\n"
      "buffer_load_dwordx4 %[vv0], s[44:47], s71 offen nt lds\n"
      "v_mfma_f32_16x16x32_bf16 v[176:179], a[168:171], a[28:31], v[176:179]\n"
      "s_add_u32 m0, %[m0q], 3072\n"
      "v_exp_f32 v156, v156\n"
      "v_mfma_f32_16x16x32_bf16 v[184:187], a[168:171], a[52:55], v[184:187]\n"
      "buffer_load_dwordx4 %[vq1], s[64:67], s72 offen nt lds\n"
      "v_fma_f32 v158, v158, %[csc], v217\n"
      "v_mfma_f32_16x16x32_bf16 v[172:175], a[172:175], a[4:7], v[172:175]\n"
      "v_exp_f32 v157, v157\n"
      "v_mfma_f32_16x16x32_bf16 v[180:183], a[172:175], a[28:31], v[180:183]\n"
      "v_cvt_pk_bf16_f32 v153, v154, v155\n"
      "v_fma_f32 v159, v159, %[csc], v217\n"
      "v_mfma_f32_16x16x32_bf16 v[188:191], a[172:175], a[52:55], v[188:191]\n"
      "s_waitcnt lgkmcnt(10)\n"
      "v_exp_f32 v158, v158\n"
      "v_mfma_f32_16x16x32_bf16 v[168:171], a[176:179], a[8:11], v[168:171]\n"
      "ds_write_b64 %[ow], a[120:121] offset:0\n"
      "v_fma_f32 v160, v160, %[csc], v218\n"
      "v_mfma_f32_16x16x32_bf16 v[176:179], a[176:179], a[32:35], v[176:179]\n"
      "v_exp_f32 v159, v159\n"
      "v_mfma_f32_16x16x32_bf16 v[184:187], a[176:179], a[56:59], v[184:187]\n"
      "s_add_u32 m0, %[m0b], 74752\n"
      "v_cvt_pk_bf16_f32 v154, v156, v157\n"
      "v_fma_f32 v161, v161, %[csc], v218\n"
      "v_mfma_f32_16x16x32_bf16 v[172:175], a[180:183], a[8:11], v[172:175]\n"
      "buffer_load_dwordx4 %[vv1], s[44:47], s71 offen nt lds\n"
      "ds_write_b64 %[ow], a[122:123] offset:32\n"
      "v_mfma_f32_16x16x32_bf16 v[180:183], a[180:183], a[32:35], v[180:183]\n"
      "v_exp_f32 v160, v160\n"
      "v_mfma_f32_16x16x32_bf16 v[188:191], a[180:183], a[56:59], v[188:191]\n"
      "ds_write_b64 %[ow], a[124:125] offset:64\n"
      "v_fma_f32 v162, v162, %[csc], v218\n"
      "v_mfma_f32_16x16x32_bf16 v[168:171], a[184:187], a[12:15], v[168:171]\n"
      "v_exp_f32 v161, v161\n"
      "v_mfma_f32_16x16x32_bf16 v[176:179], a[184:187], a[36:39], v[176:179]\n"
      "v_cvt_pk_bf16_f32 v155, v158, v159\n"
      "v_fma_f32 v163, v163, %[csc], v218\n"
      "v_mfma_f32_16x16x32_bf16 v[184:187], a[184:187], a[60:63], v[184:187]\n"
      "v_exp_f32 v162, v162\n"
      "v_mfma_f32_16x16x32_bf16 v[172:175], a[188:191], a[12:15], v[172:175]\n"
      "ds_write_b64 %[ow], a[126:127] offset:96\n"
      "v_fma_f32 v164, v164, %[csc], v218\n"
      "v_mfma_f32_16x16x32_bf16 v[180:183], a[188:191], a[36:39], v[180:183]\n"
      "v_exp_f32 v163, v163\n"
      "v_mfma_f32_16x16x32_bf16 v[188:191], a[188:191], a[60:63], v[188:191]\n"
      "s_waitcnt lgkmcnt(10)\n"
      "v_cvt_pk_bf16_f32 v160, v160, v161\n"
      "v_fma_f32 v165, v165, %[csc], v218\n"
      "v_mfma_f32_16x16x32_bf16 v[168:171], a[192:195], a[16:19], v[168:171]\n"
      "s_add_u32 m0, %[m0b], 75776\n"
      "v_exp_f32 v164, v164\n"
      "v_mfma_f32_16x16x32_bf16 v[176:179], a[192:195], a[40:43], v[176:179]\n"
      "buffer_load_dwordx4 %[vv2], s[44:47], s71 offen nt lds\n"
      "v_fma_f32 v166, v166, %[csc], v218\n"
      "v_mfma_f32_16x16x32_bf16 v[184:187], a[192:195], a[64:67], v[184:187]\n"
      "v_exp_f32 v165, v165\n"
      "v_mfma_f32_16x16x32_bf16 v[172:175], a[196:199], a[16:19], v[172:175]\n"
      "v_cvt_pk_bf16_f32 v161, v162, v163\n"
      "v_fma_f32 v167, v167, %[csc], v218\n"
      "v_mfma_f32_16x16x32_bf16 v[180:183], a[196:199], a[40:43], v[180:183]\n"
      "v_exp_f32 v166, v166\n"
      "v_mfma_f32_16x16x32_bf16 v[188:191], a[196:199], a[64:67], v[188:191]\n"
      "v_exp_f32 v167, v167\n"
      "v_mfma_f32_16x16x32_bf16 v[168:171], a[200:203], a[20:23], v[168:171]\n"
      "ds_read_b128 a[224:227], %[orb] offset:0\n"
      "v_cvt_pk_bf16_f32 v162, v164, v165\n"
      "v_mfma_f32_16x16x32_bf16 v[176:179], a[200:203], a[44:47], v[176:179]\n"
      "ds_read_b128 a[228:231], %[orb] offset:1152\n"
      "v_cvt_pk_bf16_f32 v163, v166, v167\n"
      "v_mfma_f32_16x16x32_bf16 v[184:187], a[200:203], a[68:71], v[184:187]\n"
      "ds_read_b64_tr_b16 a[156:157], %[vb3] offset:12288\n"
      "ds_read_b64_tr_b16 a[158:159], %[vb3] offset:18432\n"
      "v_mfma_f32_16x16x32_bf16 v[172:175], a[204:207], a[20:23], v[172:175]\n"
      "ds_read_b64_tr_b16 a[160:161], %[vb0] offset:12416\n"
      "ds_read_b64_tr_b16 a[162:163], %[vb0] offset:18560\n"
      "v_mfma_f32_16x16x32_bf16 v[180:183], a[204:207], a[44:47], v[180:183]\n"
      "ds_read_b64_tr_b16 a[164:165], %[vb1] offset:12416\n"
      "ds_read_b64_tr_b16 a[166:167], %[vb1] offset:18560\n"
      "v_mfma_f32_16x16x32_bf16 v[188:191], a[204:207], a[68:71], v[188:191]\n"
      "ds_read_b64_tr_b16 a[168:169], %[vb2] offset:12416\n"
      "ds_read_b64_tr_b16 a[170:171], %[vb2] offset:18560\n"
      "v_mfma_f32_16x16x32_bf16 a[208:211], a[220:223], v[144:147], a[208:211]\n"
      "s_waitcnt lgkmcnt(9)\n"
      "ds_read_b64_tr_b16 a[172:173], %[vb3] offset:12416\n"
      "ds_read_b64_tr_b16 a[174:175], %[vb3] offset:18560\n"
      "v_mfma_f32_16x16x32_bf16 a[212:215], a[220:223], v[152:155], a[212:215]\n"
      "s_add_u32 s73, s69, 4276105216\n"
      "ds_read_b64_tr_b16 a[176:177], %[vb0] offset:12544\n"
      "ds_read_b64_tr_b16 a[178:179], %[vb0] offset:18688\n"
      "v_mfma_f32_16x16x32_bf16 a[216:219], a[220:223], v[160:163], a[216:219]\n"
      "ds_read_b64_tr_b16 a[180:181], %[vb1] offset:12544\n"
      "ds_read_b64_tr_b16 a[182:183], %[vb1] offset:18688\n"
      "v_mfma_f32_16x16x32_bf16 v[0:3], a[144:147], v[144:147], v[0:3]\n"
      "buffer_store_dwordx4 a[224:227], %[ost], s[84:87], s73 offen offset:0 sc1\n"
      "v_fma_f32 v168, v168, %[csc], v216\n"
      "v_mfma_f32_16x16x32_bf16 v[48:51], a[144:147], v[152:155], v[48:51]\n"
      "ds_read_b64_tr_b16 a[184:185], %[vb2] offset:12544\n"
      "ds_read_b64_tr_b16 a[186:187], %[vb2] offset:18688\n"
      "v_mfma_f32_16x16x32_bf16 v[96:99], a[144:147], v[160:163], v[96:99]\n"
      "ds_read_b64_tr_b16 a[188:189], %[vb3] offset:12544\n"
      "ds_read_b64_tr_b16 a[190:191], %[vb3] offset:18688\n"
      "v_mfma_f32_16x16x32_bf16 v[4:7], a[148:151], v[144:147], v[4:7]\n"
      "v_fma_f32 v169, v169, %[csc], v216\n"
      "v_mfma_f32_16x16x32_bf16 v[52:55], a[148:151], v[152:155], v[52:55]\n"
      "v_exp_f32 v168, v168\n"
      "v_mfma_f32_16x16x32_bf16 v[100:103], a[148:151], v[160:163], v[100:103]\n"
      "v_fma_f32 v170, v170, %[csc], v216\n"
      "v_mfma_f32_16x16x32_bf16 v[8:11], a[152:155], v[144:147], v[8:11]\n"
      "v_exp_f32 v169, v169\n"
      "v_mfma_f32_16x16x32_bf16 v[56:59], a[152:155], v[152:155], v[56:59]\n"
      "v_fma_f32 v171, v171, %[csc], v216\n"
      "s_waitcnt vmcnt(14) lgkmcnt(0)\n"
      "s_barrier\n"
      "v_mfma_f32_16x16x32_bf16 v[104:107], a[152:155], v[160:163], v[104:107]\n"
      "s_add_u32 s70, s68, 98304\n"
      "ds_read_b128 a[192:195], %[kb00] offset:12288\n"
      "ds_read_b128 a[196:199], %[kb00] offset:18432\n"
      "v_mfma_f32_16x16x32_bf16 v[12:15], a[156:159], v[144:147], v[12:15]\n"
      "s_add_u32 m0, %[m0b], 0\n"
      "ds_read_b128 a[200:203], %[kb10] offset:12288\n"
      "ds_read_b128 a[204:207], %[kb10] offset:18432\n"
      "v_mfma_f32_16x16x32_bf16 v[60:63], a[156:159], v[152:155], v[60:63]\n"
      "buffer_load_dwordx4 %[vk0], s[40:43], s70 offen nt lds\n"
      "ds_read_b128 a[144:147], %[kb00] offset:12416\n"
      "v_mfma_f32_16x16x32_bf16 v[108:111], a[156:159], v[160:163], v[108:111]\n"
      "s_add_u32 s73, s69, 4276108288\n"
      "buffer_store_dwordx4 a[228:231], %[ost], s[84:87], s73 offen offset:0 sc1\n"
      "ds_read_b128 a[148:151], %[kb00] offset:18560\n"
      "v_mfma_f32_16x16x32_bf16 v[16:19], a[160:163], v[144:147], v[16:19]\n"
      "ds_read_b128 a[152:155], %[kb10] offset:12416\n"
      "ds_read_b128 a[156:159], %[kb10] offset:18560\n"
      "v_mfma_f32_16x16x32_bf16 v[64:67], a[160:163], v[152:155], v[64:67]\n"
      "v_exp_f32 v170, v170\n"
      "v_mfma_f32_16x16x32_bf16 v[112:115], a[160:163], v[160:163], v[112:115]\n"
      "ds_read_b128 a[160:163], %[kb00] offset:12544\n"
      "v_fma_f32 v172, v172, %[csc], v216\n"
      "v_mfma_f32_16x16x32_bf16 v[20:23], a[164:167], v[144:147], v[20:23]\n"
      "v_exp_f32 v171, v171\n"
      "v_mfma_f32_16x16x32_bf16 v[68:71], a[164:167], v[152:155], v[68:71]\n"
      "v_cvt_pk_bf16_f32 v168, v168, v169\n"
      "v_fma_f32 v173, v173, %[csc], v216\n"
      "v_mfma_f32_16x16x32_bf16 v[116:119], a[164:167], v[160:163], v[116:119]\n"
      "ds_read_b128 a[164:167], %[kb00] offset:18688\n"
      "v_mfma_f32_16x16x32_bf16 v[24:27], a[168:171], v[144:147], v[24:27]\n"
      "s_add_u32 m0, %[m0b], 1024\n"
      "v_exp_f32 v172, v172\n"
      "v_mfma_f32_16x16x32_bf16 v[72:75], a[168:171], v[152:155], v[72:75]\n"
      "buffer_load_dwordx4 %[vk1], s[40:43], s70 offen nt lds\n"
      "v_fma_f32 v174, v174, %[csc], v216\n"
      "v_mfma_f32_16x16x32_bf16 v[120:123], a[168:171], v[160:163], v[120:123]\n"
      "ds_read_b128 a[168:171], %[kb10] offset:12544\n"
      "v_mfma_f32_16x16x32_bf16 v[28:31], a[172:175], v[144:147], v[28:31]\n"
      "s_waitcnt vmcnt(27)\n"
      "ds_read_b128 a[120:123], %[qs0] offset:4096\n"
      "ds_read_b128 a[124:127], %[qs1] offset:4096\n"
      "v_mfma_f32_16x16x32_bf16 v[76:79], a[172:175], v[152:155], v[76:79]\n"
      "v_exp_f32 v173, v173\n"
      "v_mfma_f32_16x16x32_bf16 v[124:127], a[172:175], v[160:163], v[124:127]\n"
      "ds_read_b128 a[172:175], %[kb10] offset:18688\n"
      "v_cvt_pk_bf16_f32 v169, v170, v171\n"
      "v_mfma_f32_16x16x32_bf16 v[32:35], a[176:179], v[144:147], v[32:35]\n"
      "v_fma_f32 v175, v175, %[csc], v216\n"
      "v_mfma_f32_16x16x32_bf16 v[80:83], a[176:179], v[152:155], v[80:83]\n"
      "v_exp_f32 v174, v174\n"
      "v_mfma_f32_16x16x32_bf16 v[128:131], a[176:179], v[160:163], v[128:131]\n"
      "ds_read_b64_tr_b16 a[176:177], %[vb0] offset:24576\n"
      "ds_read_b64_tr_b16 a[178:179], %[vb0] offset:30720\n"
      "v_mfma_f32_16x16x32_bf16 v[36:39], a[180:183], v[144:147], v[36:39]\n"
      "v_fma_f32 v176, v176, %[csc], v217\n"
      "v_mfma_f32_16x16x32_bf16 v[84:87], a[180:183], v[152:155], v[84:87]\n"
      "s_add_u32 m0, %[m0b], 2048\n"
      "v_exp_f32 v175, v175\n"
      "v_mfma_f32_16x16x32_bf16 v[132:135], a[180:183], v[160:163], v[132:135]\n"
      "buffer_load_dwordx4 %[vk2], s[40:43], s70 offen nt lds\n"
      "v_cvt_pk_bf16_f32 v170, v172, v173\n"
      "v_mfma_f32_16x16x32_bf16 v[40:43], a[184:187], v[144:147], v[40:43]\n"
      "ds_read_b64_tr_b16 a[180:181], %[vb1] offset:24576\n"
      "ds_read_b64_tr_b16 a[182:183], %[vb1] offset:30720\n"
      "v_mfma_f32_16x16x32_bf16 v[88:91], a[184:187], v[152:155], v[88:91]\n"
      "v_fma_f32 v177, v177, %[csc], v217\n"
      "v_mfma_f32_16x16x32_bf16 v[136:139], a[184:187], v[160:163], v[136:139]\n"
      "ds_read_b64_tr_b16 a[184:185], %[vb2] offset:24576\n"
      "ds_read_b64_tr_b16 a[186:187], %[vb2] offset:30720\n"
      "v_mfma_f32_16x16x32_bf16 v[44:47], a[188:191], v[144:147], v[44:47]\n"
      "v_exp_f32 v176, v176\n"
      "v_mfma_f32_16x16x32_bf16 v[92:95], a[188:191], v[152:155], v[92:95]\n"
      "v_fma_f32 v178, v178, %[csc], v217\n"
      "v_mfma_f32_16x16x32_bf16 v[140:143], a[188:191], v[160:163], v[140:143]\n"
      "s_waitcnt lgkmcnt(15)\n"
      "v_exp_f32 v177, v177\n"
      "v_mfma_f32_16x16x32_bf16 v[144:147], a[192:195], a[0:3], 0\n"
      "v_cvt_pk_bf16_f32 v171, v174, v175\n"
      "v_fma_f32 v179, v179, %[csc], v217\n"
      "v_mfma_f32_16x16x32_bf16 v[152:155], a[192:195], a[24:27], 0\n"
      "v_exp_f32 v178, v178\n"
      "v_mfma_f32_16x16x32_bf16 v[160:163], a[192:195], a[48:51], 0\n"
      "s_add_u32 s71, s68, 86016\n"
      "v_fma_f32 v180, v180, %[csc], v217\n"
      "v_mfma_f32_16x16x32_bf16 v[148:151], a[196:199], a[0:3], 0\n"
      "s_add_u32 m0, %[m0b], 86016\n"
      "v_exp_f32 v179, v179\n"
      "v_mfma_f32_16x16x32_bf16 v[156:159], a[196:199], a[24:27], 0\n"
      "buffer_load_dwordx4 %[vv0], s[44:47], s71 offen nt lds\n"
      "v_cvt_pk_bf16_f32 v176, v176, v177\n"
      "v_mfma_f32_16x16x32_bf16 v[164:167], a[196:199], a[48:51], 0\n"
      "v_fma_f32 v181, v181, %[csc], v217\n"
      "v_mfma_f32_16x16x32_bf16 v[144:147], a[200:203], a[4:7], v[144:147]\n"
      "v_exp_f32 v180, v180\n"
      "v_mfma_f32_16x16x32_bf16 v[152:155], a[200:203], a[28:31], v[152:155]\n"
      "v_fma_f32 v182, v182, %[csc], v217\n"
      "v_mfma_f32_16x16x32_bf16 v[160:163], a[200:203], a[52:55], v[160:163]\n"
      "v_exp_f32 v181, v181\n"
      "v_mfma_f32_16x16x32_bf16 v[148:151], a[204:207], a[4:7], v[148:151]\n"
      "v_cvt_pk_bf16_f32 v177, v178, v179\n"
      "v_fma_f32 v183, v183, %[csc], v217\n"
      "v_mfma_f32_16x16x32_bf16 v[156:159], a[204:207], a[28:31], v[156:159]\n"
      "s_waitcnt lgkmcnt(7)\n"
      "v_exp_f32 v182, v182\n"
      "v_mfma_f32_16x16x32_bf16 v[164:167], a[204:207], a[52:55], v[164:167]\n"
      "s_add_u32 s72, s69, 18886784\n"
      "v_fma_f32 v184, v184, %[csc], v218\n"
      "v_mfma_f32_16x16x32_bf16 v[144:147], a[144:147], a[8:11], v[144:147]\n"
      "s_add_u32 m0, %[m0b], 87040\n"
      "v_exp_f32 v183, v183\n"
      "v_mfma_f32_16x16x32_bf16 v[152:155], a[144:147], a[32:35], v[152:155]\n"
      "buffer_load_dwordx4 %[vv1], s[44:47], s71 offen nt lds\n"
      "v_cvt_pk_bf16_f32 v178, v180, v181\n"
      "v_mfma_f32_16x16x32_bf16 v[160:163], a[144:147], a[56:59], v[160:163]\n"
      "s_add_u32 m0, %[m0q], 4096\n"
      "v_fma_f32 v185, v185, %[csc], v218\n"
      "v_mfma_f32_16x16x32_bf16 v[148:151], a[148:151], a[8:11], v[148:151]\n"
      "v_exp_f32 v184, v184\n"
      "v_mfma_f32_16x16x32_bf16 v[156:159], a[148:151], a[32:35], v[156:159]\n"
      "buffer_load_dwordx4 %[vq0], s[64:67], s72 offen nt lds\n"
      "v_fma_f32 v186, v186, %[csc], v218\n"
      "v_mfma_f32_16x16x32_bf16 v[164:167], a[148:151], a[56:59], v[164:167]\n"
      "s_add_u32 m0, %[m0q], 5120\n"
      "v_exp_f32 v185, v185\n"
      "v_mfma_f32_16x16x32_bf16 v[144:147], a[152:155], a[12:15], v[144:147]\n"
      "v_cvt_pk_bf16_f32 v179, v182, v183\n"
      "v_fma_f32 v187, v187, %[csc], v218\n"
      "v_mfma_f32_16x16x32_bf16 v[152:155], a[152:155], a[36:39], v[152:155]\n"
      "v_exp_f32 v186, v186\n"
      "v_mfma_f32_16x16x32_bf16 v[160:163], a[152:155], a[60:63], v[160:163]\n"
      "buffer_load_dwordx4 %[vq1], s[64:67], s72 offen nt lds\n"
      "v_fma_f32 v188, v188, %[csc], v218\n"
      "v_mfma_f32_16x16x32_bf16 v[148:151], a[156:159], a[12:15], v[148:151]\n"
      "v_exp_f32 v187, v187\n"
      "v_mfma_f32_16x16x32_bf16 v[156:159], a[156:159], a[36:39], v[156:159]\n"
      "s_add_u32 m0, %[m0b], 88064\n"
      "v_cvt_pk_bf16_f32 v184, v184, v185\n"
      "v_fma_f32 v189, v189, %[csc], v218\n"
      "v_mfma_f32_16x16x32_bf16 v[164:167], a[156:159], a[60:63], v[164:167]\n"
      "v_exp_f32 v188, v188\n"
      "v_mfma_f32_16x16x32_bf16 v[144:147], a[160:163], a[16:19], v[144:147]\n"
      "buffer_load_dwordx4 %[vv2], s[44:47], s71 offen nt lds\n"
      "v_fma_f32 v190, v190, %[csc], v218\n"
      "v_mfma_f32_16x16x32_bf16 v[152:155], a[160:163], a[40:43], v[152:155]\n"
      "v_exp_f32 v189, v189\n"
      "v_mfma_f32_16x16x32_bf16 v[160:163], a[160:163], a[64:67], v[160:163]\n"
      "v_cvt_pk_bf16_f32 v185, v186, v187\n"
      "v_fma_f32 v191, v191, %[csc], v218\n"
      "v_mfma_f32_16x16x32_bf16 v[148:151], a[164:167], a[16:19], v[148:151]\n"
      "v_exp_f32 v190, v190\n"
      "v_mfma_f32_16x16x32_bf16 v[156:159], a[164:167], a[40:43], v[156:159]\n"
      "v_exp_f32 v191, v191\n"
      "v_mfma_f32_16x16x32_bf16 v[164:167], a[164:167], a[64:67], v[164:167]\n"
      "ds_write_b64 %[ow], a[80:81] offset:0\n"
      "v_cvt_pk_bf16_f32 v186, v188, v189\n"
      "v_mfma_f32_16x16x32_bf16 v[144:147], a[168:171], a[20:23], v[144:147]\n"
      "ds_write_b64 %[ow], a[82:83] offset:32\n"
      "v_cvt_pk_bf16_f32 v187, v190, v191\n"
      "v_mfma_f32_16x16x32_bf16 v[152:155], a[168:171], a[44:47], v[152:155]\n"
      "ds_read_b64_tr_b16 a[188:189], %[vb3] offset:24576\n"
      "ds_read_b64_tr_b16 a[190:191], %[vb3] offset:30720\n"
      "v_mfma_f32_16x16x32_bf16 v[160:163], a[168:171], a[68:71], v[160:163]\n"
      "s_waitcnt lgkmcnt(4)\n"
      "ds_read_b64_tr_b16 a[192:193], %[vb0] offset:24704\n"
      "ds_read_b64_tr_b16 a[194:195], %[vb0] offset:30848\n"
      "v_mfma_f32_16x16x32_bf16 v[148:151], a[172:175], a[20:23], v[148:151]\n"
      "ds_read_b64_tr_b16 a[196:197], %[vb1] offset:24704\n"
      "ds_read_b64_tr_b16 a[198:199], %[vb1] offset:30848\n"
      "v_mfma_f32_16x16x32_bf16 v[156:159], a[172:175], a[44:47], v[156:159]\n"
      "ds_read_b64_tr_b16 a[200:201], %[vb2] offset:24704\n"
      "ds_read_b64_tr_b16 a[202:203], %[vb2] offset:30848\n"
      "v_mfma_f32_16x16x32_bf16 v[164:167], a[172:175], a[68:71], v[164:167]\n"
      "ds_read_b64_tr_b16 a[204:205], %[vb3] offset:24704\n"
      "ds_read_b64_tr_b16 a[206:207], %[vb3] offset:30848\n"
      "v_mfma_f32_16x16x32_bf16 a[208:211], a[220:223], v[168:171], a[208:211]\n"
      "ds_read_b64_tr_b16 a[144:145], %[vb0] offset:24832\n"
      "ds_read_b64_tr_b16 a[146:147], %[vb0] offset:30976\n"
      "v_mfma_f32_16x16x32_bf16 a[212:215], a[220:223], v[176:179], a[212:215]\n"
      "ds_read_b64_tr_b16 a[148:149], %[vb1] offset:24832\n"
      "ds_read_b64_tr_b16 a[150:151], %[vb1] offset:30976\n"
      "v_mfma_f32_16x16x32_bf16 a[216:219], a[220:223], v[184:187], a[216:219]\n"
      "ds_read_b64_tr_b16 a[152:153], %[vb2] offset:24832\n"
      "ds_read_b64_tr_b16 a[154:155], %[vb2] offset:30976\n"
      "v_mfma_f32_16x16x32_bf16 v[0:3], a[176:179], v[168:171], v[0:3]\n"
      "ds_read_b64_tr_b16 a[156:157], %[vb3] offset:24832\n"
      "ds_read_b64_tr_b16 a[158:159], %[vb3] offset:30976\n"
      "v_mfma_f32_16x16x32_bf16 v[48:51], a[176:179], v[176:179], v[48:51]\n"
      "ds_write_b64 %[ow], a[84:85] offset:64\n"
      "v_fma_f32 v144, v144, %[csc], v216\n"
      "v_mfma_f32_16x16x32_bf16 v[96:99], a[176:179], v[184:187], v[96:99]\n"
      "ds_write_b64 %[ow], a[86:87] offset:96\n"
      "v_fma_f32 v145, v145, %[csc], v216\n"
      "v_mfma_f32_16x16x32_bf16 v[4:7], a[180:183], v[168:171], v[4:7]\n"
      "ds_read_b128 a[232:235], %[orb] offset:0\n"
      "v_mfma_f32_16x16x32_bf16 v[52:55], a[180:183], v[176:179], v[52:55]\n"
      "ds_read_b128 a[236:239], %[orb] offset:1152\n"
      "v_mfma_f32_16x16x32_bf16 v[100:103], a[180:183], v[184:187], v[100:103]\n"
      "v_exp_f32 v144, v144\n"
      "s_waitcnt vmcnt(20) lgkmcnt(0)\n"
      "s_barrier\n"
      "v_mfma_f32_16x16x32_bf16 v[8:11], a[184:187], v[168:171], v[8:11]\n"
      "s_add_u32 s70, s68, 110592\n"
      "ds_read_b128 a[160:163], %[kb00] offset:24576\n"
      "ds_read_b128 a[164:167], %[kb00] offset:30720\n"
      "v_mfma_f32_16x16x32_bf16 v[56:59], a[184:187], v[176:179], v[56:59]\n"
      "s_add_u32 m0, %[m0b], 12288\n"
      "ds_read_b128 a[168:171], %[kb10] offset:24576\n"
      "ds_read_b128 a[172:175], %[kb10] offset:30720\n"
      "v_mfma_f32_16x16x32_bf16 v[104:107], a[184:187], v[184:187], v[104:107]\n"
      "buffer_load_dwordx4 %[vk0], s[40:43], s70 offen nt lds\n"
      "ds_read_b128 a[176:179], %[kb00] offset:24704\n"
      "v_mfma_f32_16x16x32_bf16 v[12:15], a[188:191], v[168:171], v[12:15]\n"
      "s_add_u32 s73, s69, 4276092928\n"
      "buffer_store_dwordx4 a[232:235], %[ost], s[84:87], s73 offen offset:128 sc1\n"
      "ds_read_b128 a[180:183], %[kb00] offset:30848\n"
      "v_mfma_f32_16x16x32_bf16 v[60:63], a[188:191], v[176:179], v[60:63]\n"
      "ds_read_b128 a[184:187], %[kb10] offset:24704\n"
      "v_fma_f32 v146, v146, %[csc], v216\n"
      "v_mfma_f32_16x16x32_bf16 v[108:111], a[188:191], v[184:187], v[108:111]\n"
      "ds_read_b128 a[188:191], %[kb10] offset:30848\n"
      "v_mfma_f32_16x16x32_bf16 v[16:19], a[192:195], v[168:171], v[16:19]\n"
      "v_exp_f32 v145, v145\n"
      "v_mfma_f32_16x16x32_bf16 v[64:67], a[192:195], v[176:179], v[64:67]\n"
      "v_fma_f32 v147, v147, %[csc], v216\n"
      "v_mfma_f32_16x16x32_bf16 v[112:115], a[192:195], v[184:187], v[112:115]\n"
      "ds_read_b128 a[192:195], %[kb00] offset:24832\n"
      "v_mfma_f32_16x16x32_bf16 v[20:23], a[196:199], v[168:171], v[20:23]\n"
      "v_exp_f32 v146, v146\n"
      "v_mfma_f32_16x16x32_bf16 v[68:71], a[196:199], v[176:179], v[68:71]\n"
      "s_add_u32 m0, %[m0b], 13312\n"
      "v_fma_f32 v148, v148, %[csc], v216\n"
      "v_mfma_f32_16x16x32_bf16 v[116:119], a[196:199], v[184:187], v[116:119]\n"
      "buffer_load_dwordx4 %[vk1], s[40:43], s70 offen nt lds\n"
      "ds_read_b128 a[196:199], %[kb00] offset:30976\n"
      "v_mfma_f32_16x16x32_bf16 v[24:27], a[200:203], v[168:171], v[24:27]\n"
      "v_exp_f32 v147, v147\n"
      "v_mfma_f32_16x16x32_bf16 v[72:75], a[200:203], v[176:179], v[72:75]\n"
      "v_cvt_pk_bf16_f32 v144, v144, v145\n"
      "v_fma_f32 v149, v149, %[csc], v216\n"
      "v_mfma_f32_16x16x32_bf16 v[120:123], a[200:203], v[184:187], v[120:123]\n"
      "s_add_u32 s73, s69, 4276096000\n"
      "buffer_store_dwordx4 a[236:239], %[ost], s[84:87], s73 offen offset:128 sc1\n"
      "ds_read_b128 a[200:203], %[kb10] offset:24832\n"
      "v_mfma_f32_16x16x32_bf16 v[28:31], a[204:207], v[168:171], v[28:31]\n"
      "v_exp_f32 v148, v148\n"
      "v_mfma_f32_16x16x32_bf16 v[76:79], a[204:207], v[176:179], v[76:79]\n"
      "v_fma_f32 v150, v150, %[csc], v216\n"
      "v_mfma_f32_16x16x32_bf16 v[124:127], a[204:207], v[184:187], v[124:127]\n"
      "ds_read_b128 a[204:207], %[kb10] offset:30976\n"
      "v_mfma_f32_16x16x32_bf16 v[32:35], a[144:147], v[168:171], v[32:35]\n"
      "v_exp_f32 v149, v149\n"
      "v_mfma_f32_16x16x32_bf16 v[80:83], a[144:147], v[176:179], v[80:83]\n"
      "v_cvt_pk_bf16_f32 v145, v146, v147\n"
      "v_fma_f32 v151, v151, %[csc], v216\n"
      "v_mfma_f32_16x16x32_bf16 v[128:131], a[144:147], v[184:187], v[128:131]\n"
      "s_add_u32 m0, %[m0b], 14336\n"
      "ds_read_b64_tr_b16 a[144:145], %[vb0] offset:36864\n"
      "ds_read_b64_tr_b16 a[146:147], %[vb0] offset:43008\n"
      "v_mfma_f32_16x16x32_bf16 v[36:39], a[148:151], v[168:171], v[36:39]\n"
      "buffer_load_dwordx4 %[vk2], s[40:43], s70 offen nt lds\n"
      "v_mfma_f32_16x16x32_bf16 v[84:87], a[148:151], v[176:179], v[84:87]\n"
      "v_exp_f32 v150, v150\n"
      "v_mfma_f32_16x16x32_bf16 v[132:135], a[148:151], v[184:187], v[132:135]\n"
      "ds_read_b64_tr_b16 a[148:149], %[vb1] offset:36864\n"
      "ds_read_b64_tr_b16 a[150:151], %[vb1] offset:43008\n"
      "v_mfma_f32_16x16x32_bf16 v[40:43], a[152:155], v[168:171], v[40:43]\n"
      "v_fma_f32 v152, v152, %[csc], v217\n"
      "v_mfma_f32_16x16x32_bf16 v[88:91], a[152:155], v[176:179], v[88:91]\n"
      "s_waitcnt vmcnt(29)\n"
      "ds_read_b128 a[80:83], %[qs0] offset:0\n"
      "ds_read_b128 a[84:87], %[qs1] offset:0\n"
      "v_mfma_f32_16x16x32_bf16 v[136:139], a[152:155], v[184:187], v[136:139]\n"
      "ds_read_b64_tr_b16 a[152:153], %[vb2] offset:36864\n"
      "ds_read_b64_tr_b16 a[154:155], %[vb2] offset:43008\n"
      "v_mfma_f32_16x16x32_bf16 v[44:47], a[156:159], v[168:171], v[44:47]\n"
      "v_exp_f32 v151, v151\n"
      "v_mfma_f32_16x16x32_bf16 v[92:95], a[156:159], v[176:179], v[92:95]\n"
      "v_cvt_pk_bf16_f32 v146, v148, v149\n"
      "v_fma_f32 v153, v153, %[csc], v217\n"
      "v_mfma_f32_16x16x32_bf16 v[140:143], a[156:159], v[184:187], v[140:143]\n"
      "s_waitcnt lgkmcnt(15)\n"
      "v_exp_f32 v152, v152\n"
      "v_mfma_f32_16x16x32_bf16 v[168:171], a[160:163], a[0:3], 0\n"
      "s_add_u32 s71, s68, 98304\n"
      "v_fma_f32 v154, v154, %[csc], v217\n"
      "v_mfma_f32_16x16x32_bf16 v[176:179], a[160:163], a[24:27], 0\n"
      "s_add_u32 m0, %[m0b], 98304\n"
      "v_exp_f32 v153, v153\n"
      "v_mfma_f32_16x16x32_bf16 v[184:187], a[160:163], a[48:51], 0\n"
      "buffer_load_dwordx4 %[vv0], s[44:47], s71 offen nt lds\n"
      "v_cvt_pk_bf16_f32 v147, v150, v151\n"
      "v_mfma_f32_16x16x32_bf16 v[172:175], a[164:167], a[0:3], 0\n"
      "v_fma_f32 v155, v155, %[csc], v217\n"
      "v_mfma_f32_16x16x32_bf16 v[180:183], a[164:167], a[24:27], 0\n"
      "v_exp_f32 v154, v154\n"
      "v_mfma_f32_16x16x32_bf16 v[188:191], a[164:167], a[48:51], 0\n"
      "v_fma_f32 v156, v156, %[csc], v217\n"
      "v_mfma_f32_16x16x32_bf16 v[168:171], a[168:171], a[4:7], v[168:171]\n"
      "v_exp_f32 v155, v155\n"
      "v_mfma_f32_16x16x32_bf16 v[176:179], a[168:171], a[28:31], v[176:179]\n"
      "v_cvt_pk_bf16_f32 v152, v152, v153\n"
      "v_fma_f32 v157, v157, %[csc], v217\n"
      "v_mfma_f32_16x16x32_bf16 v[184:187], a[168:171], a[52:55], v[184:187]\n"
      "v_exp_f32 v156, v156\n"
      "v_mfma_f32_16x16x32_bf16 v[172:175], a[172:175], a[4:7], v[172:175]\n"
      "v_fma_f32 v158, v158, %[csc], v217\n"
      "v_mfma_f32_16x16x32_bf16 v[180:183], a[172:175], a[28:31], v[180:183]\n"
      "s_add_u32 m0, %[m0b], 99328\n"
      "v_exp_f32 v157, v157\n"
      "v_mfma_f32_16x16x32_bf16 v[188:191], a[172:175], a[52:55], v[188:191]\n"
      "buffer_load_dwordx4 %[vv1], s[44:47], s71 offen nt lds\n"
      "v_cvt_pk_bf16_f32 v153, v154, v155\n"
      "v_mfma_f32_16x16x32_bf16 v[168:171], a[176:179], a[8:11], v[168:171]\n"
      "v_fma_f32 v159, v159, %[csc], v217\n"
      "v_mfma_f32_16x16x32_bf16 v[176:179], a[176:179], a[32:35], v[176:179]\n"
      "v_exp_f32 v158, v158\n"
      "v_mfma_f32_16x16x32_bf16 v[184:187], a[176:179], a[56:59], v[184:187]\n"
      "s_waitcnt lgkmcnt(11)\n"
      "v_fma_f32 v160, v160, %[csc], v218\n"
      "v_mfma_f32_16x16x32_bf16 v[172:175], a[180:183], a[8:11], v[172:175]\n"
      "v_exp_f32 v159, v159\n"
      "v_mfma_f32_16x16x32_bf16 v[180:183], a[180:183], a[32:35], v[180:183]\n"
      "v_cvt_pk_bf16_f32 v154, v156, v157\n"
      "v_fma_f32 v161, v161, %[csc], v218\n"
      "v_mfma_f32_16x16x32_bf16 v[188:191], a[180:183], a[56:59], v[188:191]\n"
      "v_exp_f32 v160, v160\n"
      "v_mfma_f32_16x16x32_bf16 v[168:171], a[184:187], a[12:15], v[168:171]\n"
      "v_fma_f32 v162, v162, %[csc], v218\n"
      "v_mfma_f32_16x16x32_bf16 v[176:179], a[184:187], a[36:39], v[176:179]\n"
      "v_exp_f32 v161, v161\n"
      "v_mfma_f32_16x16x32_bf16 v[184:187], a[184:187], a[60:63], v[184:187]\n"
      "s_add_u32 m0, %[m0b], 100352\n"
      "v_cvt_pk_bf16_f32 v155, v158, v159\n"
      "v_fma_f32 v163, v163, %[csc], v218\n"
      "v_mfma_f32_16x16x32_bf16 v[172:175], a[188:191], a[12:15], v[172:175]\n"
      "s_waitcnt lgkmcnt(2)\n"
      "buffer_load_dwordx4 %[vv2], s[44:47], s71 offen nt lds\n"
      "v_mfma_f32_16x16x32_bf16 v[180:183], a[188:191], a[36:39], v[180:183]\n"
      "s_add_u32 s72, s69, 18874624\n"
      "v_exp_f32 v162, v162\n"
      "v_mfma_f32_16x16x32_bf16 v[188:191], a[188:191], a[60:63], v[188:191]\n"
      "s_add_u32 m0, %[m0q], 0\n"
      "v_fma_f32 v164, v164, %[csc], v218\n"
      "v_mfma_f32_16x16x32_bf16 v[168:171], a[192:195], a[16:19], v[168:171]\n"
      "v_exp_f32 v163, v163\n"
      "v_mfma_f32_16x16x32_bf16 v[176:179], a[192:195], a[40:43], v[176:179]\n"
      "v_cvt_pk_bf16_f32 v160, v160, v161\n"
      "v_fma_f32 v165, v165, %[csc], v218\n"
      "v_mfma_f32_16x16x32_bf16 v[184:187], a[192:195], a[64:67], v[184:187]\n"
      "v_exp_f32 v164, v164\n"
      "v_mfma_f32_16x16x32_bf16 v[172:175], a[196:199], a[16:19], v[172:175]\n"
      "buffer_load_dwordx4 %[vq0], s[64:67], s72 offen nt lds\n"
      "v_fma_f32 v166, v166, %[csc], v218\n"
      "v_mfma_f32_16x16x32_bf16 v[180:183], a[196:199], a[40:43], v[180:183]\n"
      "s_add_u32 m0, %[m0q], 1024\n"
      "v_exp_f32 v165, v165\n"
      "v_mfma_f32_16x16x32_bf16 v[188:191], a[196:199], a[64:67], v[188:191]\n"
      "v_cvt_pk_bf16_f32 v161, v162, v163\n"
      "v_fma_f32 v167, v167, %[csc], v218\n"
      "v_mfma_f32_16x16x32_bf16 v[168:171], a[200:203], a[20:23], v[168:171]\n"
      "v_exp_f32 v166, v166\n"
      "v_mfma_f32_16x16x32_bf16 v[176:179], a[200:203], a[44:47], v[176:179]\n"
      "v_exp_f32 v167, v167\n"
      "v_mfma_f32_16x16x32_bf16 v[184:187], a[200:203], a[68:71], v[184:187]\n"
      "buffer_load_dwordx4 %[vq1], s[64:67], s72 offen nt lds\n"
      "v_cvt_pk_bf16_f32 v162, v164, v165\n"
      "v_mfma_f32_16x16x32_bf16 v[172:175], a[204:207], a[20:23], v[172:175]\n"
      "ds_write_b64 %[ow], a[104:105] offset:0\n"
      "v_cvt_pk_bf16_f32 v163, v166, v167\n"
      "v_mfma_f32_16x16x32_bf16 v[180:183], a[204:207], a[44:47], v[180:183]\n"
      "ds_read_b64_tr_b16 a[156:157], %[vb3] offset:36864\n"
      "ds_read_b64_tr_b16 a[158:159], %[vb3] offset:43008\n"
      "v_mfma_f32_16x16x32_bf16 v[188:191], a[204:207], a[68:71], v[188:191]\n"
      "ds_read_b64_tr_b16 a[160:161], %[vb0] offset:36992\n"
      "ds_read_b64_tr_b16 a[162:163], %[vb0] offset:43136\n"
      "v_mfma_f32_16x16x32_bf16 a[208:211], a[220:223], v[144:147], a[208:211]\n"
      "ds_read_b64_tr_b16 a[164:165], %[vb1] offset:36992\n"
      "ds_read_b64_tr_b16 a[166:167], %[vb1] offset:43136\n"
      "v_mfma_f32_16x16x32_bf16 a[212:215], a[220:223], v[152:155], a[212:215]\n"
      "ds_read_b64_tr_b16 a[168:169], %[vb2] offset:36992\n"
      "ds_read_b64_tr_b16 a[170:171], %[vb2] offset:43136\n"
      "v_mfma_f32_16x16x32_bf16 a[216:219], a[220:223], v[160:163], a[216:219]\n"
      "ds_read_b64_tr_b16 a[172:173], %[vb3] offset:36992\n"
      "ds_read_b64_tr_b16 a[174:175], %[vb3] offset:43136\n"
      "v_mfma_f32_16x16x32_bf16 v[0:3], a[144:147], v[144:147], v[0:3]\n"
      "ds_read_b64_tr_b16 a[176:177], %[vb0] offset:37120\n"
      "ds_read_b64_tr_b16 a[178:179], %[vb0] offset:43264\n"
      "v_mfma_f32_16x16x32_bf16 v[48:51], a[144:147], v[152:155], v[48:51]\n"
      "ds_read_b64_tr_b16 a[180:181], %[vb1] offset:37120\n"
      "ds_read_b64_tr_b16 a[182:183], %[vb1] offset:43264\n"
      "v_mfma_f32_16x16x32_bf16 v[96:99], a[144:147], v[160:163], v[96:99]\n"
      "ds_read_b64_tr_b16 a[184:185], %[vb2] offset:37120\n"
      "ds_read_b64_tr_b16 a[186:187], %[vb2] offset:43264\n"
      "v_mfma_f32_16x16x32_bf16 v[4:7], a[148:151], v[144:147], v[4:7]\n"
      "ds_read_b64_tr_b16 a[188:189], %[vb3] offset:37120\n"
      "ds_read_b64_tr_b16 a[190:191], %[vb3] offset:43264\n"
      "v_mfma_f32_16x16x32_bf16 v[52:55], a[148:151], v[152:155], v[52:55]\n"
      "ds_write_b64 %[ow], a[106:107] offset:32\n"
      "v_fma_f32 v168, v168, %[csc], v216\n"
      "v_mfma_f32_16x16x32_bf16 v[100:103], a[148:151], v[160:163], v[100:103]\n"
      "s_waitcnt lgkmcnt(15)\n"
      "ds_write_b64 %[ow], a[108:109] offset:64\n"
      "v_fma_f32 v169, v169, %[csc], v216\n"
      "v_mfma_f32_16x16x32_bf16 v[8:11], a[152:155], v[144:147], v[8:11]\n"
      "ds_write_b64 %[ow], a[110:111] offset:96\n"
      "v_mfma_f32_16x16x32_bf16 v[56:59], a[152:155], v[152:155], v[56:59]\n"
      "ds_read_b128 a[224:227], %[orb] offset:0\n"
      "v_mfma_f32_16x16x32_bf16 v[104:107], a[152:155], v[160:163], v[104:107]\n"
      "ds_read_b128 a[228:231], %[orb] offset:1152\n"
      "s_waitcnt vmcnt(20) lgkmcnt(0)\n"
      "s_barrier\n"
      "v_mfma_f32_16x16x32_bf16 v[12:15], a[156:159], v[144:147], v[12:15]\n"
      "s_add_u32 s70, s68, 122880\n"
      "ds_read_b128 a[192:195], %[kb01] offset:0\n"
      "ds_read_b128 a[196:199], %[kb01] offset:6144\n"
      "v_mfma_f32_16x16x32_bf16 v[60:63], a[156:159], v[152:155], v[60:63]\n"
      "s_add_u32 m0, %[m0b], 24576\n"
      "ds_read_b128 a[200:203], %[kb11] offset:0\n"
      "ds_read_b128 a[204:207], %[kb11] offset:6144\n"
      "v_mfma_f32_16x16x32_bf16 v[108:111], a[156:159], v[160:163], v[108:111]\n"
      "buffer_load_dwordx4 %[vk0], s[40:43], s70 offen nt lds\n"
      "ds_read_b128 a[144:147], %[kb01] offset:128\n"
      "v_mfma_f32_16x16x32_bf16 v[16:19], a[160:163], v[144:147], v[16:19]\n"
      "ds_read_b128 a[148:151], %[kb01] offset:6272\n"
      "ds_read_b128 a[152:155], %[kb11] offset:128\n"
      "v_mfma_f32_16x16x32_bf16 v[64:67], a[160:163], v[152:155], v[64:67]\n"
      "ds_read_b128 a[156:159], %[kb11] offset:6272\n"
      "v_mfma_f32_16x16x32_bf16 v[112:115], a[160:163], v[160:163], v[112:115]\n"
      "ds_read_b128 a[160:163], %[kb01] offset:256\n"
      "v_mfma_f32_16x16x32_bf16 v[20:23], a[164:167], v[144:147], v[20:23]\n"
      "v_exp_f32 v168, v168\n"
      "v_mfma_f32_16x16x32_bf16 v[68:71], a[164:167], v[152:155], v[68:71]\n"
      "v_fma_f32 v170, v170, %[csc], v216\n"
      "v_mfma_f32_16x16x32_bf16 v[116:119], a[164:167], v[160:163], v[116:119]\n"
      "ds_read_b128 a[164:167], %[kb01] offset:6400\n"
      "v_mfma_f32_16x16x32_bf16 v[24:27], a[168:171], v[144:147], v[24:27]\n"
      "s_add_u32 s73, s69, 4276099072\n"
      "buffer_store_dwordx4 a[224:227], %[ost], s[84:87], s73 offen offset:128 sc1\n"
      "v_mfma_f32_16x16x32_bf16 v[72:75], a[168:171], v[152:155], v[72:75]\n"
      "s_add_u32 m0, %[m0b], 25600\n"
      "v_exp_f32 v169, v169\n"
      "v_mfma_f32_16x16x32_bf16 v[120:123], a[168:171], v[160:163], v[120:123]\n"
      "buffer_load_dwordx4 %[vk1], s[40:43], s70 offen nt lds\n"
      "ds_read_b128 a[168:171], %[kb11] offset:256\n"
      "v_mfma_f32_16x16x32_bf16 v[28:31], a[172:175], v[144:147], v[28:31]\n"
      "v_fma_f32 v171, v171, %[csc], v216\n"
      "v_mfma_f32_16x16x32_bf16 v[76:79], a[172:175], v[152:155], v[76:79]\n"
      "v_exp_f32 v170, v170\n"
      "v_mfma_f32_16x16x32_bf16 v[124:127], a[172:175], v[160:163], v[124:127]\n"
      "ds_read_b128 a[172:175], %[kb11] offset:6400\n"
      "v_fma_f32 v172, v172, %[csc], v216\n"
      "v_mfma_f32_16x16x32_bf16 v[32:35], a[176:179], v[144:147], v[32:35]\n"
      "v_exp_f32 v171, v171\n"
      "v_mfma_f32_16x16x32_bf16 v[80:83], a[176:179], v[152:155], v[80:83]\n"
      "v_cvt_pk_bf16_f32 v168, v168, v169\n"
      "v_fma_f32 v173, v173, %[csc], v216\n"
      "v_mfma_f32_16x16x32_bf16 v[128:131], a[176:179], v[160:163], v[128:131]\n"
      "ds_read_b64_tr_b16 a[176:177], %[vb0] offset:0\n"
      "ds_read_b64_tr_b16 a[178:179], %[vb0] offset:6144\n"
      "v_mfma_f32_16x16x32_bf16 v[36:39], a[180:183], v[144:147], v[36:39]\n"
      "v_exp_f32 v172, v172\n"
      "v_mfma_f32_16x16x32_bf16 v[84:87], a[180:183], v[152:155], v[84:87]\n"
      "v_fma_f32 v174, v174, %[csc], v216\n"
      "v_mfma_f32_16x16x32_bf16 v[132:135], a[180:183], v[160:163], v[132:135]\n"
      "s_add_u32 m0, %[m0b], 26624\n"
      "ds_read_b64_tr_b16 a[180:181], %[vb1] offset:0\n"
      "ds_read_b64_tr_b16 a[182:183], %[vb1] offset:6144\n"
      "v_mfma_f32_16x16x32_bf16 v[40:43], a[184:187], v[144:147], v[40:43]\n"
      "buffer_load_dwordx4 %[vk2], s[40:43], s70 offen nt lds\n"
      "v_mfma_f32_16x16x32_bf16 v[88:91], a[184:187], v[152:155], v[88:91]\n"
      "s_add_u32 s73, s69, 4276102144\n"
      "buffer_store_dwordx4 a[228:231], %[ost], s[84:87], s73 offen offset:128 sc1\n"
      "v_mfma_f32_16x16x32_bf16 v[136:139], a[184:187], v[160:163], v[136:139]\n"
      "ds_read_b64_tr_b16 a[184:185], %[vb2] offset:0\n"
      "ds_read_b64_tr_b16 a[186:187], %[vb2] offset:6144\n"
      "v_mfma_f32_16x16x32_bf16 v[44:47], a[188:191], v[144:147], v[44:47]\n"
      "v_exp_f32 v173, v173\n"
      "v_mfma_f32_16x16x32_bf16 v[92:95], a[188:191], v[152:155], v[92:95]\n"
      "v_cvt_pk_bf16_f32 v169, v170, v171\n"
      "v_fma_f32 v175, v175, %[csc], v216\n"
      "v_mfma_f32_16x16x32_bf16 v[140:143], a[188:191], v[160:163], v[140:143]\n"
      "s_waitcnt lgkmcnt(14)\n"
      "v_exp_f32 v174, v174\n"
      "v_mfma_f32_16x16x32_bf16 v[144:147], a[192:195], a[0:3], 0\n"
      "v_fma_f32 v176, v176, %[csc], v217\n"
      "v_mfma_f32_16x16x32_bf16 v[152:155], a[192:195], a[24:27], 0\n"
      "v_exp_f32 v175, v175\n"
      "v_mfma_f32_16x16x32_bf16 v[160:163], a[192:195], a[48:51], 0\n"
      "v_cvt_pk_bf16_f32 v170, v172, v173\n"
      "v_fma_f32 v177, v177, %[csc], v217\n"
      "v_mfma_f32_16x16x32_bf16 v[148:151], a[196:199], a[0:3], 0\n"
      "s_add_u32 s71, s68, 110592\n"
      "v_exp_f32 v176, v176\n"
      "v_mfma_f32_16x16x32_bf16 v[156:159], a[196:199], a[24:27], 0\n"
      "s_add_u32 m0, %[m0b], 110592\n"
      "v_fma_f32 v178, v178, %[csc], v217\n"
      "v_mfma_f32_16x16x32_bf16 v[164:167], a[196:199], a[48:51], 0\n"
      "s_waitcnt vmcnt(27)\n"
      "buffer_load_dwordx4 %[vv0], s[44:47], s71 offen nt lds\n"
      "ds_read_b128 a[104:107], %[qs0] offset:2048\n"
      "v_mfma_f32_16x16x32_bf16 v[144:147], a[200:203], a[4:7], v[144:147]\n"
      "ds_read_b128 a[108:111], %[qs1] offset:2048\n"
      "v_mfma_f32_16x16x32_bf16 v[152:155], a[200:203], a[28:31], v[152:155]\n"
      "v_exp_f32 v177, v177\n"
      "v_mfma_f32_16x16x32_bf16 v[160:163], a[200:203], a[52:55], v[160:163]\n"
      "v_cvt_pk_bf16_f32 v171, v174, v175\n"
      "v_fma_f32 v179, v179, %[csc], v217\n"
      "v_mfma_f32_16x16x32_bf16 v[148:151], a[204:207], a[4:7], v[148:151]\n"
      "v_exp_f32 v178, v178\n"
      "v_mfma_f32_16x16x32_bf16 v[156:159], a[204:207], a[28:31], v[156:159]\n"
      "v_fma_f32 v180, v180, %[csc], v217\n"
      "v_mfma_f32_16x16x32_bf16 v[164:167], a[204:207], a[52:55], v[164:167]\n"
      "s_waitcnt lgkmcnt(12)\n"
      "v_exp_f32 v179, v179\n"
      "v_mfma_f32_16x16x32_bf16 v[144:147], a[144:147], a[8:11], v[144:147]\n"
      "v_cvt_pk_bf16_f32 v176, v176, v177\n"
      "v_fma_f32 v181, v181, %[csc], v217\n"
      "v_mfma_f32_16x16x32_bf16 v[152:155], a[144:147], a[32:35], v[152:155]\n"
      "s_add_u32 m0, %[m0b], 111616\n"
      "v_exp_f32 v180, v180\n"
      "v_mfma_f32_16x16x32_bf16 v[160:163], a[144:147], a[56:59], v[160:163]\n"
      "buffer_load_dwordx4 %[vv1], s[44:47], s71 offen nt lds\n"
      "v_fma_f32 v182, v182, %[csc], v217\n"
      "v_mfma_f32_16x16x32_bf16 v[148:151], a[148:151], a[8:11], v[148:151]\n"
      "v_exp_f32 v181, v181\n"
      "v_mfma_f32_16x16x32_bf16 v[156:159], a[148:151], a[32:35], v[156:159]\n"
      "v_cvt_pk_bf16_f32 v177, v178, v179\n"
      "v_fma_f32 v183, v183, %[csc], v217\n"
      "v_mfma_f32_16x16x32_bf16 v[164:167], a[148:151], a[56:59], v[164:167]\n"
      "v_exp_f32 v182, v182\n"
      "v_mfma_f32_16x16x32_bf16 v[144:147], a[152:155], a[12:15], v[144:147]\n"
      "v_fma_f32 v184, v184, %[csc], v218\n"
      "v_mfma_f32_16x16x32_bf16 v[152:155], a[152:155], a[36:39], v[152:155]\n"
      "v_exp_f32 v183, v183\n"
      "v_mfma_f32_16x16x32_bf16 v[160:163], a[152:155], a[60:63], v[160:163]\n"
      "v_cvt_pk_bf16_f32 v178, v180, v181\n"
      "v_fma_f32 v185, v185, %[csc], v218\n"
      "v_mfma_f32_16x16x32_bf16 v[148:151], a[156:159], a[12:15], v[148:151]\n"
      "v_exp_f32 v184, v184\n"
      "v_mfma_f32_16x16x32_bf16 v[156:159], a[156:159], a[36:39], v[156:159]\n"
      "v_fma_f32 v186, v186, %[csc], v218\n"
      "v_mfma_f32_16x16x32_bf16 v[164:167], a[156:159], a[60:63], v[164:167]\n"
      "s_waitcnt lgkmcnt(8)\n"
      "v_exp_f32 v185, v185\n"
      "v_mfma_f32_16x16x32_bf16 v[144:147], a[160:163], a[16:19], v[144:147]\n"
      "s_add_u32 m0, %[m0b], 112640\n"
      "v_cvt_pk_bf16_f32 v179, v182, v183\n"
      "v_fma_f32 v187, v187, %[csc], v218\n"
      "v_mfma_f32_16x16x32_bf16 v[152:155], a[160:163], a[40:43], v[152:155]\n"
      "v_exp_f32 v186, v186\n"
      "v_mfma_f32_16x16x32_bf16 v[160:163], a[160:163], a[64:67], v[160:163]\n"
      "buffer_load_dwordx4 %[vv2], s[44:47], s71 offen nt lds\n"
      "v_fma_f32 v188, v188, %[csc], v218\n"
      "v_mfma_f32_16x16x32_bf16 v[148:151], a[164:167], a[16:19], v[148:151]\n"
      "v_exp_f32 v187, v187\n"
      "v_mfma_f32_16x16x32_bf16 v[156:159], a[164:167], a[40:43], v[156:159]\n"
      "v_cvt_pk_bf16_f32 v184, v184, v185\n"
      "v_fma_f32 v189, v189, %[csc], v218\n"
      "v_mfma_f32_16x16x32_bf16 v[164:167], a[164:167], a[64:67], v[164:167]\n"
      "v_exp_f32 v188, v188\n"
      "v_mfma_f32_16x16x32_bf16 v[144:147], a[168:171], a[20:23], v[144:147]\n"
      "s_waitcnt lgkmcnt(0)\n"
      "v_fma_f32 v190, v190, %[csc], v218\n"
      "v_mfma_f32_16x16x32_bf16 v[152:155], a[168:171], a[44:47], v[152:155]\n"
      "s_add_u32 s72, s69, 18880768\n"
      "v_exp_f32 v189, v189\n"
      "v_mfma_f32_16x16x32_bf16 v[160:163], a[168:171], a[68:71], v[160:163]\n"
      "s_add_u32 m0, %[m0q], 2048\n"
      "v_cvt_pk_bf16_f32 v185, v186, v187\n"
      "v_fma_f32 v191, v191, %[csc], v218\n"
      "v_mfma_f32_16x16x32_bf16 v[148:151], a[172:175], a[20:23], v[148:151]\n"
      "v_exp_f32 v190, v190\n"
      "v_mfma_f32_16x16x32_bf16 v[156:159], a[172:175], a[44:47], v[156:159]\n"
      "v_exp_f32 v191, v191\n"
      "v_mfma_f32_16x16x32_bf16 v[164:167], a[172:175], a[68:71], v[164:167]\n"
      "buffer_load_dwordx4 %[vq0], s[64:67], s72 offen nt lds\n"
      "v_cvt_pk_bf16_f32 v186, v188, v189\n"
      "v_cvt_pk_bf16_f32 v187, v190, v191\n"
      "v_mfma_f32_16x16x32_bf16 a[208:211], a[220:223], v[168:171], a[208:211]\n"
      "s_add_u32 m0, %[m0q], 3072\n"
      "ds_read_b64_tr_b16 a[188:189], %[vb3] offset:0\n"
      "ds_read_b64_tr_b16 a[190:191], %[vb3] offset:6144\n"
      "v_mfma_f32_16x16x32_bf16 a[212:215], a[220:223], v[176:179], a[212:215]\n"
      "ds_read_b64_tr_b16 a[192:193], %[vb0] offset:128\n"
      "ds_read_b64_tr_b16 a[194:195], %[vb0] offset:6272\n"
      "v_mfma_f32_16x16x32_bf16 a[216:219], a[220:223], v[184:187], a[216:219]\n"
      "ds_read_b64_tr_b16 a[196:197], %[vb1] offset:128\n"
      "ds_read_b64_tr_b16 a[198:199], %[vb1] offset:6272\n"
      "v_mfma_f32_16x16x32_bf16 v[0:3], a[176:179], v[168:171], v[0:3]\n"
      "ds_read_b64_tr_b16 a[200:201], %[vb2] offset:128\n"
      "ds_read_b64_tr_b16 a[202:203], %[vb2] offset:6272\n"
      "v_mfma_f32_16x16x32_bf16 v[48:51], a[176:179], v[176:179], v[48:51]\n"
      "ds_read_b64_tr_b16 a[204:205], %[vb3] offset:128\n"
      "ds_read_b64_tr_b16 a[206:207], %[vb3] offset:6272\n"
      "v_mfma_f32_16x16x32_bf16 v[96:99], a[176:179], v[184:187], v[96:99]\n"
      "ds_read_b64_tr_b16 a[144:145], %[vb0] offset:256\n"
      "ds_read_b64_tr_b16 a[146:147], %[vb0] offset:6400\n"
      "v_mfma_f32_16x16x32_bf16 v[4:7], a[180:183], v[168:171], v[4:7]\n"
      "ds_read_b64_tr_b16 a[148:149], %[vb1] offset:256\n"
      "ds_read_b64_tr_b16 a[150:151], %[vb1] offset:6400\n"
      "v_mfma_f32_16x16x32_bf16 v[52:55], a[180:183], v[176:179], v[52:55]\n"
      "ds_read_b64_tr_b16 a[152:153], %[vb2] offset:256\n"
      "ds_read_b64_tr_b16 a[154:155], %[vb2] offset:6400\n"
      "v_mfma_f32_16x16x32_bf16 v[100:103], a[180:183], v[184:187], v[100:103]\n"
      "ds_read_b64_tr_b16 a[156:157], %[vb3] offset:256\n"
      "ds_read_b64_tr_b16 a[158:159], %[vb3] offset:6400\n"
      "v_mfma_f32_16x16x32_bf16 v[8:11], a[184:187], v[168:171], v[8:11]\n"
      "buffer_load_dwordx4 %[vq1], s[64:67], s72 offen nt lds\n"
      "ds_write_b64 %[ow], a[128:129] offset:0\n"
      "v_mfma_f32_16x16x32_bf16 v[56:59], a[184:187], v[176:179], v[56:59]\n"
      "ds_write_b64 %[ow], a[130:131] offset:32\n"
      "v_fma_f32 v144, v144, %[csc], v216\n"
      "v_mfma_f32_16x16x32_bf16 v[104:107], a[184:187], v[184:187], v[104:107]\n"
      "s_waitcnt lgkmcnt(14)\n"
      "ds_write_b64 %[ow], a[132:133] offset:64\n"
      "v_fma_f32 v145, v145, %[csc], v216\n"
      "v_mfma_f32_16x16x32_bf16 v[12:15], a[188:191], v[168:171], v[12:15]\n"
      "v_exp_f32 v144, v144\n"
      "v_mfma_f32_16x16x32_bf16 v[60:63], a[188:191], v[176:179], v[60:63]\n"
      "ds_write_b64 %[ow], a[134:135] offset:96\n"
      "v_fma_f32 v146, v146, %[csc], v216\n"
      "s_waitcnt vmcnt(20) lgkmcnt(0)\n"
      "s_barrier\n"
      "v_mfma_f32_16x16x32_bf16 v[108:111], a[188:191], v[184:187], v[108:111]\n"
      "s_add_u32 s70, s68, 135168\n"
      "ds_read_b128 a[160:163], %[kb00] offset:0\n"
      "ds_read_b128 a[164:167], %[kb00] offset:6144\n"
      "v_mfma_f32_16x16x32_bf16 v[16:19], a[192:195], v[168:171], v[16:19]\n"
      "s_add_u32 m0, %[m0b], 36864\n"
      "ds_read_b128 a[168:171], %[kb10] offset:0\n"
      "ds_read_b128 a[172:175], %[kb10] offset:6144\n"
      "v_mfma_f32_16x16x32_bf16 v[64:67], a[192:195], v[176:179], v[64:67]\n"
      "buffer_load_dwordx4 %[vk0], s[40:43], s70 offen nt lds\n"
      "ds_read_b128 a[176:179], %[kb00] offset:128\n"
      "v_mfma_f32_16x16x32_bf16 v[112:115], a[192:195], v[184:187], v[112:115]\n"
      "ds_read_b128 a[180:183], %[kb00] offset:6272\n"
      "ds_read_b128 a[184:187], %[kb10] offset:128\n"
      "v_mfma_f32_16x16x32_bf16 v[20:23], a[196:199], v[168:171], v[20:23]\n"
      "ds_read_b128 a[188:191], %[kb10] offset:6272\n"
      "ds_read_b128 a[192:195], %[kb00] offset:256\n"
      "v_mfma_f32_16x16x32_bf16 v[68:71], a[196:199], v[176:179], v[68:71]\n"
      "ds_read_b128 a[232:235], %[orb] offset:0\n"
      "v_mfma_f32_16x16x32_bf16 v[116:119], a[196:199], v[184:187], v[116:119]\n"
      "ds_read_b128 a[196:199], %[kb00] offset:6400\n"
      "ds_read_b128 a[236:239], %[orb] offset:1152\n"
      "v_mfma_f32_16x16x32_bf16 v[24:27], a[200:203], v[168:171], v[24:27]\n"
      "v_exp_f32 v145, v145\n"
      "v_mfma_f32_16x16x32_bf16 v[72:75], a[200:203], v[176:179], v[72:75]\n"
      "v_fma_f32 v147, v147, %[csc], v216\n"
      "v_mfma_f32_16x16x32_bf16 v[120:123], a[200:203], v[184:187], v[120:123]\n"
      "ds_read_b128 a[200:203], %[kb10] offset:256\n"
      "v_mfma_f32_16x16x32_bf16 v[28:31], a[204:207], v[168:171], v[28:31]\n"
      "s_add_u32 m0, %[m0b], 37888\n"
      "v_exp_f32 v146, v146\n"
      "v_mfma_f32_16x16x32_bf16 v[76:79], a[204:207], v[176:179], v[76:79]\n"
      "buffer_load_dwordx4 %[vk1], s[40:43], s70 offen nt lds\n"
      "v_fma_f32 v148, v148, %[csc], v216\n"
      "v_mfma_f32_16x16x32_bf16 v[124:127], a[204:207], v[184:187], v[124:127]\n"
      "ds_read_b128 a[204:207], %[kb10] offset:6400\n"
      "v_mfma_f32_16x16x32_bf16 v[32:35], a[144:147], v[168:171], v[32:35]\n"
      "v_exp_f32 v147, v147\n"
      "v_mfma_f32_16x16x32_bf16 v[80:83], a[144:147], v[176:179], v[80:83]\n"
      "v_cvt_pk_bf16_f32 v144, v144, v145\n"
      "v_fma_f32 v149, v149, %[csc], v216\n"
      "v_mfma_f32_16x16x32_bf16 v[128:131], a[144:147], v[184:187], v[128:131]\n"
      "ds_read_b64_tr_b16 a[144:145], %[vb0] offset:12288\n"
      "ds_read_b64_tr_b16 a[146:147], %[vb0] offset:18432\n"
      "v_mfma_f32_16x16x32_bf16 v[36:39], a[148:151], v[168:171], v[36:39]\n"
      "s_waitcnt lgkmcnt(6)\n"
      "v_exp_f32 v148, v148\n"
      "v_mfma_f32_16x16x32_bf16 v[84:87], a[148:151], v[176:179], v[84:87]\n"
      "s_add_u32 s73, s69, 4276105216\n"
      "buffer_store_dwordx4 a[232:235], %[ost], s[84:87], s73 offen offset:128 sc1\n"
      "v_fma_f32 v150, v150, %[csc], v216\n"
      "v_mfma_f32_16x16x32_bf16 v[132:135], a[148:151], v[184:187], v[132:135]\n"
      "ds_read_b64_tr_b16 a[148:149], %[vb1] offset:12288\n"
      "ds_read_b64_tr_b16 a[150:151], %[vb1] offset:18432\n"
      "v_mfma_f32_16x16x32_bf16 v[40:43], a[152:155], v[168:171], v[40:43]\n"
      "v_exp_f32 v149, v149\n"
      "v_mfma_f32_16x16x32_bf16 v[88:91], a[152:155], v[176:179], v[88:91]\n"
      "s_add_u32 m0, %[m0b], 38912\n"
      "v_cvt_pk_bf16_f32 v145, v146, v147\n"
      "v_fma_f32 v151, v151, %[csc], v216\n"
      "v_mfma_f32_16x16x32_bf16 v[136:139], a[152:155], v[184:187], v[136:139]\n"
      "buffer_load_dwordx4 %[vk2], s[40:43], s70 offen nt lds\n"
      "v_mfma_f32_16x16x32_bf16 v[44:47], a[156:159], v[168:171], v[44:47]\n"
      "ds_read_b64_tr_b16 a[152:153], %[vb2] offset:12288\n"
      "ds_read_b64_tr_b16 a[154:155], %[vb2] offset:18432\n"
      "v_mfma_f32_16x16x32_bf16 v[92:95], a[156:159], v[176:179], v[92:95]\n"
      "v_exp_f32 v150, v150\n"
      "v_mfma_f32_16x16x32_bf16 v[140:143], a[156:159], v[184:187], v[140:143]\n"
      "v_fma_f32 v152, v152, %[csc], v217\n"
      "v_mfma_f32_16x16x32_bf16 v[168:171], a[160:163], a[0:3], 0\n"
      "v_exp_f32 v151, v151\n"
      "v_mfma_f32_16x16x32_bf16 v[176:179], a[160:163], a[24:27], 0\n"
      "v_cvt_pk_bf16_f32 v146, v148, v149\n"
      "v_fma_f32 v153, v153, %[csc], v217\n"
      "v_mfma_f32_16x16x32_bf16 v[184:187], a[160:163], a[48:51], 0\n"
      "v_exp_f32 v152, v152\n"
      "v_mfma_f32_16x16x32_bf16 v[172:175], a[164:167], a[0:3], 0\n"
      "s_waitcnt lgkmcnt(8)\n"
      "v_fma_f32 v154, v154, %[csc], v217\n"
      "v_mfma_f32_16x16x32_bf16 v[180:183], a[164:167], a[24:27], 0\n"
      "s_add_u32 s73, s69, 4276108288\n"
      "buffer_store_dwordx4 a[236:239], %[ost], s[84:87], s73 offen offset:128 sc1\n"
      "v_mfma_f32_16x16x32_bf16 v[188:191], a[164:167], a[48:51], 0\n"
      "s_add_u32 s71, s68, 122880\n"
      "v_exp_f32 v153, v153\n"
      "v_mfma_f32_16x16x32_bf16 v[168:171], a[168:171], a[4:7], v[168:171]\n"
      "s_add_u32 m0, %[m0b], 73728\n"
      "v_cvt_pk_bf16_f32 v147, v150, v151\n"
      "v_fma_f32 v155, v155, %[csc], v217\n"
      "v_mfma_f32_16x16x32_bf16 v[176:179], a[168:171], a[28:31], v[176:179]\n"
      "buffer_load_dwordx4 %[vv0], s[44:47], s71 offen nt lds\n"
      "v_mfma_f32_16x16x32_bf16 v[184:187], a[168:171], a[52:55], v[184:187]\n"
      "v_exp_f32 v154, v154\n"
      "v_mfma_f32_16x16x32_bf16 v[172:175], a[172:175], a[4:7], v[172:175]\n"
      "v_fma_f32 v156, v156, %[csc], v217\n"
      "v_mfma_f32_16x16x32_bf16 v[180:183], a[172:175], a[28:31], v[180:183]\n"
      "v_exp_f32 v155, v155\n"
      "v_mfma_f32_16x16x32_bf16 v[188:191], a[172:175], a[52:55], v[188:191]\n"
      "v_cvt_pk_bf16_f32 v152, v152, v153\n"
      "v_fma_f32 v157, v157, %[csc], v217\n"
      "v_mfma_f32_16x16x32_bf16 v[168:171], a[176:179], a[8:11], v[168:171]\n"
      "v_exp_f32 v156, v156\n"
      "v_mfma_f32_16x16x32_bf16 v[176:179], a[176:179], a[32:35], v[176:179]\n"
      "v_fma_f32 v158, v158, %[csc], v217\n"
      "v_mfma_f32_16x16x32_bf16 v[184:187], a[176:179], a[56:59], v[184:187]\n"
      "v_exp_f32 v157, v157\n"
      "v_mfma_f32_16x16x32_bf16 v[172:175], a[180:183], a[8:11], v[172:175]\n"
      "s_add_u32 m0, %[m0b], 74752\n"
      "v_cvt_pk_bf16_f32 v153, v154, v155\n"
      "v_fma_f32 v159, v159, %[csc], v217\n"
      "v_mfma_f32_16x16x32_bf16 v[180:183], a[180:183], a[32:35], v[180:183]\n"
      "s_waitcnt vmcnt(27)\n"
      "buffer_load_dwordx4 %[vv1], s[44:47], s71 offen nt lds\n"
      "ds_read_b128 a[128:131], %[qs0] offset:4096\n"
      "v_mfma_f32_16x16x32_bf16 v[188:191], a[180:183], a[56:59], v[188:191]\n"
      "v_exp_f32 v158, v158\n"
      "v_mfma_f32_16x16x32_bf16 v[168:171], a[184:187], a[12:15], v[168:171]\n"
      "ds_read_b128 a[132:135], %[qs1] offset:4096\n"
      "v_fma_f32 v160, v160, %[csc], v218\n"
      "v_mfma_f32_16x16x32_bf16 v[176:179], a[184:187], a[36:39], v[176:179]\n"
      "v_exp_f32 v159, v159\n"
      "v_mfma_f32_16x16x32_bf16 v[184:187], a[184:187], a[60:63], v[184:187]\n"
      "v_cvt_pk_bf16_f32 v154, v156, v157\n"
      "v_fma_f32 v161, v161, %[csc], v218\n"
      "v_mfma_f32_16x16x32_bf16 v[172:175], a[188:191], a[12:15], v[172:175]\n"
      "v_exp_f32 v160, v160\n"
      "v_mfma_f32_16x16x32_bf16 v[180:183], a[188:191], a[36:39], v[180:183]\n"
      "v_fma_f32 v162, v162, %[csc], v218\n"
      "v_mfma_f32_16x16x32_bf16 v[188:191], a[188:191], a[60:63], v[188:191]\n"
      "v_exp_f32 v161, v161\n"
      "v_mfma_f32_16x16x32_bf16 v[168:171], a[192:195], a[16:19], v[168:171]\n"
      "v_cvt_pk_bf16_f32 v155, v158, v159\n"
      "v_fma_f32 v163, v163, %[csc], v218\n"
      "v_mfma_f32_16x16x32_bf16 v[176:179], a[192:195], a[40:43], v[176:179]\n"
      "s_add_u32 m0, %[m0b], 75776\n"
      "v_exp_f32 v162, v162\n"
      "v_mfma_f32_16x16x32_bf16 v[184:187], a[192:195], a[64:67], v[184:187]\n"
      "buffer_load_dwordx4 %[vv2], s[44:47], s71 offen nt lds\n"
      "v_fma_f32 v164, v164, %[csc], v218\n"
      "v_mfma_f32_16x16x32_bf16 v[172:175], a[196:199], a[16:19], v[172:175]\n"
      "v_exp_f32 v163, v163\n"
      "v_mfma_f32_16x16x32_bf16 v[180:183], a[196:199], a[40:43], v[180:183]\n"
      "v_cvt_pk_bf16_f32 v160, v160, v161\n"
      "v_fma_f32 v165, v165, %[csc], v218\n"
      "v_mfma_f32_16x16x32_bf16 v[188:191], a[196:199], a[64:67], v[188:191]\n"
      "s_waitcnt lgkmcnt(4)\n"
      "v_exp_f32 v164, v164\n"
      "v_mfma_f32_16x16x32_bf16 v[168:171], a[200:203], a[20:23], v[168:171]\n"
      "v_fma_f32 v166, v166, %[csc], v218\n"
      "v_mfma_f32_16x16x32_bf16 v[176:179], a[200:203], a[44:47], v[176:179]\n"
      "v_exp_f32 v165, v165\n"
      "v_mfma_f32_16x16x32_bf16 v[184:187], a[200:203], a[68:71], v[184:187]\n"
      "v_cvt_pk_bf16_f32 v161, v162, v163\n"
      "v_fma_f32 v167, v167, %[csc], v218\n"
      "v_mfma_f32_16x16x32_bf16 v[172:175], a[204:207], a[20:23], v[172:175]\n"
      "v_exp_f32 v166, v166\n"
      "v_mfma_f32_16x16x32_bf16 v[180:183], a[204:207], a[44:47], v[180:183]\n"
      "v_exp_f32 v167, v167\n"
      "v_mfma_f32_16x16x32_bf16 v[188:191], a[204:207], a[68:71], v[188:191]\n"
      "v_cvt_pk_bf16_f32 v162, v164, v165\n"
      "v_cvt_pk_bf16_f32 v163, v166, v167\n"
      "v_mfma_f32_16x16x32_bf16 a[208:211], a[220:223], v[144:147], a[208:211]\n"
      "ds_read_b64_tr_b16 a[156:157], %[vb3] offset:12288\n"
      "ds_read_b64_tr_b16 a[158:159], %[vb3] offset:18432\n"
      "v_mfma_f32_16x16x32_bf16 a[212:215], a[220:223], v[152:155], a[212:215]\n"
      "ds_read_b64_tr_b16 a[160:161], %[vb0] offset:12416\n"
      "ds_read_b64_tr_b16 a[162:163], %[vb0] offset:18560\n"
      "v_mfma_f32_16x16x32_bf16 a[216:219], a[220:223], v[160:163], a[216:219]\n"
      "ds_read_b64_tr_b16 a[164:165], %[vb1] offset:12416\n"
      "ds_read_b64_tr_b16 a[166:167], %[vb1] offset:18560\n"
      "v_mfma_f32_16x16x32_bf16 v[0:3], a[144:147], v[144:147], v[0:3]\n"
      "ds_read_b64_tr_b16 a[168:169], %[vb2] offset:12416\n"
      "ds_read_b64_tr_b16 a[170:171], %[vb2] offset:18560\n"
      "v_mfma_f32_16x16x32_bf16 v[48:51], a[144:147], v[152:155], v[48:51]\n"
      "s_waitcnt lgkmcnt(8)\n"
      "ds_read_b64_tr_b16 a[172:173], %[vb3] offset:12416\n"
      "ds_read_b64_tr_b16 a[174:175], %[vb3] offset:18560\n"
      "v_mfma_f32_16x16x32_bf16 v[96:99], a[144:147], v[160:163], v[96:99]\n"
      "s_add_u32 s72, s69, 18886912\n"
      "ds_read_b64_tr_b16 a[176:177], %[vb0] offset:12544\n"
      "ds_read_b64_tr_b16 a[178:179], %[vb0] offset:18688\n"
      "v_mfma_f32_16x16x32_bf16 v[4:7], a[148:151], v[144:147], v[4:7]\n"
      "s_add_u32 m0, %[m0q], 4096\n"
      "ds_read_b64_tr_b16 a[180:181], %[vb1] offset:12544\n"
      "ds_read_b64_tr_b16 a[182:183], %[vb1] offset:18688\n"
      "v_mfma_f32_16x16x32_bf16 v[52:55], a[148:151], v[152:155], v[52:55]\n"
      "ds_read_b64_tr_b16 a[184:185], %[vb2] offset:12544\n"
      "ds_read_b64_tr_b16 a[186:187], %[vb2] offset:18688\n"
      "v_mfma_f32_16x16x32_bf16 v[100:103], a[148:151], v[160:163], v[100:103]\n"
      "ds_read_b64_tr_b16 a[188:189], %[vb3] offset:12544\n"
      "ds_read_b64_tr_b16 a[190:191], %[vb3] offset:18688\n"
      "v_mfma_f32_16x16x32_bf16 v[8:11], a[152:155], v[144:147], v[8:11]\n"
      "buffer_load_dwordx4 %[vq0], s[64:67], s72 offen nt lds\n"
      "v_fma_f32 v168, v168, %[csc], v216\n"
      "v_mfma_f32_16x16x32_bf16 v[56:59], a[152:155], v[152:155], v[56:59]\n"
      "s_add_u32 m0, %[m0q], 5120\n"
      "v_fma_f32 v169, v169, %[csc], v216\n"
      "v_mfma_f32_16x16x32_bf16 v[104:107], a[152:155], v[160:163], v[104:107]\n"
      "s_waitcnt lgkmcnt(12)\n"
      "buffer_load_dwordx4 %[vq1], s[64:67], s72 offen nt lds\n"
      "v_mfma_f32_16x16x32_bf16 v[12:15], a[156:159], v[144:147], v[12:15]\n"
      "v_exp_f32 v168, v168\n"
      "v_mfma_f32_16x16x32_bf16 v[60:63], a[156:159], v[152:155], v[60:63]\n"
      "v_fma_f32 v170, v170, %[csc], v216\n"
      "s_waitcnt vmcnt(22) lgkmcnt(0)\n"
      "s_barrier\n"
      "v_mfma_f32_16x16x32_bf16 v[108:111], a[156:159], v[160:163], v[108:111]\n"
      "s_add_u32 s70, s68, 18874368\n"
      "ds_read_b128 a[192:195], %[kb00] offset:12288\n"
      "ds_read_b128 a[196:199], %[kb00] offset:18432\n"
      "v_mfma_f32_16x16x32_bf16 v[16:19], a[160:163], v[144:147], v[16:19]\n"
      "s_add_u32 m0, %[m0b], 0\n"
      "ds_read_b128 a[200:203], %[kb10] offset:12288\n"
      "ds_read_b128 a[204:207], %[kb10] offset:18432\n"
      "v_mfma_f32_16x16x32_bf16 v[64:67], a[160:163], v[152:155], v[64:67]\n"
      "buffer_load_dwordx4 %[vk0], s[56:59], s70 offen nt lds\n"
      "ds_read_b128 a[144:147], %[kb00] offset:12416\n"
      "v_mfma_f32_16x16x32_bf16 v[112:115], a[160:163], v[160:163], v[112:115]\n"
      "ds_read_b128 a[148:151], %[kb00] offset:18560\n"
      "ds_read_b128 a[152:155], %[kb10] offset:12416\n"
      "v_mfma_f32_16x16x32_bf16 v[20:23], a[164:167], v[144:147], v[20:23]\n"
      "ds_read_b128 a[156:159], %[kb10] offset:18560\n"
      "ds_read_b128 a[160:163], %[kb00] offset:12544\n"
      "v_mfma_f32_16x16x32_bf16 v[68:71], a[164:167], v[152:155], v[68:71]\n"
      "ds_write_b64 %[ow], a[88:89] offset:0\n"
      "v_mfma_f32_16x16x32_bf16 v[116:119], a[164:167], v[160:163], v[116:119]\n"
      "ds_read_b128 a[164:167], %[kb00] offset:18688\n"
      "ds_write_b64 %[ow], a[90:91] offset:32\n"
      "v_mfma_f32_16x16x32_bf16 v[24:27], a[168:171], v[144:147], v[24:27]\n"
      "ds_write_b64 %[ow], a[92:93] offset:64\n"
      "v_mfma_f32_16x16x32_bf16 v[72:75], a[168:171], v[152:155], v[72:75]\n"
      "v_exp_f32 v169, v169\n"
      "v_mfma_f32_16x16x32_bf16 v[120:123], a[168:171], v[160:163], v[120:123]\n"
      "ds_read_b128 a[168:171], %[kb10] offset:12544\n"
      "ds_write_b64 %[ow], a[94:95] offset:96\n"
      "v_mfma_f32_16x16x32_bf16 v[28:31], a[172:175], v[144:147], v[28:31]\n"
      "s_add_u32 m0, %[m0b], 1024\n"
      "v_fma_f32 v171, v171, %[csc], v216\n"
      "v_mfma_f32_16x16x32_bf16 v[76:79], a[172:175], v[152:155], v[76:79]\n"
      "buffer_load_dwordx4 %[vk1], s[56:59], s70 offen nt lds\n"
      "v_mfma_f32_16x16x32_bf16 v[124:127], a[172:175], v[160:163], v[124:127]\n"
      "ds_read_b128 a[172:175], %[kb10] offset:18688\n"
      "v_mfma_f32_16x16x32_bf16 v[32:35], a[176:179], v[144:147], v[32:35]\n"
      "v_exp_f32 v170, v170\n"
      "v_mfma_f32_16x16x32_bf16 v[80:83], a[176:179], v[152:155], v[80:83]\n"
      "ds_read_b128 a[224:227], %[orb] offset:0\n"
      "v_fma_f32 v172, v172, %[csc], v216\n"
      "v_mfma_f32_16x16x32_bf16 v[128:131], a[176:179], v[160:163], v[128:131]\n"
      "ds_read_b64_tr_b16 a[176:177], %[vb0] offset:24576\n"
      "ds_read_b64_tr_b16 a[178:179], %[vb0] offset:30720\n"
      "v_mfma_f32_16x16x32_bf16 v[36:39], a[180:183], v[144:147], v[36:39]\n"
      "v_exp_f32 v171, v171\n"
      "v_mfma_f32_16x16x32_bf16 v[84:87], a[180:183], v[152:155], v[84:87]\n"
      "v_cvt_pk_bf16_f32 v168, v168, v169\n"
      "v_fma_f32 v173, v173, %[csc], v216\n"
      "v_mfma_f32_16x16x32_bf16 v[132:135], a[180:183], v[160:163], v[132:135]\n"
      "ds_read_b64_tr_b16 a[180:181], %[vb1] offset:24576\n"
      "ds_read_b64_tr_b16 a[182:183], %[vb1] offset:30720\n"
      "v_mfma_f32_16x16x32_bf16 v[40:43], a[184:187], v[144:147], v[40:43]\n"
      "v_exp_f32 v172, v172\n"
      "v_mfma_f32_16x16x32_bf16 v[88:91], a[184:187], v[152:155], v[88:91]\n"
      "s_add_u32 m0, %[m0b], 2048\n"
      "ds_read_b128 a[228:231], %[orb] offset:1152\n"
      "v_fma_f32 v174, v174, %[csc], v216\n"
      "v_mfma_f32_16x16x32_bf16 v[136:139], a[184:187], v[160:163], v[136:139]\n"
      "buffer_load_dwordx4 %[vk2], s[56:59], s70 offen nt lds\n"
      "v_mfma_f32_16x16x32_bf16 v[44:47], a[188:191], v[144:147], v[44:47]\n"
      "ds_read_b64_tr_b16 a[184:185], %[vb2] offset:24576\n"
      "ds_read_b64_tr_b16 a[186:187], %[vb2] offset:30720\n"
      "v_mfma_f32_16x16x32_bf16 v[92:95], a[188:191], v[152:155], v[92:95]\n"
      "v_exp_f32 v173, v173\n"
      "v_mfma_f32_16x16x32_bf16 v[140:143], a[188:191], v[160:163], v[140:143]\n"
      "s_waitcnt lgkmcnt(15)\n"
      "v_cvt_pk_bf16_f32 v169, v170, v171\n"
      "v_fma_f32 v175, v175, %[csc], v216\n"
      "v_mfma_f32_16x16x32_bf16 v[144:147], a[192:195], a[0:3], 0\n"
      "v_exp_f32 v174, v174\n"
      "v_mfma_f32_16x16x32_bf16 v[152:155], a[192:195], a[24:27], 0\n"
      "s_waitcnt lgkmcnt(7)\n"
      "v_fma_f32 v176, v176, %[csc], v217\n"
      "v_mfma_f32_16x16x32_bf16 v[160:163], a[192:195], a[48:51], 0\n"
      "s_add_u32 s73, s69, 4276092928\n"
      "buffer_store_dwordx4 a[224:227], %[ost], s[84:87], s73 offen offset:256 sc1\n"
      "v_mfma_f32_16x16x32_bf16 v[148:151], a[196:199], a[0:3], 0\n"
      "v_exp_f32 v175, v175\n"
      "v_mfma_f32_16x16x32_bf16 v[156:159], a[196:199], a[24:27], 0\n"
      "v_cvt_pk_bf16_f32 v170, v172, v173\n"
      "v_fma_f32 v177, v177, %[csc], v217\n"
      "v_mfma_f32_16x16x32_bf16 v[164:167], a[196:199], a[48:51], 0\n"
      "s_add_u32 s71, s68, 135168\n"
      "v_exp_f32 v176, v176\n"
      "v_mfma_f32_16x16x32_bf16 v[144:147], a[200:203], a[4:7], v[144:147]\n"
      "s_add_u32 m0, %[m0b], 86016\n"
      "v_fma_f32 v178, v178, %[csc], v217\n"
      "v_mfma_f32_16x16x32_bf16 v[152:155], a[200:203], a[28:31], v[152:155]\n"
      "buffer_load_dwordx4 %[vv0], s[44:47], s71 offen nt lds\n"
      "v_mfma_f32_16x16x32_bf16 v[160:163], a[200:203], a[52:55], v[160:163]\n"
      "v_exp_f32 v177, v177\n"
      "v_mfma_f32_16x16x32_bf16 v[148:151], a[204:207], a[4:7], v[148:151]\n"
      "v_cvt_pk_bf16_f32 v171, v174, v175\n"
      "v_fma_f32 v179, v179, %[csc], v217\n"
      "v_mfma_f32_16x16x32_bf16 v[156:159], a[204:207], a[28:31], v[156:159]\n"
      "v_exp_f32 v178, v178\n"
      "v_mfma_f32_16x16x32_bf16 v[164:167], a[204:207], a[52:55], v[164:167]\n"
      "v_fma_f32 v180, v180, %[csc], v217\n"
      "v_mfma_f32_16x16x32_bf16 v[144:147], a[144:147], a[8:11], v[144:147]\n"
      "v_exp_f32 v179, v179\n"
      "v_mfma_f32_16x16x32_bf16 v[152:155], a[144:147], a[32:35], v[152:155]\n"
      "s_waitcnt lgkmcnt(2)\n"
      "v_cvt_pk_bf16_f32 v176, v176, v177\n"
      "v_fma_f32 v181, v181, %[csc], v217\n"
      "v_mfma_f32_16x16x32_bf16 v[160:163], a[144:147], a[56:59], v[160:163]\n"
      "s_add_u32 s73, s69, 4276096000\n"
      "buffer_store_dwordx4 a[228:231], %[ost], s[84:87], s73 offen offset:256 sc1\n"
      "v_mfma_f32_16x16x32_bf16 v[148:151], a[148:151], a[8:11], v[148:151]\n"
      "s_add_u32 m0, %[m0b], 87040\n"
      "v_exp_f32 v180, v180\n"
      "v_mfma_f32_16x16x32_bf16 v[156:159], a[148:151], a[32:35], v[156:159]\n"
      "buffer_load_dwordx4 %[vv1], s[44:47], s71 offen nt lds\n"
      "v_fma_f32 v182, v182, %[csc], v217\n"
      "v_mfma_f32_16x16x32_bf16 v[164:167], a[148:151], a[56:59], v[164:167]\n"
      "v_exp_f32 v181, v181\n"
      "v_mfma_f32_16x16x32_bf16 v[144:147], a[152:155], a[12:15], v[144:147]\n"
      "v_cvt_pk_bf16_f32 v177, v178, v179\n"
      "v_fma_f32 v183, v183, %[csc], v217\n"
      "v_mfma_f32_16x16x32_bf16 v[152:155], a[152:155], a[36:39], v[152:155]\n"
      "v_exp_f32 v182, v182\n"
      "v_mfma_f32_16x16x32_bf16 v[160:163], a[152:155], a[60:63], v[160:163]\n"
      "v_fma_f32 v184, v184, %[csc], v218\n"
      "v_mfma_f32_16x16x32_bf16 v[148:151], a[156:159], a[12:15], v[148:151]\n"
      "v_exp_f32 v183, v183\n"
      "v_mfma_f32_16x16x32_bf16 v[156:159], a[156:159], a[36:39], v[156:159]\n"
      "v_cvt_pk_bf16_f32 v178, v180, v181\n"
      "v_fma_f32 v185, v185, %[csc], v218\n"
      "v_mfma_f32_16x16x32_bf16 v[164:167], a[156:159], a[60:63], v[164:167]\n"
      "v_exp_f32 v184, v184\n"
      "v_mfma_f32_16x16x32_bf16 v[144:147], a[160:163], a[16:19], v[144:147]\n"
      "v_fma_f32 v186, v186, %[csc], v218\n"
      "v_mfma_f32_16x16x32_bf16 v[152:155], a[160:163], a[40:43], v[152:155]\n"
      "s_add_u32 m0, %[m0b], 88064\n"
      "v_exp_f32 v185, v185\n"
      "v_mfma_f32_16x16x32_bf16 v[160:163], a[160:163], a[64:67], v[160:163]\n"
      "s_waitcnt vmcnt(27)\n"
      "v_cvt_pk_bf16_f32 v179, v182, v183\n"
      "v_fma_f32 v187, v187, %[csc], v218\n"
      "v_mfma_f32_16x16x32_bf16 v[148:151], a[164:167], a[16:19], v[148:151]\n"
      "v_exp_f32 v186, v186\n"
      "v_mfma_f32_16x16x32_bf16 v[156:159], a[164:167], a[40:43], v[156:159]\n"
      "buffer_load_dwordx4 %[vv2], s[44:47], s71 offen nt lds\n"
      "v_fma_f32 v188, v188, %[csc], v218\n"
      "v_mfma_f32_16x16x32_bf16 v[164:167], a[164:167], a[64:67], v[164:167]\n"
      "v_exp_f32 v187, v187\n"
      "v_mfma_f32_16x16x32_bf16 v[144:147], a[168:171], a[20:23], v[144:147]\n"
      "v_cvt_pk_bf16_f32 v184, v184, v185\n"
      "v_fma_f32 v189, v189, %[csc], v218\n"
      "v_mfma_f32_16x16x32_bf16 v[152:155], a[168:171], a[44:47], v[152:155]\n"
      "v_exp_f32 v188, v188\n"
      "v_mfma_f32_16x16x32_bf16 v[160:163], a[168:171], a[68:71], v[160:163]\n"
      "ds_read_b128 a[88:91], %[qs0] offset:0\n"
      "v_fma_f32 v190, v190, %[csc], v218\n"
      "v_mfma_f32_16x16x32_bf16 v[148:151], a[172:175], a[20:23], v[148:151]\n"
      "v_exp_f32 v189, v189\n"
      "v_mfma_f32_16x16x32_bf16 v[156:159], a[172:175], a[44:47], v[156:159]\n"
      "v_cvt_pk_bf16_f32 v185, v186, v187\n"
      "v_fma_f32 v191, v191, %[csc], v218\n"
      "v_mfma_f32_16x16x32_bf16 v[164:167], a[172:175], a[68:71], v[164:167]\n"
      "v_exp_f32 v190, v190\n"
      "v_exp_f32 v191, v191\n"
      "v_cvt_pk_bf16_f32 v186, v188, v189\n"
      "v_cvt_pk_bf16_f32 v187, v190, v191\n"
      "v_mfma_f32_16x16x32_bf16 a[208:211], a[220:223], v[168:171], a[208:211]\n"
      "ds_read_b64_tr_b16 a[188:189], %[vb3] offset:24576\n"
      "ds_read_b64_tr_b16 a[190:191], %[vb3] offset:30720\n"
      "v_mfma_f32_16x16x32_bf16 a[212:215], a[220:223], v[176:179], a[212:215]\n"
      "ds_read_b64_tr_b16 a[192:193], %[vb0] offset:24704\n"
      "ds_read_b64_tr_b16 a[194:195], %[vb0] offset:30848\n"
      "v_mfma_f32_16x16x32_bf16 a[216:219], a[220:223], v[184:187], a[216:219]\n"
      "ds_read_b64_tr_b16 a[196:197], %[vb1] offset:24704\n"
      "ds_read_b64_tr_b16 a[198:199], %[vb1] offset:30848\n"
      "v_mfma_f32_16x16x32_bf16 v[0:3], a[176:179], v[168:171], v[0:3]\n"
      "ds_read_b64_tr_b16 a[200:201], %[vb2] offset:24704\n"
      "ds_read_b64_tr_b16 a[202:203], %[vb2] offset:30848\n"
      "v_mfma_f32_16x16x32_bf16 v[48:51], a[176:179], v[176:179], v[48:51]\n"
      "ds_read_b128 a[92:95], %[qs1] offset:0\n"
      "v_fma_f32 v144, v144, %[csc], v216\n"
      "v_mfma_f32_16x16x32_bf16 v[96:99], a[176:179], v[184:187], v[96:99]\n"
      "ds_read_b64_tr_b16 a[204:205], %[vb3] offset:24704\n"
      "ds_read_b64_tr_b16 a[206:207], %[vb3] offset:30848\n"
      "v_mfma_f32_16x16x32_bf16 v[4:7], a[180:183], v[168:171], v[4:7]\n"
      "ds_read_b64_tr_b16 a[144:145], %[vb0] offset:24832\n"
      "ds_read_b64_tr_b16 a[146:147], %[vb0] offset:30976\n"
      "v_mfma_f32_16x16x32_bf16 v[52:55], a[180:183], v[176:179], v[52:55]\n"
      "ds_read_b64_tr_b16 a[148:149], %[vb1] offset:24832\n"
      "ds_read_b64_tr_b16 a[150:151], %[vb1] offset:30976\n"
      "v_mfma_f32_16x16x32_bf16 v[100:103], a[180:183], v[184:187], v[100:103]\n"
      "s_waitcnt lgkmcnt(15)\n"
      "ds_read_b64_tr_b16 a[152:153], %[vb2] offset:24832\n"
      "ds_read_b64_tr_b16 a[154:155], %[vb2] offset:30976\n"
      "v_mfma_f32_16x16x32_bf16 v[8:11], a[184:187], v[168:171], v[8:11]\n"
      "ds_read_b64_tr_b16 a[156:157], %[vb3] offset:24832\n"
      "ds_read_b64_tr_b16 a[158:159], %[vb3] offset:30976\n"
      "v_mfma_f32_16x16x32_bf16 v[56:59], a[184:187], v[176:179], v[56:59]\n"
      "v_fma_f32 v145, v145, %[csc], v216\n"
      "v_mfma_f32_16x16x32_bf16 v[104:107], a[184:187], v[184:187], v[104:107]\n"
      "s_waitcnt lgkmcnt(13)\n"
      "v_exp_f32 v144, v144\n"
      "v_mfma_f32_16x16x32_bf16 v[12:15], a[188:191], v[168:171], v[12:15]\n"
      "v_fma_f32 v146, v146, %[csc], v216\n"
      "v_mfma_f32_16x16x32_bf16 v[60:63], a[188:191], v[176:179], v[60:63]\n"
      "v_exp_f32 v145, v145\n"
      "v_mfma_f32_16x16x32_bf16 v[108:111], a[188:191], v[184:187], v[108:111]\n"
      "v_fma_f32 v147, v147, %[csc], v216\n"
      "s_waitcnt vmcnt(20) lgkmcnt(0)\n"
      "s_barrier\n"
      "v_mfma_f32_16x16x32_bf16 v[16:19], a[192:195], v[168:171], v[16:19]\n"
      "s_add_u32 s70, s68, 18886656\n"
      "ds_read_b128 a[160:163], %[kb00] offset:24576\n"
      "ds_read_b128 a[164:167], %[kb00] offset:30720\n"
      "v_mfma_f32_16x16x32_bf16 v[64:67], a[192:195], v[176:179], v[64:67]\n"
      "s_add_u32 m0, %[m0b], 12288\n"
      "ds_read_b128 a[168:171], %[kb10] offset:24576\n"
      "ds_read_b128 a[172:175], %[kb10] offset:30720\n"
      "v_mfma_f32_16x16x32_bf16 v[112:115], a[192:195], v[184:187], v[112:115]\n"
      "buffer_load_dwordx4 %[vk0], s[56:59], s70 offen nt lds\n"
      "ds_read_b128 a[176:179], %[kb00] offset:24704\n"
      "v_mfma_f32_16x16x32_bf16 v[20:23], a[196:199], v[168:171], v[20:23]\n"
      "ds_read_b128 a[180:183], %[kb00] offset:30848\n"
      "ds_read_b128 a[184:187], %[kb10] offset:24704\n"
      "v_mfma_f32_16x16x32_bf16 v[68:71], a[196:199], v[176:179], v[68:71]\n"
      "ds_read_b128 a[188:191], %[kb10] offset:30848\n"
      "ds_read_b128 a[192:195], %[kb00] offset:24832\n"
      "v_mfma_f32_16x16x32_bf16 v[116:119], a[196:199], v[184:187], v[116:119]\n"
      "ds_read_b128 a[196:199], %[kb00] offset:30976\n"
      "v_mfma_f32_16x16x32_bf16 v[24:27], a[200:203], v[168:171], v[24:27]\n"
      "v_exp_f32 v146, v146\n"
      "v_mfma_f32_16x16x32_bf16 v[72:75], a[200:203], v[176:179], v[72:75]\n"
      "v_fma_f32 v148, v148, %[csc], v216\n"
      "v_mfma_f32_16x16x32_bf16 v[120:123], a[200:203], v[184:187], v[120:123]\n"
      "ds_read_b128 a[200:203], %[kb10] offset:24832\n"
      "v_mfma_f32_16x16x32_bf16 v[28:31], a[204:207], v[168:171], v[28:31]\n"
      "ds_write_b64 %[ow], a[112:113] offset:0\n"
      "v_mfma_f32_16x16x32_bf16 v[76:79], a[204:207], v[176:179], v[76:79]\n"
      "s_add_u32 m0, %[m0b], 13312\n"
      "v_exp_f32 v147, v147\n"
      "v_mfma_f32_16x16x32_bf16 v[124:127], a[204:207], v[184:187], v[124:127]\n"
      "buffer_load_dwordx4 %[vk1], s[56:59], s70 offen nt lds\n"
      "ds_read_b128 a[204:207], %[kb10] offset:30976\n"
      "v_mfma_f32_16x16x32_bf16 v[32:35], a[144:147], v[168:171], v[32:35]\n"
      "ds_write_b64 %[ow], a[114:115] offset:32\n"
      "v_cvt_pk_bf16_f32 v144, v144, v145\n"
      "v_mfma_f32_16x16x32_bf16 v[80:83], a[144:147], v[176:179], v[80:83]\n"
      "v_fma_f32 v149, v149, %[csc], v216\n"
      "v_mfma_f32_16x16x32_bf16 v[128:131], a[144:147], v[184:187], v[128:131]\n"
      "ds_read_b64_tr_b16 a[144:145], %[vb0] offset:36864\n"
      "ds_read_b64_tr_b16 a[146:147], %[vb0] offset:43008\n"
      "v_mfma_f32_16x16x32_bf16 v[36:39], a[148:151], v[168:171], v[36:39]\n"
      "ds_write_b64 %[ow], a[116:117] offset:64\n"
      "v_mfma_f32_16x16x32_bf16 v[84:87], a[148:151], v[176:179], v[84:87]\n"
      "v_exp_f32 v148, v148\n"
      "v_mfma_f32_16x16x32_bf16 v[132:135], a[148:151], v[184:187], v[132:135]\n"
      "ds_read_b64_tr_b16 a[148:149], %[vb1] offset:36864\n"
      "ds_read_b64_tr_b16 a[150:151], %[vb1] offset:43008\n"
      "v_mfma_f32_16x16x32_bf16 v[40:43], a[152:155], v[168:171], v[40:43]\n"
      "ds_write_b64 %[ow], a[118:119] offset:96\n"
      "v_fma_f32 v150, v150, %[csc], v216\n"
      "v_mfma_f32_16x16x32_bf16 v[88:91], a[152:155], v[176:179], v[88:91]\n"
      "v_exp_f32 v149, v149\n"
      "v_mfma_f32_16x16x32_bf16 v[136:139], a[152:155], v[184:187], v[136:139]\n"
      "s_add_u32 m0, %[m0b], 14336\n"
      "ds_read_b64_tr_b16 a[152:153], %[vb2] offset:36864\n"
      "ds_read_b64_tr_b16 a[154:155], %[vb2] offset:43008\n"
      "v_mfma_f32_16x16x32_bf16 v[44:47], a[156:159], v[168:171], v[44:47]\n"
      "buffer_load_dwordx4 %[vk2], s[56:59], s70 offen nt lds\n"
      "v_cvt_pk_bf16_f32 v145, v146, v147\n"
      "v_mfma_f32_16x16x32_bf16 v[92:95], a[156:159], v[176:179], v[92:95]\n"
      "v_fma_f32 v151, v151, %[csc], v216\n"
      "v_mfma_f32_16x16x32_bf16 v[140:143], a[156:159], v[184:187], v[140:143]\n"
      "s_waitcnt lgkmcnt(15)\n"
      "v_exp_f32 v150, v150\n"
      "v_mfma_f32_16x16x32_bf16 v[168:171], a[160:163], a[0:3], 0\n"
      "ds_read_b128 a[232:235], %[orb] offset:0\n"
      "v_fma_f32 v152, v152, %[csc], v217\n"
      "v_mfma_f32_16x16x32_bf16 v[176:179], a[160:163], a[24:27], 0\n"
      "v_exp_f32 v151, v151\n"
      "v_mfma_f32_16x16x32_bf16 v[184:187], a[160:163], a[48:51], 0\n"
      "v_cvt_pk_bf16_f32 v146, v148, v149\n"
      "v_fma_f32 v153, v153, %[csc], v217\n"
      "v_mfma_f32_16x16x32_bf16 v[172:175], a[164:167], a[0:3], 0\n"
      "v_exp_f32 v152, v152\n"
      "v_mfma_f32_16x16x32_bf16 v[180:183], a[164:167], a[24:27], 0\n"
      "ds_read_b128 a[236:239], %[orb] offset:1152\n"
      "v_fma_f32 v154, v154, %[csc], v217\n"
      "v_mfma_f32_16x16x32_bf16 v[188:191], a[164:167], a[48:51], 0\n"
      "v_exp_f32 v153, v153\n"
      "v_mfma_f32_16x16x32_bf16 v[168:171], a[168:171], a[4:7], v[168:171]\n"
      "s_add_u32 s71, s68, 18874368\n"
      "v_cvt_pk_bf16_f32 v147, v150, v151\n"
      "v_fma_f32 v155, v155, %[csc], v217\n"
      "v_mfma_f32_16x16x32_bf16 v[176:179], a[168:171], a[28:31], v[176:179]\n"
      "s_add_u32 m0, %[m0b], 98304\n"
      "v_exp_f32 v154, v154\n"
      "v_mfma_f32_16x16x32_bf16 v[184:187], a[168:171], a[52:55], v[184:187]\n"
      "buffer_load_dwordx4 %[vv0], s[60:63], s71 offen nt lds\n"
      "v_fma_f32 v156, v156, %[csc], v217\n"
      "v_mfma_f32_16x16x32_bf16 v[172:175], a[172:175], a[4:7], v[172:175]\n"
      "v_exp_f32 v155, v155\n"
      "v_mfma_f32_16x16x32_bf16 v[180:183], a[172:175], a[28:31], v[180:183]\n"
      "v_cvt_pk_bf16_f32 v152, v152, v153\n"
      "v_fma_f32 v157, v157, %[csc], v217\n"
      "v_mfma_f32_16x16x32_bf16 v[188:191], a[172:175], a[52:55], v[188:191]\n"
      "s_waitcnt lgkmcnt(1)\n"
      "v_exp_f32 v156, v156\n"
      "v_mfma_f32_16x16x32_bf16 v[168:171], a[176:179], a[8:11], v[168:171]\n"
      "s_add_u32 s73, s69, 4276099072\n"
      "buffer_store_dwordx4 a[232:235], %[ost], s[84:87], s73 offen offset:256 sc1\n"
      "v_fma_f32 v158, v158, %[csc], v217\n"
      "v_mfma_f32_16x16x32_bf16 v[176:179], a[176:179], a[32:35], v[176:179]\n"
      "v_exp_f32 v157, v157\n"
      "v_mfma_f32_16x16x32_bf16 v[184:187], a[176:179], a[56:59], v[184:187]\n"
      "v_cvt_pk_bf16_f32 v153, v154, v155\n"
      "v_fma_f32 v159, v159, %[csc], v217\n"
      "v_mfma_f32_16x16x32_bf16 v[172:175], a[180:183], a[8:11], v[172:175]\n"
      "v_exp_f32 v158, v158\n"
      "v_mfma_f32_16x16x32_bf16 v[180:183], a[180:183], a[32:35], v[180:183]\n"
      "s_add_u32 m0, %[m0b], 99328\n"
      "v_fma_f32 v160, v160, %[csc], v218\n"
      "v_mfma_f32_16x16x32_bf16 v[188:191], a[180:183], a[56:59], v[188:191]\n"
      "buffer_load_dwordx4 %[vv1], s[60:63], s71 offen nt lds\n"
      "v_mfma_f32_16x16x32_bf16 v[168:171], a[184:187], a[12:15], v[168:171]\n"
      "v_exp_f32 v159, v159\n"
      "v_mfma_f32_16x16x32_bf16 v[176:179], a[184:187], a[36:39], v[176:179]\n"
      "v_cvt_pk_bf16_f32 v154, v156, v157\n"
      "v_fma_f32 v161, v161, %[csc], v218\n"
      "v_mfma_f32_16x16x32_bf16 v[184:187], a[184:187], a[60:63], v[184:187]\n"
      "v_exp_f32 v160, v160\n"
      "v_mfma_f32_16x16x32_bf16 v[172:175], a[188:191], a[12:15], v[172:175]\n"
      "v_fma_f32 v162, v162, %[csc], v218\n"
      "v_mfma_f32_16x16x32_bf16 v[180:183], a[188:191], a[36:39], v[180:183]\n"
      "v_exp_f32 v161, v161\n"
      "v_mfma_f32_16x16x32_bf16 v[188:191], a[188:191], a[60:63], v[188:191]\n"
      "s_waitcnt lgkmcnt(0)\n"
      "v_cvt_pk_bf16_f32 v155, v158, v159\n"
      "v_fma_f32 v163, v163, %[csc], v218\n"
      "v_mfma_f32_16x16x32_bf16 v[168:171], a[192:195], a[16:19], v[168:171]\n"
      "s_add_u32 s73, s69, 4276102144\n"
      "v_exp_f32 v162, v162\n"
      "v_mfma_f32_16x16x32_bf16 v[176:179], a[192:195], a[40:43], v[176:179]\n"
      "buffer_store_dwordx4 a[236:239], %[ost], s[84:87], s73 offen offset:256 sc1\n"
      "v_fma_f32 v164, v164, %[csc], v218\n"
      "v_mfma_f32_16x16x32_bf16 v[184:187], a[192:195], a[64:67], v[184:187]\n"
      "s_add_u32 m0, %[m0b], 100352\n"
      "v_exp_f32 v163, v163\n"
      "v_mfma_f32_16x16x32_bf16 v[172:175], a[196:199], a[16:19], v[172:175]\n"
      "v_cvt_pk_bf16_f32 v160, v160, v161\n"
      "v_fma_f32 v165, v165, %[csc], v218\n"
      "v_mfma_f32_16x16x32_bf16 v[180:183], a[196:199], a[40:43], v[180:183]\n"
      "v_exp_f32 v164, v164\n"
      "v_mfma_f32_16x16x32_bf16 v[188:191], a[196:199], a[64:67], v[188:191]\n"
      "buffer_load_dwordx4 %[vv2], s[60:63], s71 offen nt lds\n"
      "v_fma_f32 v166, v166, %[csc], v218\n"
      "v_mfma_f32_16x16x32_bf16 v[168:171], a[200:203], a[20:23], v[168:171]\n"
      "v_exp_f32 v165, v165\n"
      "v_mfma_f32_16x16x32_bf16 v[176:179], a[200:203], a[44:47], v[176:179]\n"
      "v_cvt_pk_bf16_f32 v161, v162, v163\n"
      "v_fma_f32 v167, v167, %[csc], v218\n"
      "v_mfma_f32_16x16x32_bf16 v[184:187], a[200:203], a[68:71], v[184:187]\n"
      "v_exp_f32 v166, v166\n"
      "v_mfma_f32_16x16x32_bf16 v[172:175], a[204:207], a[20:23], v[172:175]\n"
      "v_exp_f32 v167, v167\n"
      "v_mfma_f32_16x16x32_bf16 v[180:183], a[204:207], a[44:47], v[180:183]\n"
      "v_cvt_pk_bf16_f32 v162, v164, v165\n"
      "v_mfma_f32_16x16x32_bf16 v[188:191], a[204:207], a[68:71], v[188:191]\n"
      "s_waitcnt vmcnt(26)\n"
      "ds_read_b128 a[112:115], %[qs0] offset:2048\n"
      "v_cvt_pk_bf16_f32 v163, v166, v167\n"
      "v_mfma_f32_16x16x32_bf16 a[208:211], a[220:223], v[144:147], a[208:211]\n"
      "ds_read_b64_tr_b16 a[156:157], %[vb3] offset:36864\n"
      "ds_read_b64_tr_b16 a[158:159], %[vb3] offset:43008\n"
      "v_mfma_f32_16x16x32_bf16 a[212:215], a[220:223], v[152:155], a[212:215]\n"
      "ds_read_b64_tr_b16 a[160:161], %[vb0] offset:36992\n"
      "ds_read_b64_tr_b16 a[162:163], %[vb0] offset:43136\n"
      "v_mfma_f32_16x16x32_bf16 a[216:219], a[220:223], v[160:163], a[216:219]\n"
      "ds_read_b64_tr_b16 a[164:165], %[vb1] offset:36992\n"
      "ds_read_b64_tr_b16 a[166:167], %[vb1] offset:43136\n"
      "v_mfma_f32_16x16x32_bf16 v[0:3], a[144:147], v[144:147], v[0:3]\n"
      "ds_read_b64_tr_b16 a[168:169], %[vb2] offset:36992\n"
      "ds_read_b64_tr_b16 a[170:171], %[vb2] offset:43136\n"
      "v_mfma_f32_16x16x32_bf16 v[48:51], a[144:147], v[152:155], v[48:51]\n"
      "ds_read_b64_tr_b16 a[172:173], %[vb3] offset:36992\n"
      "ds_read_b64_tr_b16 a[174:175], %[vb3] offset:43136\n"
      "v_mfma_f32_16x16x32_bf16 v[96:99], a[144:147], v[160:163], v[96:99]\n"
      "ds_read_b64_tr_b16 a[176:177], %[vb0] offset:37120\n"
      "ds_read_b64_tr_b16 a[178:179], %[vb0] offset:43264\n"
      "v_mfma_f32_16x16x32_bf16 v[4:7], a[148:151], v[144:147], v[4:7]\n"
      "ds_read_b64_tr_b16 a[180:181], %[vb1] offset:37120\n"
      "ds_read_b64_tr_b16 a[182:183], %[vb1] offset:43264\n"
      "v_mfma_f32_16x16x32_bf16 v[52:55], a[148:151], v[152:155], v[52:55]\n"
      "ds_read_b128 a[116:119], %[qs1] offset:2048\n"
      "v_fma_f32 v168, v168, %[csc], v216\n"
      "v_mfma_f32_16x16x32_bf16 v[100:103], a[148:151], v[160:163], v[100:103]\n"
      "ds_read_b64_tr_b16 a[184:185], %[vb2] offset:37120\n"
      "ds_read_b64_tr_b16 a[186:187], %[vb2] offset:43264\n"
      "v_mfma_f32_16x16x32_bf16 v[8:11], a[152:155], v[144:147], v[8:11]\n"
      "ds_read_b64_tr_b16 a[188:189], %[vb3] offset:37120\n"
      "ds_read_b64_tr_b16 a[190:191], %[vb3] offset:43264\n"
      "v_mfma_f32_16x16x32_bf16 v[56:59], a[152:155], v[152:155], v[56:59]\n"
      "v_fma_f32 v169, v169, %[csc], v216\n"
      "v_mfma_f32_16x16x32_bf16 v[104:107], a[152:155], v[160:163], v[104:107]\n"
      "s_waitcnt lgkmcnt(13)\n"
      "v_exp_f32 v168, v168\n"
      "v_mfma_f32_16x16x32_bf16 v[12:15], a[156:159], v[144:147], v[12:15]\n"
      "v_fma_f32 v170, v170, %[csc], v216\n"
      "v_mfma_f32_16x16x32_bf16 v[60:63], a[156:159], v[152:155], v[60:63]\n"
      "v_exp_f32 v169, v169\n"
      "v_mfma_f32_16x16x32_bf16 v[108:111], a[156:159], v[160:163], v[108:111]\n"
      "v_fma_f32 v171, v171, %[csc], v216\n"
      "s_waitcnt vmcnt(18) lgkmcnt(0)\n"
      "s_barrier\n"
      "v_mfma_f32_16x16x32_bf16 v[16:19], a[160:163], v[144:147], v[16:19]\n"
      "s_add_u32 s70, s68, 18898944\n"
      "ds_read_b128 a[192:195], %[kb01] offset:0\n"
      "ds_read_b128 a[196:199], %[kb01] offset:6144\n"
      "v_mfma_f32_16x16x32_bf16 v[64:67], a[160:163], v[152:155], v[64:67]\n"
      "s_add_u32 m0, %[m0b], 24576\n"
      "ds_read_b128 a[200:203], %[kb11] offset:0\n"
      "ds_read_b128 a[204:207], %[kb11] offset:6144\n"
      "v_mfma_f32_16x16x32_bf16 v[112:115], a[160:163], v[160:163], v[112:115]\n"
      "buffer_load_dwordx4 %[vk0], s[56:59], s70 offen nt lds\n"
      "ds_read_b128 a[144:147], %[kb01] offset:128\n"
      "v_mfma_f32_16x16x32_bf16 v[20:23], a[164:167], v[144:147], v[20:23]\n"
      "ds_read_b128 a[148:151], %[kb01] offset:6272\n"
      "ds_read_b128 a[152:155], %[kb11] offset:128\n"
      "v_mfma_f32_16x16x32_bf16 v[68:71], a[164:167], v[152:155], v[68:71]\n"
      "ds_read_b128 a[156:159], %[kb11] offset:6272\n"
      "ds_read_b128 a[160:163], %[kb01] offset:256\n"
      "v_mfma_f32_16x16x32_bf16 v[116:119], a[164:167], v[160:163], v[116:119]\n"
      "ds_read_b128 a[164:167], %[kb01] offset:6400\n"
      "v_mfma_f32_16x16x32_bf16 v[24:27], a[168:171], v[144:147], v[24:27]\n"
      "v_exp_f32 v170, v170\n"
      "v_mfma_f32_16x16x32_bf16 v[72:75], a[168:171], v[152:155], v[72:75]\n"
      "v_fma_f32 v172, v172, %[csc], v216\n"
      "v_mfma_f32_16x16x32_bf16 v[120:123], a[168:171], v[160:163], v[120:123]\n"
      "ds_read_b128 a[168:171], %[kb11] offset:256\n"
      "v_mfma_f32_16x16x32_bf16 v[28:31], a[172:175], v[144:147], v[28:31]\n"
      "v_exp_f32 v171, v171\n"
      "v_mfma_f32_16x16x32_bf16 v[76:79], a[172:175], v[152:155], v[76:79]\n"
      "s_add_u32 m0, %[m0b], 25600\n"
      "v_cvt_pk_bf16_f32 v168, v168, v169\n"
      "v_fma_f32 v173, v173, %[csc], v216\n"
      "v_mfma_f32_16x16x32_bf16 v[124:127], a[172:175], v[160:163], v[124:127]\n"
      "buffer_load_dwordx4 %[vk1], s[56:59], s70 offen nt lds\n"
      "ds_read_b128 a[172:175], %[kb11] offset:6400\n"
      "v_mfma_f32_16x16x32_bf16 v[32:35], a[176:179], v[144:147], v[32:35]\n"
      "v_exp_f32 v172, v172\n"
      "v_mfma_f32_16x16x32_bf16 v[80:83], a[176:179], v[152:155], v[80:83]\n"
      "v_fma_f32 v174, v174, %[csc], v216\n"
      "v_mfma_f32_16x16x32_bf16 v[128:131], a[176:179], v[160:163], v[128:131]\n"
      "ds_read_b64_tr_b16 a[176:177], %[vb0] offset:0\n"
      "ds_read_b64_tr_b16 a[178:179], %[vb0] offset:6144\n"
      "v_mfma_f32_16x16x32_bf16 v[36:39], a[180:183], v[144:147], v[36:39]\n"
      "v_exp_f32 v173, v173\n"
      "v_mfma_f32_16x16x32_bf16 v[84:87], a[180:183], v[152:155], v[84:87]\n"
      "v_cvt_pk_bf16_f32 v169, v170, v171\n"
      "v_fma_f32 v175, v175, %[csc], v216\n"
      "v_mfma_f32_16x16x32_bf16 v[132:135], a[180:183], v[160:163], v[132:135]\n"
      "ds_read_b64_tr_b16 a[180:181], %[vb1] offset:0\n"
      "ds_read_b64_tr_b16 a[182:183], %[vb1] offset:6144\n"
      "v_mfma_f32_16x16x32_bf16 v[40:43], a[184:187], v[144:147], v[40:43]\n"
      "v_exp_f32 v174, v174\n"
      "v_mfma_f32_16x16x32_bf16 v[88:91], a[184:187], v[152:155], v[88:91]\n"
      "ds_write_b64 %[ow], a[136:137] offset:0\n"
      "v_fma_f32 v176, v176, %[csc], v217\n"
      "v_mfma_f32_16x16x32_bf16 v[136:139], a[184:187], v[160:163], v[136:139]\n"
      "s_add_u32 m0, %[m0b], 26624\n"
      "ds_read_b64_tr_b16 a[184:185], %[vb2] offset:0\n"
      "ds_read_b64_tr_b16 a[186:187], %[vb2] offset:6144\n"
      "v_mfma_f32_16x16x32_bf16 v[44:47], a[188:191], v[144:147], v[44:47]\n"
      "buffer_load_dwordx4 %[vk2], s[56:59], s70 offen nt lds\n"
      "v_mfma_f32_16x16x32_bf16 v[92:95], a[188:191], v[152:155], v[92:95]\n"
      "v_exp_f32 v175, v175\n"
      "v_mfma_f32_16x16x32_bf16 v[140:143], a[188:191], v[160:163], v[140:143]\n"
      "s_waitcnt lgkmcnt(15)\n"
      "v_cvt_pk_bf16_f32 v170, v172, v173\n"
      "v_fma_f32 v177, v177, %[csc], v217\n"
      "v_mfma_f32_16x16x32_bf16 v[192:195], a[192:195], a[0:3], 0\n"
      "v_exp_f32 v176, v176\n"
      "v_mfma_f32_16x16x32_bf16 v[200:203], a[192:195], a[24:27], 0\n"
      "ds_write_b64 %[ow], a[138:139] offset:32\n"
      "v_fma_f32 v178, v178, %[csc], v217\n"
      "v_mfma_f32_16x16x32_bf16 v[208:211], a[192:195], a[48:51], 0\n"
      "v_exp_f32 v177, v177\n"
      "v_mfma_f32_16x16x32_bf16 v[196:199], a[196:199], a[0:3], 0\n"
      "v_cvt_pk_bf16_f32 v171, v174, v175\n"
      "v_fma_f32 v179, v179, %[csc], v217\n"
      "v_mfma_f32_16x16x32_bf16 v[204:207], a[196:199], a[24:27], 0\n"
      "v_exp_f32 v178, v178\n"
      "v_mfma_f32_16x16x32_bf16 v[212:215], a[196:199], a[48:51], 0\n"
      "ds_write_b64 %[ow], a[140:141] offset:64\n"
      "v_fma_f32 v180, v180, %[csc], v217\n"
      "v_mfma_f32_16x16x32_bf16 v[192:195], a[200:203], a[4:7], v[192:195]\n"
      "s_add_u32 s71, s68, 18886656\n"
      "v_exp_f32 v179, v179\n"
      "v_mfma_f32_16x16x32_bf16 v[200:203], a[200:203], a[28:31], v[200:203]\n"
      "s_add_u32 m0, %[m0b], 110592\n"
      "v_cvt_pk_bf16_f32 v176, v176, v177\n"
      "v_fma_f32 v181, v181, %[csc], v217\n"
      "v_mfma_f32_16x16x32_bf16 v[208:211], a[200:203], a[52:55], v[208:211]\n"
      "buffer_load_dwordx4 %[vv0], s[60:63], s71 offen nt lds\n"
      "ds_write_b64 %[ow], a[142:143] offset:96\n"
      "v_mfma_f32_16x16x32_bf16 v[196:199], a[204:207], a[4:7], v[196:199]\n"
      "v_exp_f32 v180, v180\n"
      "v_mfma_f32_16x16x32_bf16 v[204:207], a[204:207], a[28:31], v[204:207]\n"
      "ds_read_b128 a[224:227], %[orb] offset:0\n"
      "v_fma_f32 v182, v182, %[csc], v217\n"
      "v_mfma_f32_16x16x32_bf16 v[212:215], a[204:207], a[52:55], v[212:215]\n"
      "s_waitcnt lgkmcnt(15)\n"
      "v_exp_f32 v181, v181\n"
      "v_mfma_f32_16x16x32_bf16 v[192:195], a[144:147], a[8:11], v[192:195]\n"
      "v_cvt_pk_bf16_f32 v177, v178, v179\n"
      "v_fma_f32 v183, v183, %[csc], v217\n"
      "v_mfma_f32_16x16x32_bf16 v[200:203], a[144:147], a[32:35], v[200:203]\n"
      "v_exp_f32 v182, v182\n"
      "v_mfma_f32_16x16x32_bf16 v[208:211], a[144:147], a[56:59], v[208:211]\n"
      "ds_read_b128 a[228:231], %[orb] offset:1152\n"
      "v_fma_f32 v184, v184, %[csc], v218\n"
      "v_mfma_f32_16x16x32_bf16 v[196:199], a[148:151], a[8:11], v[196:199]\n"
      "v_exp_f32 v183, v183\n"
      "v_mfma_f32_16x16x32_bf16 v[204:207], a[148:151], a[32:35], v[204:207]\n"
      "s_add_u32 m0, %[m0b], 111616\n"
      "v_cvt_pk_bf16_f32 v178, v180, v181\n"
      "v_fma_f32 v185, v185, %[csc], v218\n"
      "v_mfma_f32_16x16x32_bf16 v[212:215], a[148:151], a[56:59], v[212:215]\n"
      "buffer_load_dwordx4 %[vv1], s[60:63], s71 offen nt lds\n"
      "v_mfma_f32_16x16x32_bf16 v[192:195], a[152:155], a[12:15], v[192:195]\n"
      "v_exp_f32 v184, v184\n"
      "v_mfma_f32_16x16x32_bf16 v[200:203], a[152:155], a[36:39], v[200:203]\n"
      "v_fma_f32 v186, v186, %[csc], v218\n"
      "v_mfma_f32_16x16x32_bf16 v[208:211], a[152:155], a[60:63], v[208:211]\n"
      "v_exp_f32 v185, v185\n"
      "v_mfma_f32_16x16x32_bf16 v[196:199], a[156:159], a[12:15], v[196:199]\n"
      "s_waitcnt lgkmcnt(1)\n"
      "v_cvt_pk_bf16_f32 v179, v182, v183\n"
      "v_fma_f32 v187, v187, %[csc], v218\n"
      "v_mfma_f32_16x16x32_bf16 v[204:207], a[156:159], a[36:39], v[204:207]\n"
      "s_add_u32 s73, s69, 4276105216\n"
      "v_exp_f32 v186, v186\n"
      "v_mfma_f32_16x16x32_bf16 v[212:215], a[156:159], a[60:63], v[212:215]\n"
      "buffer_store_dwordx4 a[224:227], %[ost], s[84:87], s73 offen offset:256 sc1\n"
      "v_fma_f32 v188, v188, %[csc], v218\n"
      "v_mfma_f32_16x16x32_bf16 v[192:195], a[160:163], a[16:19], v[192:195]\n"
      "v_exp_f32 v187, v187\n"
      "v_mfma_f32_16x16x32_bf16 v[200:203], a[160:163], a[40:43], v[200:203]\n"
      "v_cvt_pk_bf16_f32 v184, v184, v185\n"
      "v_fma_f32 v189, v189, %[csc], v218\n"
      "v_mfma_f32_16x16x32_bf16 v[208:211], a[160:163], a[64:67], v[208:211]\n"
      "s_add_u32 m0, %[m0b], 112640\n"
      "v_exp_f32 v188, v188\n"
      "v_mfma_f32_16x16x32_bf16 v[196:199], a[164:167], a[16:19], v[196:199]\n"
      "buffer_load_dwordx4 %[vv2], s[60:63], s71 offen nt lds\n"
      "v_fma_f32 v190, v190, %[csc], v218\n"
      "v_mfma_f32_16x16x32_bf16 v[204:207], a[164:167], a[40:43], v[204:207]\n"
      "v_exp_f32 v189, v189\n"
      "v_mfma_f32_16x16x32_bf16 v[212:215], a[164:167], a[64:67], v[212:215]\n"
      "v_cvt_pk_bf16_f32 v185, v186, v187\n"
      "v_fma_f32 v191, v191, %[csc], v218\n"
      "v_mfma_f32_16x16x32_bf16 v[192:195], a[168:171], a[20:23], v[192:195]\n"
      "v_exp_f32 v190, v190\n"
      "v_mfma_f32_16x16x32_bf16 v[200:203], a[168:171], a[44:47], v[200:203]\n"
      "v_exp_f32 v191, v191\n"
      "v_mfma_f32_16x16x32_bf16 v[208:211], a[168:171], a[68:71], v[208:211]\n"
      "v_cvt_pk_bf16_f32 v186, v188, v189\n"
      "v_mfma_f32_16x16x32_bf16 v[196:199], a[172:175], a[20:23], v[196:199]\n"
      "s_waitcnt lgkmcnt(0)\n"
      "v_cvt_pk_bf16_f32 v187, v190, v191\n"
      "v_mfma_f32_16x16x32_bf16 v[204:207], a[172:175], a[44:47], v[204:207]\n"
      "s_add_u32 s73, s69, 4276108288\n"
      "ds_read_b64_tr_b16 a[188:189], %[vb3] offset:0\n"
      "ds_read_b64_tr_b16 a[190:191], %[vb3] offset:6144\n"
      "v_mfma_f32_16x16x32_bf16 v[212:215], a[172:175], a[68:71], v[212:215]\n"
      "ds_read_b64_tr_b16 a[192:193], %[vb0] offset:128\n"
      "ds_read_b64_tr_b16 a[194:195], %[vb0] offset:6272\n"
      "v_mfma_f32_16x16x32_bf16 a[208:211], a[220:223], v[168:171], a[208:211]\n"
      "s_add_u32 s72, s69, 37748736\n"
      "ds_read_b64_tr_b16 a[196:197], %[vb1] offset:128\n"
      "ds_read_b64_tr_b16 a[198:199], %[vb1] offset:6272\n"
      "v_mfma_f32_16x16x32_bf16 a[212:215], a[220:223], v[176:179], a[212:215]\n"
      "s_add_u32 m0, %[m0q], 0\n"
      "ds_read_b64_tr_b16 a[200:201], %[vb2] offset:128\n"
      "ds_read_b64_tr_b16 a[202:203], %[vb2] offset:6272\n"
      "v_mfma_f32_16x16x32_bf16 a[216:219], a[220:223], v[184:187], a[216:219]\n"
      "ds_read_b64_tr_b16 a[204:205], %[vb3] offset:128\n"
      "ds_read_b64_tr_b16 a[206:207], %[vb3] offset:6272\n"
      "v_mfma_f32_16x16x32_bf16 v[0:3], a[176:179], v[168:171], v[0:3]\n"
      "ds_read_b64_tr_b16 a[144:145], %[vb0] offset:256\n"
      "ds_read_b64_tr_b16 a[146:147], %[vb0] offset:6400\n"
      "v_mfma_f32_16x16x32_bf16 v[48:51], a[176:179], v[176:179], v[48:51]\n"
      "buffer_store_dwordx4 a[228:231], %[ost], s[84:87], s73 offen offset:256 sc1\n"
      "buffer_load_dwordx4 %[vq0], s[80:83], s72 offen nt lds\n"
      "v_mfma_f32_16x16x32_bf16 v[96:99], a[176:179], v[184:187], v[96:99]\n"
      "s_add_u32 m0, %[m0q], 1024\n"
      "ds_read_b64_tr_b16 a[148:149], %[vb1] offset:256\n"
      "ds_read_b64_tr_b16 a[150:151], %[vb1] offset:6400\n"
      "v_mfma_f32_16x16x32_bf16 v[4:7], a[180:183], v[168:171], v[4:7]\n"
      "ds_read_b64_tr_b16 a[152:153], %[vb2] offset:256\n"
      "ds_read_b64_tr_b16 a[154:155], %[vb2] offset:6400\n"
      "v_mfma_f32_16x16x32_bf16 v[52:55], a[180:183], v[176:179], v[52:55]\n"
      "ds_read_b64_tr_b16 a[156:157], %[vb3] offset:256\n"
      "ds_read_b64_tr_b16 a[158:159], %[vb3] offset:6400\n"
      "v_mfma_f32_16x16x32_bf16 v[100:103], a[180:183], v[184:187], v[100:103]\n"
      "buffer_load_dwordx4 %[vq1], s[80:83], s72 offen nt lds\n"
      "v_fma_f32 v192, v192, %[csc], v216\n"
      "v_mfma_f32_16x16x32_bf16 v[8:11], a[184:187], v[168:171], v[8:11]\n"
      "s_waitcnt vmcnt(26)\n"
      "ds_read_b128 a[136:139], %[qs0] offset:4096\n"
      "ds_read_b128 a[140:143], %[qs1] offset:4096\n"
      "v_mfma_f32_16x16x32_bf16 v[56:59], a[184:187], v[176:179], v[56:59]\n"
      "v_fma_f32 v193, v193, %[csc], v216\n"
      "v_mfma_f32_16x16x32_bf16 v[104:107], a[184:187], v[184:187], v[104:107]\n"
      "s_waitcnt lgkmcnt(12)\n"
      "v_exp_f32 v192, v192\n"
      "v_mfma_f32_16x16x32_bf16 v[12:15], a[188:191], v[168:171], v[12:15]\n"
      "v_fma_f32 v194, v194, %[csc], v216\n"
      "s_waitcnt vmcnt(18) lgkmcnt(0)\n"
      "s_barrier\n"
      "v_mfma_f32_16x16x32_bf16 v[60:63], a[188:191], v[176:179], v[60:63]\n"
      "s_add_u32 s70, s68, 18911232\n"
      "ds_read_b128 a[160:163], %[kb00] offset:0\n"
      "ds_read_b128 a[164:167], %[kb00] offset:6144\n"
      "v_mfma_f32_16x16x32_bf16 v[108:111], a[188:191], v[184:187], v[108:111]\n"
      "s_add_u32 m0, %[m0b], 36864\n"
      "ds_read_b128 a[168:171], %[kb10] offset:0\n"
      "ds_read_b128 a[172:175], %[kb10] offset:6144\n"
      "v_mfma_f32_16x16x32_bf16 v[16:19], a[192:195], v[168:171], v[16:19]\n"
      "buffer_load_dwordx4 %[vk0], s[56:59], s70 offen nt lds\n"
      "ds_read_b128 a[176:179], %[kb00] offset:128\n"
      "v_mfma_f32_16x16x32_bf16 v[64:67], a[192:195], v[176:179], v[64:67]\n"
      "ds_read_b128 a[180:183], %[kb00] offset:6272\n"
      "ds_read_b128 a[184:187], %[kb10] offset:128\n"
      "v_mfma_f32_16x16x32_bf16 v[112:115], a[192:195], v[184:187], v[112:115]\n"
      "ds_read_b128 a[188:191], %[kb10] offset:6272\n"
      "ds_read_b128 a[192:195], %[kb00] offset:256\n"
      "v_mfma_f32_16x16x32_bf16 v[20:23], a[196:199], v[168:171], v[20:23]\n"
      "v_exp_f32 v193, v193\n"
      "v_mfma_f32_16x16x32_bf16 v[68:71], a[196:199], v[176:179], v[68:71]\n"
      "v_fma_f32 v195, v195, %[csc], v216\n"
      "v_mfma_f32_16x16x32_bf16 v[116:119], a[196:199], v[184:187], v[116:119]\n"
      "ds_read_b128 a[196:199], %[kb00] offset:6400\n"
      "v_mfma_f32_16x16x32_bf16 v[24:27], a[200:203], v[168:171], v[24:27]\n"
      "v_exp_f32 v194, v194\n"
      "v_mfma_f32_16x16x32_bf16 v[72:75], a[200:203], v[176:179], v[72:75]\n"
      "v_fma_f32 v196, v196, %[csc], v216\n"
      "v_mfma_f32_16x16x32_bf16 v[120:123], a[200:203], v[184:187], v[120:123]\n"
      "s_add_u32 m0, %[m0b], 37888\n"
      "ds_read_b128 a[200:203], %[kb10] offset:256\n"
      "v_mfma_f32_16x16x32_bf16 v[28:31], a[204:207], v[168:171], v[28:31]\n"
      "buffer_load_dwordx4 %[vk1], s[56:59], s70 offen nt lds\n"
      "v_mfma_f32_16x16x32_bf16 v[76:79], a[204:207], v[176:179], v[76:79]\n"
      "v_exp_f32 v195, v195\n"
      "v_mfma_f32_16x16x32_bf16 v[124:127], a[204:207], v[184:187], v[124:127]\n"
      "ds_read_b128 a[204:207], %[kb10] offset:6400\n"
      "v_cvt_pk_bf16_f32 v192, v192, v193\n"
      "v_mfma_f32_16x16x32_bf16 v[32:35], a[144:147], v[168:171], v[32:35]\n"
      "v_fma_f32 v197, v197, %[csc], v216\n"
      "v_mfma_f32_16x16x32_bf16 v[80:83], a[144:147], v[176:179], v[80:83]\n"
      "v_exp_f32 v196, v196\n"
      "v_mfma_f32_16x16x32_bf16 v[128:131], a[144:147], v[184:187], v[128:131]\n"
      "ds_read_b64_tr_b16 a[144:145], %[vb0] offset:12288\n"
      "ds_read_b64_tr_b16 a[146:147], %[vb0] offset:18432\n"
      "v_mfma_f32_16x16x32_bf16 v[36:39], a[148:151], v[168:171], v[36:39]\n"
      "v_fma_f32 v198, v198, %[csc], v216\n"
      "v_mfma_f32_16x16x32_bf16 v[84:87], a[148:151], v[176:179], v[84:87]\n"
      "v_exp_f32 v197, v197\n"
      "v_mfma_f32_16x16x32_bf16 v[132:135], a[148:151], v[184:187], v[132:135]\n"
      "ds_read_b64_tr_b16 a[148:149], %[vb1] offset:12288\n"
      "ds_read_b64_tr_b16 a[150:151], %[vb1] offset:18432\n"
      "v_mfma_f32_16x16x32_bf16 v[40:43], a[152:155], v[168:171], v[40:43]\n"
      "s_add_u32 m0, %[m0b], 38912\n"
      "v_cvt_pk_bf16_f32 v193, v194, v195\n"
      "v_fma_f32 v199, v199, %[csc], v216\n"
      "v_mfma_f32_16x16x32_bf16 v[88:91], a[152:155], v[176:179], v[88:91]\n"
      "buffer_load_dwordx4 %[vk2], s[56:59], s70 offen nt lds\n"
      "v_mfma_f32_16x16x32_bf16 v[136:139], a[152:155], v[184:187], v[136:139]\n"
      "ds_read_b64_tr_b16 a[152:153], %[vb2] offset:12288\n"
      "ds_read_b64_tr_b16 a[154:155], %[vb2] offset:18432\n"
      "v_mfma_f32_16x16x32_bf16 v[44:47], a[156:159], v[168:171], v[44:47]\n"
      "v_exp_f32 v198, v198\n"
      "v_mfma_f32_16x16x32_bf16 v[92:95], a[156:159], v[176:179], v[92:95]\n"
      "v_fma_f32 v200, v200, %[csc], v217\n"
      "v_mfma_f32_16x16x32_bf16 v[140:143], a[156:159], v[184:187], v[140:143]\n"
      "s_waitcnt lgkmcnt(14)\n"
      "v_exp_f32 v199, v199\n"
      "v_mfma_f32_16x16x32_bf16 v[144:147], a[160:163], a[72:75], 0\n"
      "v_cvt_pk_bf16_f32 v194, v196, v197\n"
      "v_fma_f32 v201, v201, %[csc], v217\n"
      "v_mfma_f32_16x16x32_bf16 v[152:155], a[160:163], a[96:99], 0\n"
      "v_exp_f32 v200, v200\n"
      "v_mfma_f32_16x16x32_bf16 v[160:163], a[160:163], a[120:123], 0\n"
      "v_fma_f32 v202, v202, %[csc], v217\n"
      "v_mfma_f32_16x16x32_bf16 v[148:151], a[164:167], a[72:75], 0\n"
      "v_exp_f32 v201, v201\n"
      "v_mfma_f32_16x16x32_bf16 v[156:159], a[164:167], a[96:99], 0\n"
      "s_add_u32 s71, s68, 18898944\n"
      "v_cvt_pk_bf16_f32 v195, v198, v199\n"
      "v_fma_f32 v203, v203, %[csc], v217\n"
      "v_mfma_f32_16x16x32_bf16 v[164:167], a[164:167], a[120:123], 0\n"
      "s_add_u32 m0, %[m0b], 73728\n"
      "v_exp_f32 v202, v202\n"
      "v_mfma_f32_16x16x32_bf16 v[144:147], a[168:171], a[76:79], v[144:147]\n"
      "buffer_load_dwordx4 %[vv0], s[60:63], s71 offen nt lds\n"
      "v_fma_f32 v204, v204, %[csc], v217\n"
      "v_mfma_f32_16x16x32_bf16 v[152:155], a[168:171], a[100:103], v[152:155]\n"
      "v_exp_f32 v203, v203\n"
      "v_mfma_f32_16x16x32_bf16 v[160:163], a[168:171], a[124:127], v[160:163]\n"
      "v_cvt_pk_bf16_f32 v200, v200, v201\n"
      "v_fma_f32 v205, v205, %[csc], v217\n"
      "v_mfma_f32_16x16x32_bf16 v[148:151], a[172:175], a[76:79], v[148:151]\n"
      "v_exp_f32 v204, v204\n"
      "v_mfma_f32_16x16x32_bf16 v[156:159], a[172:175], a[100:103], v[156:159]\n"
      "v_fma_f32 v206, v206, %[csc], v217\n"
      "v_mfma_f32_16x16x32_bf16 v[164:167], a[172:175], a[124:127], v[164:167]\n"
      "s_waitcnt lgkmcnt(10)\n"
      "v_exp_f32 v205, v205\n"
      "v_mfma_f32_16x16x32_bf16 v[144:147], a[176:179], a[80:83], v[144:147]\n"
      "v_cvt_pk_bf16_f32 v201, v202, v203\n"
      "v_fma_f32 v207, v207, %[csc], v217\n"
      "v_mfma_f32_16x16x32_bf16 v[152:155], a[176:179], a[104:107], v[152:155]\n"
      "v_exp_f32 v206, v206\n"
      "v_mfma_f32_16x16x32_bf16 v[160:163], a[176:179], a[128:131], v[160:163]\n"
      "s_add_u32 m0, %[m0b], 74752\n"
      "v_fma_f32 v208, v208, %[csc], v218\n"
      "v_mfma_f32_16x16x32_bf16 v[148:151], a[180:183], a[80:83], v[148:151]\n"
      "buffer_load_dwordx4 %[vv1], s[60:63], s71 offen nt lds\n"
      "v_mfma_f32_16x16x32_bf16 v[156:159], a[180:183], a[104:107], v[156:159]\n"
      "v_exp_f32 v207, v207\n"
      "v_mfma_f32_16x16x32_bf16 v[164:167], a[180:183], a[128:131], v[164:167]\n"
      "v_cvt_pk_bf16_f32 v202, v204, v205\n"
      "v_fma_f32 v209, v209, %[csc], v218\n"
      "v_mfma_f32_16x16x32_bf16 v[144:147], a[184:187], a[84:87], v[144:147]\n"
      "v_exp_f32 v208, v208\n"
      "v_mfma_f32_16x16x32_bf16 v[152:155], a[184:187], a[108:111], v[152:155]\n"
      "v_fma_f32 v210, v210, %[csc], v218\n"
      "v_mfma_f32_16x16x32_bf16 v[160:163], a[184:187], a[132:135], v[160:163]\n"
      "v_exp_f32 v209, v209\n"
      "v_mfma_f32_16x16x32_bf16 v[148:151], a[188:191], a[84:87], v[148:151]\n"
      "v_cvt_pk_bf16_f32 v203, v206, v207\n"
      "v_fma_f32 v211, v211, %[csc], v218\n"
      "v_mfma_f32_16x16x32_bf16 v[156:159], a[188:191], a[108:111], v[156:159]\n"
      "v_exp_f32 v210, v210\n"
      "v_mfma_f32_16x16x32_bf16 v[164:167], a[188:191], a[132:135], v[164:167]\n"
      "s_waitcnt lgkmcnt(6)\n"
      "v_fma_f32 v212, v212, %[csc], v218\n"
      "v_mfma_f32_16x16x32_bf16 v[144:147], a[192:195], a[88:91], v[144:147]\n"
      "s_add_u32 m0, %[m0b], 75776\n"
      "v_exp_f32 v211, v211\n"
      "v_mfma_f32_16x16x32_bf16 v[152:155], a[192:195], a[112:115], v[152:155]\n"
      "v_cvt_pk_bf16_f32 v208, v208, v209\n"
      "v_fma_f32 v213, v213, %[csc], v218\n"
      "v_mfma_f32_16x16x32_bf16 v[160:163], a[192:195], a[136:139], v[160:163]\n"
      "v_exp_f32 v212, v212\n"
      "v_mfma_f32_16x16x32_bf16 v[148:151], a[196:199], a[88:91], v[148:151]\n"
      "buffer_load_dwordx4 %[vv2], s[60:63], s71 offen nt lds\n"
      "v_fma_f32 v214, v214, %[csc], v218\n"
      "v_mfma_f32_16x16x32_bf16 v[156:159], a[196:199], a[112:115], v[156:159]\n"
      "v_exp_f32 v213, v213\n"
      "v_mfma_f32_16x16x32_bf16 v[164:167], a[196:199], a[136:139], v[164:167]\n"
      "v_cvt_pk_bf16_f32 v209, v210, v211\n"
      "v_fma_f32 v215, v215, %[csc], v218\n"
      "v_mfma_f32_16x16x32_bf16 v[144:147], a[200:203], a[92:95], v[144:147]\n"
      "v_exp_f32 v214, v214\n"
      "v_mfma_f32_16x16x32_bf16 v[152:155], a[200:203], a[116:119], v[152:155]\n"
      "v_exp_f32 v215, v215\n"
      "v_mfma_f32_16x16x32_bf16 v[160:163], a[200:203], a[140:143], v[160:163]\n"
      "v_cvt_pk_bf16_f32 v210, v212, v213\n"
      "v_mfma_f32_16x16x32_bf16 v[148:151], a[204:207], a[92:95], v[148:151]\n"
      "v_cvt_pk_bf16_f32 v211, v214, v215\n"
      "v_mfma_f32_16x16x32_bf16 v[156:159], a[204:207], a[116:119], v[156:159]\n"
      "ds_read_b64_tr_b16 a[156:157], %[vb3] offset:12288\n"
      "ds_read_b64_tr_b16 a[158:159], %[vb3] offset:18432\n"
      "v_mfma_f32_16x16x32_bf16 v[164:167], a[204:207], a[140:143], v[164:167]\n"
      "ds_read_b64_tr_b16 a[160:161], %[vb0] offset:12416\n"
      "ds_read_b64_tr_b16 a[162:163], %[vb0] offset:18560\n"
      "v_mfma_f32_16x16x32_bf16 a[208:211], a[220:223], v[192:195], a[208:211]\n"
      "ds_read_b64_tr_b16 a[164:165], %[vb1] offset:12416\n"
      "ds_read_b64_tr_b16 a[166:167], %[vb1] offset:18560\n"
      "v_mfma_f32_16x16x32_bf16 a[212:215], a[220:223], v[200:203], a[212:215]\n"
      "ds_read_b64_tr_b16 a[168:169], %[vb2] offset:12416\n"
      "ds_read_b64_tr_b16 a[170:171], %[vb2] offset:18560\n"
      "v_mfma_f32_16x16x32_bf16 a[216:219], a[220:223], v[208:211], a[216:219]\n"
      "s_waitcnt lgkmcnt(8)\n"
      "ds_read_b64_tr_b16 a[172:173], %[vb3] offset:12416\n"
      "ds_read_b64_tr_b16 a[174:175], %[vb3] offset:18560\n"
      "v_mfma_f32_16x16x32_bf16 v[0:3], a[144:147], v[192:195], v[0:3]\n"
      "ds_read_b64_tr_b16 a[176:177], %[vb0] offset:12544\n"
      "ds_read_b64_tr_b16 a[178:179], %[vb0] offset:18688\n"
      "v_mfma_f32_16x16x32_bf16 v[48:51], a[144:147], v[200:203], v[48:51]\n"
      "ds_read_b64_tr_b16 a[180:181], %[vb1] offset:12544\n"
      "ds_read_b64_tr_b16 a[182:183], %[vb1] offset:18688\n"
      "v_mfma_f32_16x16x32_bf16 v[96:99], a[144:147], v[208:211], v[96:99]\n"
      "ds_read_b64_tr_b16 a[184:185], %[vb2] offset:12544\n"
      "ds_read_b64_tr_b16 a[186:187], %[vb2] offset:18688\n"
      "v_mfma_f32_16x16x32_bf16 v[4:7], a[148:151], v[192:195], v[4:7]\n"
      "ds_read_b64_tr_b16 a[188:189], %[vb3] offset:12544\n"
      "ds_read_b64_tr_b16 a[190:191], %[vb3] offset:18688\n"
      "v_mfma_f32_16x16x32_bf16 v[52:55], a[148:151], v[200:203], v[52:55]\n"
      "v_max3_f32 v222, v144, v145, v146\n"
      "v_max3_f32 v223, v147, v148, v149\n"
      "v_mfma_f32_16x16x32_bf16 v[100:103], a[148:151], v[208:211], v[100:103]\n"
      "v_max3_f32 v222, v222, v150, v151\n"
      "v_max_f32 v222, v222, v223\n"
      "v_mfma_f32_16x16x32_bf16 v[8:11], a[152:155], v[192:195], v[8:11]\n"
      "v_mov_b32 v223, v222\n"
      "v_accvgpr_read_b32 v224, a208\n"
      "v_mfma_f32_16x16x32_bf16 v[56:59], a[152:155], v[200:203], v[56:59]\n"
      "s_add_u32 s72, s69, 37754880\n"
      "v_permlane16_swap_b32 v222, v223\n"
      "v_max_f32 v222, v222, v223\n"
      "v_mfma_f32_16x16x32_bf16 v[104:107], a[152:155], v[208:211], v[104:107]\n"
      "s_waitcnt lgkmcnt(10)\n"
      "v_mov_b32 v223, v222\n"
      "v_cmp_nge_f32 vcc, 0x47800000, v224\n"
      "s_waitcnt vmcnt(13) lgkmcnt(0)\n"
      "s_barrier\n"
      "v_mfma_f32_16x16x32_bf16 v[12:15], a[156:159], v[192:195], v[12:15]\n"
      "s_add_u32 s70, s68, 18923520\n"
      "ds_read_b128 a[192:195], %[kb00] offset:12288\n"
      "ds_read_b128 a[196:199], %[kb00] offset:18432\n"
      "v_mfma_f32_16x16x32_bf16 v[60:63], a[156:159], v[200:203], v[60:63]\n"
      "s_add_u32 m0, %[m0b], 0\n"
      "ds_read_b128 a[200:203], %[kb10] offset:12288\n"
      "ds_read_b128 a[204:207], %[kb10] offset:18432\n"
      "v_mfma_f32_16x16x32_bf16 v[108:111], a[156:159], v[208:211], v[108:111]\n"
      "buffer_load_dwordx4 %[vk0], s[56:59], s70 offen nt lds\n"
      "ds_read_b128 a[144:147], %[kb00] offset:12416\n"
      "v_mfma_f32_16x16x32_bf16 v[16:19], a[160:163], v[192:195], v[16:19]\n"
      "s_add_u32 m0, %[m0q], 2048\n"
      "ds_read_b128 a[148:151], %[kb00] offset:18560\n"
      "ds_read_b128 a[152:155], %[kb10] offset:12416\n"
      "v_mfma_f32_16x16x32_bf16 v[64:67], a[160:163], v[200:203], v[64:67]\n"
      "buffer_load_dwordx4 %[vq0], s[80:83], s72 offen nt lds\n"
      "ds_read_b128 a[156:159], %[kb10] offset:18560\n"
      "v_mfma_f32_16x16x32_bf16 v[112:115], a[160:163], v[208:211], v[112:115]\n"
      "s_add_u32 m0, %[m0q], 3072\n"
      "ds_read_b128 a[160:163], %[kb00] offset:12544\n"
      "v_permlane32_swap_b32 v222, v223\n"
      "v_mfma_f32_16x16x32_bf16 v[20:23], a[164:167], v[192:195], v[20:23]\n"
      "buffer_load_dwordx4 %[vq1], s[80:83], s72 offen nt lds\n"
      "v_max_f32 v222, v222, v223\n"
      "v_mfma_f32_16x16x32_bf16 v[68:71], a[164:167], v[200:203], v[68:71]\n"
      "v_mul_f32_e64 v216, -%[csc], v222\n"
      "v_max3_f32 v222, v152, v153, v154\n"
      "v_rcp_f32 v219, v224\n"
      "s_or_b64 %[flagA], %[flagA], vcc\n"
      "v_accvgpr_read_b32 v224, a212\n"
      "v_cmp_nge_f32 vcc, 0x47800000, v224\n"
      "v_rcp_f32 v220, v224\n"
      "s_nop 2\n"
      "s_or_b64 %[flagA], %[flagA], vcc\n"
      "v_accvgpr_read_b32 v224, a216\n"
      "v_cmp_nge_f32 vcc, 0x47800000, v224\n"
      "v_rcp_f32 v221, v224\n"
      "s_nop 2\n"
      "s_or_b64 %[flagA], %[flagA], vcc\n"
      "v_mul_f32 v0, v0, v219\n"
      "v_mul_f32 v1, v1, v219\n"
      "v_mul_f32 v2, v2, v219\n"
      "v_mul_f32 v3, v3, v219\n"
      "v_mul_f32 v48, v48, v220\n"
      "v_mul_f32 v49, v49, v220\n"
      "v_cvt_pk_bf16_f32 v0, v0, v1\n"
      "v_mul_f32 v50, v50, v220\n"
      "v_cvt_pk_bf16_f32 v1, v2, v3\n"
      "v_mul_f32 v51, v51, v220\n"
      "v_mul_f32 v96, v96, v221\n"
      "v_accvgpr_write_b32 a0, v0\n"
      "v_mul_f32 v97, v97, v221\n"
      "v_cvt_pk_bf16_f32 v48, v48, v49\n"
      "v_mul_f32 v98, v98, v221\n"
      "v_cvt_pk_bf16_f32 v49, v50, v51\n"
      "v_mul_f32 v99, v99, v221\n"
      "v_accvgpr_write_b32 a1, v1\n"
      "v_mfma_f32_16x16x32_bf16 v[116:119], a[164:167], v[208:211], v[116:119]\n"
      "v_mul_f32 v4, v4, v219\n"
      "v_accvgpr_write_b32 a24, v48\n"
      "v_mfma_f32_16x16x32_bf16 v[24:27], a[168:171], v[192:195], v[24:27]\n"
      "v_mul_f32 v5, v5, v219\n"
      "v_cvt_pk_bf16_f32 v96, v96, v97\n"
      "v_mfma_f32_16x16x32_bf16 v[72:75], a[168:171], v[200:203], v[72:75]\n"
      "s_add_u32 m0, %[m0b], 1024\n"
      "v_mul_f32 v6, v6, v219\n"
      "v_cvt_pk_bf16_f32 v97, v98, v99\n"
      "v_mfma_f32_16x16x32_bf16 v[120:123], a[168:171], v[208:211], v[120:123]\n"
      "v_mul_f32 v7, v7, v219\n"
      "v_accvgpr_write_b32 a25, v49\n"
      "v_mul_f32 v52, v52, v220\n"
      "v_accvgpr_write_b32 a48, v96\n"
      "v_mul_f32 v53, v53, v220\n"
      "v_cvt_pk_bf16_f32 v4, v4, v5\n"
      "v_mul_f32 v54, v54, v220\n"
      "v_cvt_pk_bf16_f32 v5, v6, v7\n"
      "v_mul_f32 v55, v55, v220\n"
      "v_accvgpr_write_b32 a49, v97\n"
      "v_mul_f32 v100, v100, v221\n"
      "v_accvgpr_write_b32 a2, v4\n"
      "v_mul_f32 v101, v101, v221\n"
      "v_cvt_pk_bf16_f32 v52, v52, v53\n"
      "v_mul_f32 v102, v102, v221\n"
      "v_cvt_pk_bf16_f32 v53, v54, v55\n"
      "v_mul_f32 v103, v103, v221\n"
      "v_accvgpr_write_b32 a3, v5\n"
      "v_mfma_f32_16x16x32_bf16 v[28:31], a[172:175], v[192:195], v[28:31]\n"
      "buffer_load_dwordx4 %[vk1], s[56:59], s70 offen nt lds\n"
      "v_mul_f32 v8, v8, v219\n"
      "v_mfma_f32_16x16x32_bf16 v[76:79], a[172:175], v[200:203], v[76:79]\n"
      "v_accvgpr_write_b32 a26, v52\n"
      "v_mul_f32 v9, v9, v219\n"
      "v_mfma_f32_16x16x32_bf16 v[124:127], a[172:175], v[208:211], v[124:127]\n"
      "v_cvt_pk_bf16_f32 v100, v100, v101\n"
      "v_mul_f32 v10, v10, v219\n"
      "v_cvt_pk_bf16_f32 v101, v102, v103\n"
      "v_mul_f32 v11, v11, v219\n"
      "v_accvgpr_write_b32 a27, v53\n"
      "v_mul_f32 v56, v56, v220\n"
      "v_accvgpr_write_b32 a50, v100\n"
      "v_mul_f32 v57, v57, v220\n"
      "v_cvt_pk_bf16_f32 v8, v8, v9\n"
      "v_mul_f32 v58, v58, v220\n"
      "v_cvt_pk_bf16_f32 v9, v10, v11\n"
      "v_mul_f32 v59, v59, v220\n"
      "v_accvgpr_write_b32 a51, v101\n"
      "v_mul_f32 v104, v104, v221\n"
      "v_accvgpr_write_b32 a4, v8\n"
      "v_mul_f32 v105, v105, v221\n"
      "v_cvt_pk_bf16_f32 v56, v56, v57\n"
      "v_mul_f32 v106, v106, v221\n"
      "v_cvt_pk_bf16_f32 v57, v58, v59\n"
      "v_mul_f32 v107, v107, v221\n"
      "v_accvgpr_write_b32 a5, v9\n"
      "v_mfma_f32_16x16x32_bf16 v[32:35], a[176:179], v[192:195], v[32:35]\n"
      "v_mul_f32 v12, v12, v219\n"
      "v_accvgpr_write_b32 a28, v56\n"
      "v_mfma_f32_16x16x32_bf16 v[80:83], a[176:179], v[200:203], v[80:83]\n"
      "ds_read_b128 a[164:167], %[kb00] offset:18688\n"
      "v_mul_f32 v13, v13, v219\n"
      "v_mfma_f32_16x16x32_bf16 v[128:131], a[176:179], v[208:211], v[128:131]\n"
      "v_cvt_pk_bf16_f32 v104, v104, v105\n"
      "v_mul_f32 v14, v14, v219\n"
      "v_mfma_f32_16x16x32_bf16 v[36:39], a[180:183], v[192:195], v[36:39]\n"
      "v_cvt_pk_bf16_f32 v105, v106, v107\n"
      "v_mul_f32 v15, v15, v219\n"
      "v_accvgpr_write_b32 a29, v57\n"
      "v_mul_f32 v60, v60, v220\n"
      "v_accvgpr_write_b32 a52, v104\n"
      "v_mul_f32 v61, v61, v220\n"
      "v_cvt_pk_bf16_f32 v12, v12, v13\n"
      "v_mul_f32 v62, v62, v220\n"
      "v_cvt_pk_bf16_f32 v13, v14, v15\n"
      "v_mul_f32 v63, v63, v220\n"
      "v_accvgpr_write_b32 a53, v105\n"
      "v_mul_f32 v108, v108, v221\n"
      "v_accvgpr_write_b32 a6, v12\n"
      "v_mul_f32 v109, v109, v221\n"
      "v_cvt_pk_bf16_f32 v60, v60, v61\n"
      "v_mul_f32 v110, v110, v221\n"
      "v_cvt_pk_bf16_f32 v61, v62, v63\n"
      "v_mul_f32 v111, v111, v221\n"
      "v_accvgpr_write_b32 a7, v13\n"
      "v_mfma_f32_16x16x32_bf16 v[84:87], a[180:183], v[200:203], v[84:87]\n"
      "ds_read_b128 a[168:171], %[kb10] offset:12544\n"
      "ds_read_b128 a[172:175], %[kb10] offset:18688\n"
      "v_mfma_f32_16x16x32_bf16 v[132:135], a[180:183], v[208:211], v[132:135]\n"
      "s_add_u32 m0, %[m0b], 2048\n"
      "v_mul_f32 v16, v16, v219\n"
      "v_accvgpr_write_b32 a30, v60\n"
      "v_mfma_f32_16x16x32_bf16 v[40:43], a[184:187], v[192:195], v[40:43]\n"
      "buffer_load_dwordx4 %[vk2], s[56:59], s70 offen nt lds\n"
      "v_mul_f32 v17, v17, v219\n"
      "v_mfma_f32_16x16x32_bf16 v[88:91], a[184:187], v[200:203], v[88:91]\n"
      "v_cvt_pk_bf16_f32 v108, v108, v109\n"
      "v_mul_f32 v18, v18, v219\n"
      "v_mfma_f32_16x16x32_bf16 v[136:139], a[184:187], v[208:211], v[136:139]\n"
      "v_cvt_pk_bf16_f32 v109, v110, v111\n"
      "v_mul_f32 v19, v19, v219\n"
      "v_mfma_f32_16x16x32_bf16 v[44:47], a[188:191], v[192:195], v[44:47]\n"
      "ds_read_b128 a[176:179], %[kb00] offset:24576\n"
      "v_accvgpr_write_b32 a31, v61\n"
      "v_mfma_f32_16x16x32_bf16 v[92:95], a[188:191], v[200:203], v[92:95]\n"
      "ds_read_b128 a[180:183], %[kb00] offset:30720\n"
      "ds_read_b128 a[184:187], %[kb10] offset:24576\n"
      "v_mfma_f32_16x16x32_bf16 v[140:143], a[188:191], v[208:211], v[140:143]\n"
      "s_waitcnt lgkmcnt(11)\n"
      "ds_read_b128 a[188:191], %[kb10] offset:30720\n"
      "v_mul_f32 v64, v64, v220\n"
      "v_mfma_f32_16x16x32_bf16 v[168:171], a[192:195], a[72:75], 0\n"
      "v_accvgpr_write_b32 a54, v108\n"
      "v_mul_f32 v65, v65, v220\n"
      "v_mfma_f32_16x16x32_bf16 v[176:179], a[192:195], a[96:99], 0\n"
      "v_cvt_pk_bf16_f32 v16, v16, v17\n"
      "v_mul_f32 v66, v66, v220\n"
      "v_mfma_f32_16x16x32_bf16 v[184:187], a[192:195], a[120:123], 0\n"
      "ds_read_b128 a[192:195], %[kb00] offset:24704\n"
      "v_cvt_pk_bf16_f32 v17, v18, v19\n"
      "v_mfma_f32_16x16x32_bf16 v[172:175], a[196:199], a[72:75], 0\n"
      "s_add_u32 s71, s68, 18911232\n"
      "v_mul_f32 v67, v67, v220\n"
      "v_accvgpr_write_b32 a55, v109\n"
      "v_mfma_f32_16x16x32_bf16 v[180:183], a[196:199], a[96:99], 0\n"
      "s_add_u32 m0, %[m0b], 86016\n"
      "v_mul_f32 v112, v112, v221\n"
      "v_accvgpr_write_b32 a8, v16\n"
      "v_mfma_f32_16x16x32_bf16 v[188:191], a[196:199], a[120:123], 0\n"
      "buffer_load_dwordx4 %[vv0], s[60:63], s71 offen nt lds\n"
      "ds_read_b128 a[196:199], %[kb00] offset:30848\n"
      "v_mfma_f32_16x16x32_bf16 v[168:171], a[200:203], a[76:79], v[168:171]\n"
      "v_mul_f32 v113, v113, v221\n"
      "v_cvt_pk_bf16_f32 v64, v64, v65\n"
      "v_mfma_f32_16x16x32_bf16 v[176:179], a[200:203], a[100:103], v[176:179]\n"
      "v_mul_f32 v114, v114, v221\n"
      "v_cvt_pk_bf16_f32 v65, v66, v67\n"
      "v_mfma_f32_16x16x32_bf16 v[184:187], a[200:203], a[124:127], v[184:187]\n"
      "ds_read_b128 a[200:203], %[kb10] offset:24704\n"
      "v_mul_f32 v115, v115, v221\n"
      "v_mfma_f32_16x16x32_bf16 v[172:175], a[204:207], a[76:79], v[172:175]\n"
      "v_accvgpr_write_b32 a9, v17\n"
      "v_mul_f32 v20, v20, v219\n"
      "v_mfma_f32_16x16x32_bf16 v[180:183], a[204:207], a[100:103], v[180:183]\n"
      "v_accvgpr_write_b32 a32, v64\n"
      "v_mul_f32 v21, v21, v219\n"
      "v_mfma_f32_16x16x32_bf16 v[188:191], a[204:207], a[124:127], v[188:191]\n"
      "s_waitcnt lgkmcnt(11)\n"
      "ds_read_b128 a[204:207], %[kb10] offset:30848\n"
      "v_cvt_pk_bf16_f32 v112, v112, v113\n"
      "v_mfma_f32_16x16x32_bf16 v[168:171], a[144:147], a[80:83], v[168:171]\n"
      "v_mul_f32 v22, v22, v219\n"
      "v_cvt_pk_bf16_f32 v113, v114, v115\n"
      "v_mfma_f32_16x16x32_bf16 v[176:179], a[144:147], a[104:107], v[176:179]\n"
      "s_add_u32 m0, %[m0b], 87040\n"
      "v_mul_f32 v23, v23, v219\n"
      "v_accvgpr_write_b32 a33, v65\n"
      "v_mfma_f32_16x16x32_bf16 v[184:187], a[144:147], a[128:131], v[184:187]\n"
      "buffer_load_dwordx4 %[vv1], s[60:63], s71 offen nt lds\n"
      "v_mul_f32 v68, v68, v220\n"
      "v_mfma_f32_16x16x32_bf16 v[172:175], a[148:151], a[80:83], v[172:175]\n"
      "ds_read_b128 a[144:147], %[kb00] offset:24832\n"
      "v_accvgpr_write_b32 a56, v112\n"
      "v_mfma_f32_16x16x32_bf16 v[180:183], a[148:151], a[104:107], v[180:183]\n"
      "v_mul_f32 v69, v69, v220\n"
      "v_cvt_pk_bf16_f32 v20, v20, v21\n"
      "v_mfma_f32_16x16x32_bf16 v[188:191], a[148:151], a[128:131], v[188:191]\n"
      "v_mul_f32 v70, v70, v220\n"
      "v_cvt_pk_bf16_f32 v21, v22, v23\n"
      "v_mfma_f32_16x16x32_bf16 v[168:171], a[152:155], a[84:87], v[168:171]\n"
      "ds_read_b128 a[148:151], %[kb00] offset:30976\n"
      "v_mul_f32 v71, v71, v220\n"
      "v_mfma_f32_16x16x32_bf16 v[176:179], a[152:155], a[108:111], v[176:179]\n"
      "v_accvgpr_write_b32 a57, v113\n"
      "v_mul_f32 v116, v116, v221\n"
      "v_mfma_f32_16x16x32_bf16 v[184:187], a[152:155], a[132:135], v[184:187]\n"
      "ds_read_b128 a[152:155], %[kb10] offset:24832\n"
      "v_accvgpr_write_b32 a10, v20\n"
      "v_mfma_f32_16x16x32_bf16 v[172:175], a[156:159], a[84:87], v[172:175]\n"
      "v_mul_f32 v117, v117, v221\n"
      "v_cvt_pk_bf16_f32 v68, v68, v69\n"
      "v_mfma_f32_16x16x32_bf16 v[180:183], a[156:159], a[108:111], v[180:183]\n"
      "v_mul_f32 v118, v118, v221\n"
      "v_cvt_pk_bf16_f32 v69, v70, v71\n"
      "v_mfma_f32_16x16x32_bf16 v[188:191], a[156:159], a[132:135], v[188:191]\n"
      "s_waitcnt lgkmcnt(11)\n"
      "v_mul_f32 v119, v119, v221\n"
      "v_accvgpr_write_b32 a11, v21\n"
      "v_mfma_f32_16x16x32_bf16 v[168:171], a[160:163], a[88:91], v[168:171]\n"
      "s_add_u32 m0, %[m0b], 88064\n"
      "v_mul_f32 v24, v24, v219\n"
      "v_accvgpr_write_b32 a34, v68\n"
      "v_mfma_f32_16x16x32_bf16 v[176:179], a[160:163], a[112:115], v[176:179]\n"
      "buffer_load_dwordx4 %[vv2], s[60:63], s71 offen nt lds\n"
      "v_mul_f32 v25, v25, v219\n"
      "v_mfma_f32_16x16x32_bf16 v[184:187], a[160:163], a[136:139], v[184:187]\n"
      "v_cvt_pk_bf16_f32 v116, v116, v117\n"
      "v_mul_f32 v26, v26, v219\n"
      "v_mfma_f32_16x16x32_bf16 v[172:175], a[164:167], a[88:91], v[172:175]\n"
      "v_cvt_pk_bf16_f32 v117, v118, v119\n"
      "v_mul_f32 v27, v27, v219\n"
      "v_mfma_f32_16x16x32_bf16 v[180:183], a[164:167], a[112:115], v[180:183]\n"
      "v_accvgpr_write_b32 a35, v69\n"
      "v_mul_f32 v72, v72, v220\n"
      "v_mfma_f32_16x16x32_bf16 v[188:191], a[164:167], a[136:139], v[188:191]\n"
      "v_accvgpr_write_b32 a58, v116\n"
      "v_mul_f32 v73, v73, v220\n"
      "v_mfma_f32_16x16x32_bf16 v[168:171], a[168:171], a[92:95], v[168:171]\n"
      "v_cvt_pk_bf16_f32 v24, v24, v25\n"
      "v_mul_f32 v74, v74, v220\n"
      "v_cvt_pk_bf16_f32 v25, v26, v27\n"
      "v_mul_f32 v75, v75, v220\n"
      "v_accvgpr_write_b32 a59, v117\n"
      "v_mul_f32 v120, v120, v221\n"
      "v_accvgpr_write_b32 a12, v24\n"
      "v_mul_f32 v121, v121, v221\n"
      "v_cvt_pk_bf16_f32 v72, v72, v73\n"
      "v_mul_f32 v122, v122, v221\n"
      "v_cvt_pk_bf16_f32 v73, v74, v75\n"
      "v_mul_f32 v123, v123, v221\n"
      "v_accvgpr_write_b32 a13, v25\n"
      "v_mfma_f32_16x16x32_bf16 v[176:179], a[168:171], a[116:119], v[176:179]\n"
      "v_mul_f32 v28, v28, v219\n"
      "v_accvgpr_write_b32 a36, v72\n"
      "v_mfma_f32_16x16x32_bf16 v[184:187], a[168:171], a[140:143], v[184:187]\n"
      "ds_read_b128 a[156:159], %[kb10] offset:30976\n"
      "v_mul_f32 v29, v29, v219\n"
      "v_mfma_f32_16x16x32_bf16 v[172:175], a[172:175], a[92:95], v[172:175]\n"
      "v_cvt_pk_bf16_f32 v120, v120, v121\n"
      "v_mul_f32 v30, v30, v219\n"
      "v_cvt_pk_bf16_f32 v121, v122, v123\n"
      "v_mul_f32 v31, v31, v219\n"
      "v_accvgpr_write_b32 a37, v73\n"
      "v_mul_f32 v76, v76, v220\n"
      "v_accvgpr_write_b32 a60, v120\n"
      "v_mul_f32 v77, v77, v220\n"
      "v_cvt_pk_bf16_f32 v28, v28, v29\n"
      "v_mul_f32 v78, v78, v220\n"
      "v_cvt_pk_bf16_f32 v29, v30, v31\n"
      "v_mul_f32 v79, v79, v220\n"
      "v_accvgpr_write_b32 a61, v121\n"
      "v_mul_f32 v124, v124, v221\n"
      "v_accvgpr_write_b32 a14, v28\n"
      "v_mul_f32 v125, v125, v221\n"
      "v_cvt_pk_bf16_f32 v76, v76, v77\n"
      "v_mul_f32 v126, v126, v221\n"
      "v_cvt_pk_bf16_f32 v77, v78, v79\n"
      "v_mul_f32 v127, v127, v221\n"
      "v_accvgpr_write_b32 a15, v29\n"
      "v_mfma_f32_16x16x32_bf16 v[180:183], a[172:175], a[116:119], v[180:183]\n"
      "v_mul_f32 v32, v32, v219\n"
      "v_accvgpr_write_b32 a38, v76\n"
      "v_mfma_f32_16x16x32_bf16 v[188:191], a[172:175], a[140:143], v[188:191]\n"
      "s_waitcnt lgkmcnt(8)\n"
      "v_mul_f32 v33, v33, v219\n"
      "v_cvt_pk_bf16_f32 v124, v124, v125\n"
      "v_mfma_f32_16x16x32_bf16 v[192:195], a[176:179], a[72:75], 0\n"
      "v_mul_f32 v34, v34, v219\n"
      "v_cvt_pk_bf16_f32 v125, v126, v127\n"
      "v_mfma_f32_16x16x32_bf16 v[200:203], a[176:179], a[96:99], 0\n"
      "v_mul_f32 v35, v35, v219\n"
      "v_accvgpr_write_b32 a39, v77\n"
      "s_waitcnt vmcnt(17) lgkmcnt(0)\n"
      "s_barrier\n"
      "v_mfma_f32_16x16x32_bf16 v[208:211], a[176:179], a[120:123], 0\n"
      "s_add_u32 s70, s68, 18935808\n"
      "ds_read_b64_tr_b16 a[160:161], %[vb0] offset:24576\n"
      "ds_read_b64_tr_b16 a[162:163], %[vb0] offset:30720\n"
      "v_mfma_f32_16x16x32_bf16 v[196:199], a[180:183], a[72:75], 0\n"
      "s_add_u32 m0, %[m0b], 12288\n"
      "ds_read_b64_tr_b16 a[164:165], %[vb1] offset:24576\n"
      "ds_read_b64_tr_b16 a[166:167], %[vb1] offset:30720\n"
      "v_mfma_f32_16x16x32_bf16 v[204:207], a[180:183], a[96:99], 0\n"
      "buffer_load_dwordx4 %[vk0], s[56:59], s70 offen nt lds\n"
      "v_max3_f32 v223, v155, v156, v157\n"
      "v_mfma_f32_16x16x32_bf16 v[212:215], a[180:183], a[120:123], 0\n"
      "ds_read_b64_tr_b16 a[168:169], %[vb2] offset:24576\n"
      "ds_read_b64_tr_b16 a[170:171], %[vb2] offset:30720\n"
      "v_mfma_f32_16x16x32_bf16 v[192:195], a[184:187], a[76:79], v[192:195]\n"
      "v_max3_f32 v222, v222, v158, v159\n"
      "v_max_f32 v222, v222, v223\n"
      "v_mfma_f32_16x16x32_bf16 v[200:203], a[184:187], a[100:103], v[200:203]\n"
      "v_mov_b32 v223, v222\n"
      "v_mul_f32 v80, v80, v220\n"
      "v_mfma_f32_16x16x32_bf16 v[208:211], a[184:187], a[124:127], v[208:211]\n"
      "v_accvgpr_write_b32 a62, v124\n"
      "v_permlane16_swap_b32 v222, v223\n"
      "v_mfma_f32_16x16x32_bf16 v[196:199], a[188:191], a[76:79], v[196:199]\n"
      "v_max_f32 v222, v222, v223\n"
      "v_mov_b32 v223, v222\n"
      "v_mfma_f32_16x16x32_bf16 v[204:207], a[188:191], a[100:103], v[204:207]\n"
      "s_nop 0\n"
      "v_permlane32_swap_b32 v222, v223\n"
      "v_max_f32 v222, v222, v223\n"
      "v_mfma_f32_16x16x32_bf16 v[212:215], a[188:191], a[124:127], v[212:215]\n"
      "v_mul_f32_e64 v217, -%[csc], v222\n"
      "v_max3_f32 v222, v160, v161, v162\n"
      "v_mfma_f32_16x16x32_bf16 v[192:195], a[192:195], a[80:83], v[192:195]\n"
      "s_add_u32 m0, %[m0b], 13312\n"
      "v_max3_f32 v223, v163, v164, v165\n"
      "v_max3_f32 v222, v222, v166, v167\n"
      "v_mfma_f32_16x16x32_bf16 v[200:203], a[192:195], a[104:107], v[200:203]\n"
      "buffer_load_dwordx4 %[vk1], s[56:59], s70 offen nt lds\n"
      "v_max_f32 v222, v222, v223\n"
      "v_mfma_f32_16x16x32_bf16 v[208:211], a[192:195], a[128:131], v[208:211]\n"
      "v_mov_b32 v223, v222\n"
      "v_mul_f32 v81, v81, v220\n"
      "v_mfma_f32_16x16x32_bf16 v[196:199], a[196:199], a[80:83], v[196:199]\n"
      "v_cvt_pk_bf16_f32 v32, v32, v33\n"
      "v_permlane16_swap_b32 v222, v223\n"
      "v_mfma_f32_16x16x32_bf16 v[204:207], a[196:199], a[104:107], v[204:207]\n"
      "v_max_f32 v222, v222, v223\n"
      "v_mov_b32 v223, v222\n"
      "v_mfma_f32_16x16x32_bf16 v[212:215], a[196:199], a[128:131], v[212:215]\n"
      "s_nop 0\n"
      "v_permlane32_swap_b32 v222, v223\n"
      "v_max_f32 v222, v222, v223\n"
      "v_mfma_f32_16x16x32_bf16 v[192:195], a[200:203], a[84:87], v[192:195]\n"
      "v_mul_f32_e64 v218, -%[csc], v222\n"
      "v_fma_f32 v144, v144, %[csc], v216\n"
      "v_mfma_f32_16x16x32_bf16 v[200:203], a[200:203], a[108:111], v[200:203]\n"
      "v_fma_f32 v145, v145, %[csc], v216\n"
      "v_mul_f32 v82, v82, v220\n"
      "v_mfma_f32_16x16x32_bf16 v[208:211], a[200:203], a[132:135], v[208:211]\n"
      "s_add_u32 s72, s69, 37761024\n"
      "v_cvt_pk_bf16_f32 v33, v34, v35\n"
      "v_mul_f32 v83, v83, v220\n"
      "v_mfma_f32_16x16x32_bf16 v[196:199], a[204:207], a[84:87], v[196:199]\n"
      "s_add_u32 m0, %[m0q], 4096\n"
      "v_accvgpr_write_b32 a63, v125\n"
      "v_mul_f32 v128, v128, v221\n"
      "v_mfma_f32_16x16x32_bf16 v[204:207], a[204:207], a[108:111], v[204:207]\n"
      "buffer_load_dwordx4 %[vq0], s[80:83], s72 offen nt lds\n"
      "v_accvgpr_write_b32 a16, v32\n"
      "v_mfma_f32_16x16x32_bf16 v[212:215], a[204:207], a[132:135], v[212:215]\n"
      "s_add_u32 m0, %[m0b], 14336\n"
      "v_exp_f32 v144, v144\n"
      "v_mfma_f32_16x16x32_bf16 v[192:195], a[144:147], a[88:91], v[192:195]\n"
      "buffer_load_dwordx4 %[vk2], s[56:59], s70 offen nt lds\n"
      "v_fma_f32 v146, v146, %[csc], v216\n"
      "v_mfma_f32_16x16x32_bf16 v[200:203], a[144:147], a[112:115], v[200:203]\n"
      "s_add_u32 m0, %[m0q], 5120\n"
      "v_exp_f32 v145, v145\n"
      "v_mfma_f32_16x16x32_bf16 v[208:211], a[144:147], a[136:139], v[208:211]\n"
      "buffer_load_dwordx4 %[vq1], s[80:83], s72 offen nt lds\n"
      "v_fma_f32 v147, v147, %[csc], v216\n"
      "v_mfma_f32_16x16x32_bf16 v[196:199], a[148:151], a[88:91], v[196:199]\n"
      "v_exp_f32 v146, v146\n"
      "v_mfma_f32_16x16x32_bf16 v[204:207], a[148:151], a[112:115], v[204:207]\n"
      "v_fma_f32 v148, v148, %[csc], v216\n"
      "v_mul_f32 v129, v129, v221\n"
      "v_mfma_f32_16x16x32_bf16 v[212:215], a[148:151], a[136:139], v[212:215]\n"
      "v_exp_f32 v147, v147\n"
      "v_cvt_pk_bf16_f32 v80, v80, v81\n"
      "v_mul_f32 v130, v130, v221\n"
      "v_cvt_pk_bf16_f32 v81, v82, v83\n"
      "v_mul_f32 v131, v131, v221\n"
      "v_accvgpr_write_b32 a17, v33\n"
      "v_mfma_f32_16x16x32_bf16 v[192:195], a[152:155], a[92:95], v[192:195]\n"
      "v_mul_f32 v36, v36, v219\n"
      "v_cvt_pk_bf16_f32 v144, v144, v145\n"
      "v_mfma_f32_16x16x32_bf16 v[200:203], a[152:155], a[116:119], v[200:203]\n"
      "v_fma_f32 v149, v149, %[csc], v216\n"
      "v_accvgpr_write_b32 a40, v80\n"
      "v_mfma_f32_16x16x32_bf16 v[208:211], a[152:155], a[140:143], v[208:211]\n"
      "s_add_u32 s70, s68, 18948096\n"
      "v_mul_f32 v37, v37, v219\n"
      "v_cvt_pk_bf16_f32 v128, v128, v129\n"
      "v_mfma_f32_16x16x32_bf16 v[196:199], a[156:159], a[92:95], v[196:199]\n"
      "s_add_u32 m0, %[m0b], 24576\n"
      "v_mul_f32 v38, v38, v219\n"
      "v_cvt_pk_bf16_f32 v129, v130, v131\n"
      "v_mul_f32 v39, v39, v219\n"
      "v_accvgpr_write_b32 a41, v81\n"
      "v_mul_f32 v84, v84, v220\n"
      "v_accvgpr_write_b32 a64, v128\n"
      "v_mul_f32 v85, v85, v220\n"
      "v_cvt_pk_bf16_f32 v36, v36, v37\n"
      "v_mul_f32 v86, v86, v220\n"
      "v_cvt_pk_bf16_f32 v37, v38, v39\n"
      "v_mul_f32 v87, v87, v220\n"
      "v_accvgpr_write_b32 a65, v129\n"
      "v_mul_f32 v132, v132, v221\n"
      "v_accvgpr_write_b32 a18, v36\n"
      "v_mul_f32 v133, v133, v221\n"
      "v_cvt_pk_bf16_f32 v84, v84, v85\n"
      "v_mul_f32 v134, v134, v221\n"
      "v_cvt_pk_bf16_f32 v85, v86, v87\n"
      "v_mul_f32 v135, v135, v221\n"
      "v_accvgpr_write_b32 a19, v37\n"
      "v_mfma_f32_16x16x32_bf16 v[204:207], a[156:159], a[116:119], v[204:207]\n"
      "v_exp_f32 v148, v148\n"
      "v_mfma_f32_16x16x32_bf16 v[212:215], a[156:159], a[140:143], v[212:215]\n"
      "v_fma_f32 v150, v150, %[csc], v216\n"
      "v_exp_f32 v149, v149\n"
      "v_cvt_pk_bf16_f32 v145, v146, v147\n"
      "v_fma_f32 v151, v151, %[csc], v216\n"
      "v_exp_f32 v150, v150\n"
      "v_fma_f32 v152, v152, %[csc], v217\n"
      "v_exp_f32 v151, v151\n"
      "v_cvt_pk_bf16_f32 v146, v148, v149\n"
      "v_fma_f32 v153, v153, %[csc], v217\n"
      "v_exp_f32 v152, v152\n"
      "v_fma_f32 v154, v154, %[csc], v217\n"
      "v_exp_f32 v153, v153\n"
      "v_cvt_pk_bf16_f32 v147, v150, v151\n"
      "v_fma_f32 v155, v155, %[csc], v217\n"
      "v_exp_f32 v154, v154\n"
      "v_fma_f32 v156, v156, %[csc], v217\n"
      "v_exp_f32 v155, v155\n"
      "v_cvt_pk_bf16_f32 v152, v152, v153\n"
      "v_fma_f32 v157, v157, %[csc], v217\n"
      "v_exp_f32 v156, v156\n"
      "v_fma_f32 v158, v158, %[csc], v217\n"
      "v_exp_f32 v157, v157\n"
      "v_cvt_pk_bf16_f32 v153, v154, v155\n"
      "v_fma_f32 v159, v159, %[csc], v217\n"
      "v_exp_f32 v158, v158\n"
      "v_fma_f32 v160, v160, %[csc], v218\n"
      "v_exp_f32 v159, v159\n"
      "v_cvt_pk_bf16_f32 v154, v156, v157\n"
      "v_fma_f32 v161, v161, %[csc], v218\n"
      "v_exp_f32 v160, v160\n"
      "v_fma_f32 v162, v162, %[csc], v218\n"
      "v_exp_f32 v161, v161\n"
      "v_cvt_pk_bf16_f32 v155, v158, v159\n"
      "v_fma_f32 v163, v163, %[csc], v218\n"
      "v_exp_f32 v162, v162\n"
      "v_fma_f32 v164, v164, %[csc], v218\n"
      "v_exp_f32 v163, v163\n"
      "v_cvt_pk_bf16_f32 v160, v160, v161\n"
      "v_fma_f32 v165, v165, %[csc], v218\n"
      "v_exp_f32 v164, v164\n"
      "v_fma_f32 v166, v166, %[csc], v218\n"
      "v_exp_f32 v165, v165\n"
      "v_cvt_pk_bf16_f32 v161, v162, v163\n"
      "v_fma_f32 v167, v167, %[csc], v218\n"
      "v_exp_f32 v166, v166\n"
      "v_exp_f32 v167, v167\n"
      "v_cvt_pk_bf16_f32 v162, v164, v165\n"
      "v_cvt_pk_bf16_f32 v163, v166, v167\n"
      "v_mfma_f32_16x16x32_bf16 a[208:211], a[220:223], v[144:147], 0\n"
      "ds_read_b64_tr_b16 a[172:173], %[vb3] offset:24576\n"
      "ds_read_b64_tr_b16 a[174:175], %[vb3] offset:30720\n"
      "v_mfma_f32_16x16x32_bf16 a[212:215], a[220:223], v[152:155], 0\n"
      "buffer_load_dwordx4 %[vk0], s[56:59], s70 offen nt lds\n"
      "v_mul_f32 v40, v40, v219\n"
      "v_accvgpr_write_b32 a42, v84\n"
      "v_mul_f32 v41, v41, v219\n"
      "v_cvt_pk_bf16_f32 v132, v132, v133\n"
      "v_mul_f32 v42, v42, v219\n"
      "v_cvt_pk_bf16_f32 v133, v134, v135\n"
      "v_mul_f32 v43, v43, v219\n"
      "v_accvgpr_write_b32 a43, v85\n"
      "v_mul_f32 v88, v88, v220\n"
      "v_accvgpr_write_b32 a66, v132\n"
      "v_mul_f32 v89, v89, v220\n"
      "v_cvt_pk_bf16_f32 v40, v40, v41\n"
      "v_mul_f32 v90, v90, v220\n"
      "v_cvt_pk_bf16_f32 v41, v42, v43\n"
      "v_mul_f32 v91, v91, v220\n"
      "v_accvgpr_write_b32 a67, v133\n"
      "v_mul_f32 v136, v136, v221\n"
      "v_accvgpr_write_b32 a20, v40\n"
      "v_mul_f32 v137, v137, v221\n"
      "v_cvt_pk_bf16_f32 v88, v88, v89\n"
      "v_mul_f32 v138, v138, v221\n"
      "v_cvt_pk_bf16_f32 v89, v90, v91\n"
      "v_mul_f32 v139, v139, v221\n"
      "v_accvgpr_write_b32 a21, v41\n"
      "v_mfma_f32_16x16x32_bf16 a[216:219], a[220:223], v[160:163], 0\n"
      "s_waitcnt lgkmcnt(2)\n"
      "ds_read_b64_tr_b16 a[176:177], %[vb0] offset:24704\n"
      "ds_read_b64_tr_b16 a[178:179], %[vb0] offset:30848\n"
      "v_mfma_f32_16x16x32_bf16 v[0:3], a[160:163], v[144:147], 0\n"
      "v_mul_f32 v44, v44, v219\n"
      "v_accvgpr_write_b32 a44, v88\n"
      "v_mfma_f32_16x16x32_bf16 v[48:51], a[160:163], v[152:155], 0\n"
      "v_mul_f32 v45, v45, v219\n"
      "v_cvt_pk_bf16_f32 v136, v136, v137\n"
      "v_mul_f32 v46, v46, v219\n"
      "v_cvt_pk_bf16_f32 v137, v138, v139\n"
      "v_mul_f32 v47, v47, v219\n"
      "v_accvgpr_write_b32 a45, v89\n"
      "v_mul_f32 v92, v92, v220\n"
      "v_accvgpr_write_b32 a68, v136\n"
      "v_mul_f32 v93, v93, v220\n"
      "v_cvt_pk_bf16_f32 v44, v44, v45\n"
      "v_mul_f32 v94, v94, v220\n"
      "v_cvt_pk_bf16_f32 v45, v46, v47\n"
      "v_mul_f32 v95, v95, v220\n"
      "v_accvgpr_write_b32 a69, v137\n"
      "v_mul_f32 v140, v140, v221\n"
      "v_accvgpr_write_b32 a22, v44\n"
      "v_mul_f32 v141, v141, v221\n"
      "v_cvt_pk_bf16_f32 v92, v92, v93\n"
      "v_mul_f32 v142, v142, v221\n"
      "v_cvt_pk_bf16_f32 v93, v94, v95\n"
      "v_mul_f32 v143, v143, v221\n"
      "v_accvgpr_write_b32 a23, v45\n"
      "v_mfma_f32_16x16x32_bf16 v[96:99], a[160:163], v[160:163], 0\n"
      "ds_read_b64_tr_b16 a[180:181], %[vb1] offset:24704\n"
      "ds_read_b64_tr_b16 a[182:183], %[vb1] offset:30848\n"
      "v_mfma_f32_16x16x32_bf16 v[4:7], a[164:167], v[144:147], 0\n"
      "s_add_u32 m0, %[m0b], 25600\n"
      "ds_read_b64_tr_b16 a[184:185], %[vb2] offset:24704\n"
      "ds_read_b64_tr_b16 a[186:187], %[vb2] offset:30848\n"
      "v_mfma_f32_16x16x32_bf16 v[52:55], a[164:167], v[152:155], 0\n"
      "ds_read_b64_tr_b16 a[188:189], %[vb3] offset:24704\n"
      "ds_read_b64_tr_b16 a[190:191], %[vb3] offset:30848\n"
      "v_mfma_f32_16x16x32_bf16 v[100:103], a[164:167], v[160:163], 0\n"
      "ds_read_b64_tr_b16 a[192:193], %[vb0] offset:24832\n"
      "ds_read_b64_tr_b16 a[194:195], %[vb0] offset:30976\n"
      "v_mfma_f32_16x16x32_bf16 v[8:11], a[168:171], v[144:147], 0\n"
      "ds_read_b64_tr_b16 a[196:197], %[vb1] offset:24832\n"
      "ds_read_b64_tr_b16 a[198:199], %[vb1] offset:30976\n"
      "v_mfma_f32_16x16x32_bf16 v[56:59], a[168:171], v[152:155], 0\n"
      "buffer_load_dwordx4 %[vk1], s[56:59], s70 offen nt lds\n"
      "v_accvgpr_write_b32 a46, v92\n"
      "v_mfma_f32_16x16x32_bf16 v[104:107], a[168:171], v[160:163], 0\n"
      "s_waitcnt lgkmcnt(10)\n"
      "ds_read_b64_tr_b16 a[200:201], %[vb2] offset:24832\n"
      "ds_read_b64_tr_b16 a[202:203], %[vb2] offset:30976\n"
      "v_mfma_f32_16x16x32_bf16 v[12:15], a[172:175], v[144:147], 0\n"
      "ds_read_b64_tr_b16 a[204:205], %[vb3] offset:24832\n"
      "ds_read_b64_tr_b16 a[206:207], %[vb3] offset:30976\n"
      "v_mfma_f32_16x16x32_bf16 v[60:63], a[172:175], v[152:155], 0\n"
      "ds_read_b64_tr_b16 a[144:145], %[vb0] offset:36864\n"
      "ds_read_b64_tr_b16 a[146:147], %[vb0] offset:43008\n"
      "v_mfma_f32_16x16x32_bf16 v[108:111], a[172:175], v[160:163], 0\n"
      "ds_read_b64_tr_b16 a[148:149], %[vb1] offset:36864\n"
      "ds_read_b64_tr_b16 a[150:151], %[vb1] offset:43008\n"
      "v_mfma_f32_16x16x32_bf16 v[16:19], a[176:179], v[144:147], 0\n"
      "v_cvt_pk_bf16_f32 v140, v140, v141\n"
      "v_cvt_pk_bf16_f32 v141, v142, v143\n"
      "v_mfma_f32_16x16x32_bf16 v[64:67], a[176:179], v[152:155], 0\n"
      "s_add_u32 m0, %[m0b], 26624\n"
      "v_accvgpr_write_b32 a47, v93\n"
      "v_accvgpr_write_b32 a70, v140\n"
      "v_mfma_f32_16x16x32_bf16 v[112:115], a[176:179], v[160:163], 0\n"
      "s_waitcnt lgkmcnt(10)\n"
      "buffer_load_dwordx4 %[vk2], s[56:59], s70 offen nt lds\n"
      "v_accvgpr_write_b32 a71, v141\n"
      "v_mfma_f32_16x16x32_bf16 v[20:23], a[180:183], v[144:147], 0\n"
      "ds_read_b64_tr_b16 a[152:153], %[vb2] offset:36864\n"
      "ds_read_b64_tr_b16 a[154:155], %[vb2] offset:43008\n"
      "v_mfma_f32_16x16x32_bf16 v[68:71], a[180:183], v[152:155], 0\n"
      "ds_write_b64 %[ow], a[0:1] offset:0\n"
      "v_fma_f32 v168, v168, %[csc], v216\n"
      "v_mfma_f32_16x16x32_bf16 v[116:119], a[180:183], v[160:163], 0\n"
      "ds_write_b64 %[ow], a[2:3] offset:32\n"
      "v_fma_f32 v169, v169, %[csc], v216\n"
      "v_mfma_f32_16x16x32_bf16 v[24:27], a[184:187], v[144:147], 0\n"
      "ds_write_b64 %[ow], a[4:5] offset:64\n"
      "v_mfma_f32_16x16x32_bf16 v[72:75], a[184:187], v[152:155], 0\n"
      "ds_write_b64 %[ow], a[6:7] offset:96\n"
      "v_mfma_f32_16x16x32_bf16 v[120:123], a[184:187], v[160:163], 0\n"
      "v_exp_f32 v168, v168\n"
      "v_mfma_f32_16x16x32_bf16 v[28:31], a[188:191], v[144:147], 0\n"
      "ds_read_b128 a[224:227], %[orb] offset:0\n"
      "v_fma_f32 v170, v170, %[csc], v216\n"
      "v_mfma_f32_16x16x32_bf16 v[76:79], a[188:191], v[152:155], 0\n"
      "v_exp_f32 v169, v169\n"
      "v_mfma_f32_16x16x32_bf16 v[124:127], a[188:191], v[160:163], 0\n"
      "ds_read_b128 a[228:231], %[orb] offset:1152\n"
      "v_fma_f32 v171, v171, %[csc], v216\n"
      "v_mfma_f32_16x16x32_bf16 v[32:35], a[192:195], v[144:147], 0\n"
      "s_waitcnt lgkmcnt(1)\n"
      "v_exp_f32 v170, v170\n"
      "v_mfma_f32_16x16x32_bf16 v[80:83], a[192:195], v[152:155], 0\n"
      "s_add_u32 s73, s69, 0\n"
      "buffer_store_dwordx4 a[224:227], %[ost], s[52:55], s73 offen offset:0 sc1\n"
      "v_fma_f32 v172, v172, %[csc], v216\n"
      "v_mfma_f32_16x16x32_bf16 v[128:131], a[192:195], v[160:163], 0\n"
      "v_exp_f32 v171, v171\n"
      "v_mfma_f32_16x16x32_bf16 v[36:39], a[196:199], v[144:147], 0\n"
      "v_cvt_pk_bf16_f32 v168, v168, v169\n"
      "v_fma_f32 v173, v173, %[csc], v216\n"
      "v_mfma_f32_16x16x32_bf16 v[84:87], a[196:199], v[152:155], 0\n"
      "s_waitcnt lgkmcnt(0)\n"
      "v_exp_f32 v172, v172\n"
      "v_mfma_f32_16x16x32_bf16 v[132:135], a[196:199], v[160:163], 0\n"
      "s_add_u32 s73, s69, 3072\n"
      "buffer_store_dwordx4 a[228:231], %[ost], s[52:55], s73 offen offset:0 sc1\n"
      "v_fma_f32 v174, v174, %[csc], v216\n"
      "v_mfma_f32_16x16x32_bf16 v[40:43], a[200:203], v[144:147], 0\n"
      "v_exp_f32 v173, v173\n"
      "v_mfma_f32_16x16x32_bf16 v[88:91], a[200:203], v[152:155], 0\n"
      "v_cvt_pk_bf16_f32 v169, v170, v171\n"
      "v_fma_f32 v175, v175, %[csc], v216\n"
      "v_mfma_f32_16x16x32_bf16 v[136:139], a[200:203], v[160:163], 0\n"
      "v_exp_f32 v174, v174\n"
      "v_mfma_f32_16x16x32_bf16 v[44:47], a[204:207], v[144:147], 0\n"
      "v_fma_f32 v176, v176, %[csc], v217\n"
      "v_mfma_f32_16x16x32_bf16 v[92:95], a[204:207], v[152:155], 0\n"
      "v_exp_f32 v175, v175\n"
      "v_mfma_f32_16x16x32_bf16 v[140:143], a[204:207], v[160:163], 0\n"
      "v_cvt_pk_bf16_f32 v170, v172, v173\n"
      "v_fma_f32 v177, v177, %[csc], v217\n"
      "v_exp_f32 v176, v176\n"
      "v_fma_f32 v178, v178, %[csc], v217\n"
      "v_exp_f32 v177, v177\n"
      "v_cvt_pk_bf16_f32 v171, v174, v175\n"
      "v_fma_f32 v179, v179, %[csc], v217\n"
      "v_exp_f32 v178, v178\n"
      "v_fma_f32 v180, v180, %[csc], v217\n"
      "v_exp_f32 v179, v179\n"
      "v_cvt_pk_bf16_f32 v176, v176, v177\n"
      "v_fma_f32 v181, v181, %[csc], v217\n"
      "v_exp_f32 v180, v180\n"
      "v_fma_f32 v182, v182, %[csc], v217\n"
      "v_exp_f32 v181, v181\n"
      "v_cvt_pk_bf16_f32 v177, v178, v179\n"
      "v_fma_f32 v183, v183, %[csc], v217\n"
      "v_exp_f32 v182, v182\n"
      "v_fma_f32 v184, v184, %[csc], v218\n"
      "v_exp_f32 v183, v183\n"
      "v_cvt_pk_bf16_f32 v178, v180, v181\n"
      "v_fma_f32 v185, v185, %[csc], v218\n"
      "v_exp_f32 v184, v184\n"
      "v_fma_f32 v186, v186, %[csc], v218\n"
      "v_exp_f32 v185, v185\n"
      "v_cvt_pk_bf16_f32 v179, v182, v183\n"
      "v_fma_f32 v187, v187, %[csc], v218\n"
      "v_exp_f32 v186, v186\n"
      "v_fma_f32 v188, v188, %[csc], v218\n"
      "v_exp_f32 v187, v187\n"
      "v_cvt_pk_bf16_f32 v184, v184, v185\n"
      "v_fma_f32 v189, v189, %[csc], v218\n"
      "v_exp_f32 v188, v188\n"
      "v_fma_f32 v190, v190, %[csc], v218\n"
      "v_exp_f32 v189, v189\n"
      "v_cvt_pk_bf16_f32 v185, v186, v187\n"
      "v_fma_f32 v191, v191, %[csc], v218\n"
      "v_exp_f32 v190, v190\n"
      "v_exp_f32 v191, v191\n"
      "v_cvt_pk_bf16_f32 v186, v188, v189\n"
      "v_cvt_pk_bf16_f32 v187, v190, v191\n"
      "v_mfma_f32_16x16x32_bf16 a[208:211], a[220:223], v[168:171], a[208:211]\n"
      "ds_read_b64_tr_b16 a[156:157], %[vb3] offset:36864\n"
      "ds_read_b64_tr_b16 a[158:159], %[vb3] offset:43008\n"
      "v_mfma_f32_16x16x32_bf16 a[212:215], a[220:223], v[176:179], a[212:215]\n"
      "ds_read_b64_tr_b16 a[160:161], %[vb0] offset:36992\n"
      "ds_read_b64_tr_b16 a[162:163], %[vb0] offset:43136\n"
      "v_mfma_f32_16x16x32_bf16 a[216:219], a[220:223], v[184:187], a[216:219]\n"
      "ds_read_b64_tr_b16 a[164:165], %[vb1] offset:36992\n"
      "ds_read_b64_tr_b16 a[166:167], %[vb1] offset:43136\n"
      "v_mfma_f32_16x16x32_bf16 v[0:3], a[144:147], v[168:171], v[0:3]\n"
      "ds_read_b64_tr_b16 a[168:169], %[vb2] offset:36992\n"
      "ds_read_b64_tr_b16 a[170:171], %[vb2] offset:43136\n"
      "v_mfma_f32_16x16x32_bf16 v[48:51], a[144:147], v[176:179], v[48:51]\n"
      "s_waitcnt vmcnt(24)\n"
      "ds_read_b64_tr_b16 a[172:173], %[vb3] offset:36992\n"
      "ds_read_b64_tr_b16 a[174:175], %[vb3] offset:43136\n"
      "v_mfma_f32_16x16x32_bf16 v[96:99], a[144:147], v[184:187], v[96:99]\n"
      "ds_read_b64_tr_b16 a[176:177], %[vb0] offset:37120\n"
      "ds_read_b64_tr_b16 a[178:179], %[vb0] offset:43264\n"
      "v_mfma_f32_16x16x32_bf16 v[4:7], a[148:151], v[168:171], v[4:7]\n"
      "ds_read_b64_tr_b16 a[180:181], %[vb1] offset:37120\n"
      "ds_read_b64_tr_b16 a[182:183], %[vb1] offset:43264\n"
      "v_mfma_f32_16x16x32_bf16 v[52:55], a[148:151], v[176:179], v[52:55]\n"
      "ds_read_b64_tr_b16 a[184:185], %[vb2] offset:37120\n"
      "ds_read_b64_tr_b16 a[186:187], %[vb2] offset:43264\n"
      "v_mfma_f32_16x16x32_bf16 v[100:103], a[148:151], v[184:187], v[100:103]\n"
      "ds_read_b64_tr_b16 a[188:189], %[vb3] offset:37120\n"
      "ds_read_b64_tr_b16 a[190:191], %[vb3] offset:43264\n"
      "v_mfma_f32_16x16x32_bf16 v[8:11], a[152:155], v[168:171], v[8:11]\n"
      "ds_read_b128 a[0:3], %[qs0] offset:0\n"
      "ds_read_b128 a[4:7], %[qs1] offset:0\n"
      "v_mfma_f32_16x16x32_bf16 v[56:59], a[152:155], v[176:179], v[56:59]\n"
      "v_fma_f32 v192, v192, %[csc], v216\n"
      "v_fma_f32 v193, v193, %[csc], v216\n"
      "v_mfma_f32_16x16x32_bf16 v[104:107], a[152:155], v[184:187], v[104:107]\n"
      "s_waitcnt lgkmcnt(14)\n"
      "v_exp_f32 v192, v192\n"
      "v_mfma_f32_16x16x32_bf16 v[12:15], a[156:159], v[168:171], v[12:15]\n"
      "v_fma_f32 v194, v194, %[csc], v216\n"
      "v_mfma_f32_16x16x32_bf16 v[60:63], a[156:159], v[176:179], v[60:63]\n"
      "v_exp_f32 v193, v193\n"
      "s_waitcnt vmcnt(18) lgkmcnt(0)\n"
      "s_barrier\n"
      "v_mfma_f32_16x16x32_bf16 v[108:111], a[156:159], v[184:187], v[108:111]\n"
      "s_add_u32 s71, s68, 18923520\n"
      "ds_read_b128 a[192:195], %[kb01] offset:0\n"
      "ds_read_b128 a[196:199], %[kb01] offset:6144\n"
      "v_mfma_f32_16x16x32_bf16 v[16:19], a[160:163], v[168:171], v[16:19]\n"
      "s_add_u32 m0, %[m0b], 98304\n"
      "ds_read_b128 a[200:203], %[kb11] offset:0\n"
      "ds_read_b128 a[204:207], %[kb11] offset:6144\n"
      "v_mfma_f32_16x16x32_bf16 v[64:67], a[160:163], v[176:179], v[64:67]\n"
      "buffer_load_dwordx4 %[vv0], s[60:63], s71 offen nt lds\n"
      "ds_read_b128 a[144:147], %[kb01] offset:128\n"
      "v_mfma_f32_16x16x32_bf16 v[112:115], a[160:163], v[184:187], v[112:115]\n"
      "ds_read_b128 a[148:151], %[kb01] offset:6272\n"
      "ds_read_b128 a[152:155], %[kb11] offset:128\n"
      "v_mfma_f32_16x16x32_bf16 v[20:23], a[164:167], v[168:171], v[20:23]\n"
      "ds_read_b128 a[156:159], %[kb11] offset:6272\n"
      "ds_read_b128 a[160:163], %[kb01] offset:256\n"
      "v_mfma_f32_16x16x32_bf16 v[68:71], a[164:167], v[176:179], v[68:71]\n"
      "v_fma_f32 v195, v195, %[csc], v216\n"
      "v_mfma_f32_16x16x32_bf16 v[116:119], a[164:167], v[184:187], v[116:119]\n"
      "ds_read_b128 a[164:167], %[kb01] offset:6400\n"
      "v_mfma_f32_16x16x32_bf16 v[24:27], a[168:171], v[168:171], v[24:27]\n"
      "v_exp_f32 v194, v194\n"
      "v_mfma_f32_16x16x32_bf16 v[72:75], a[168:171], v[176:179], v[72:75]\n"
      "v_fma_f32 v196, v196, %[csc], v216\n"
      "v_mfma_f32_16x16x32_bf16 v[120:123], a[168:171], v[184:187], v[120:123]\n"
      "ds_read_b128 a[168:171], %[kb11] offset:256\n"
      "v_mfma_f32_16x16x32_bf16 v[28:31], a[172:175], v[168:171], v[28:31]\n"
      "s_add_u32 m0, %[m0b], 99328\n"
      "v_exp_f32 v195, v195\n"
      "v_mfma_f32_16x16x32_bf16 v[76:79], a[172:175], v[176:179], v[76:79]\n"
      "buffer_load_dwordx4 %[vv1], s[60:63], s71 offen nt lds\n"
      "v_cvt_pk_bf16_f32 v192, v192, v193\n"
      "v_mfma_f32_16x16x32_bf16 v[124:127], a[172:175], v[184:187], v[124:127]\n"
      "ds_read_b128 a[172:175], %[kb11] offset:6400\n"
      "v_fma_f32 v197, v197, %[csc], v216\n"
      "v_mfma_f32_16x16x32_bf16 v[32:35], a[176:179], v[168:171], v[32:35]\n"
      "v_exp_f32 v196, v196\n"
      "v_mfma_f32_16x16x32_bf16 v[80:83], a[176:179], v[176:179], v[80:83]\n"
      "v_fma_f32 v198, v198, %[csc], v216\n"
      "v_mfma_f32_16x16x32_bf16 v[128:131], a[176:179], v[184:187], v[128:131]\n"
      "ds_read_b64_tr_b16 a[176:177], %[vb0] offset:0\n"
      "ds_read_b64_tr_b16 a[178:179], %[vb0] offset:6144\n"
      "v_mfma_f32_16x16x32_bf16 v[36:39], a[180:183], v[168:171], v[36:39]\n"
      "s_add_u32 s72, s69, 37748864\n"
      "v_exp_f32 v197, v197\n"
      "v_mfma_f32_16x16x32_bf16 v[84:87], a[180:183], v[176:179], v[84:87]\n"
      "s_add_u32 m0, %[m0q], 0\n"
      "v_cvt_pk_bf16_f32 v193, v194, v195\n"
      "v_fma_f32 v199, v199, %[csc], v216\n"
      "v_mfma_f32_16x16x32_bf16 v[132:135], a[180:183], v[184:187], v[132:135]\n"
      "buffer_load_dwordx4 %[vq0], s[80:83], s72 offen nt lds\n"
      "v_mfma_f32_16x16x32_bf16 v[40:43], a[184:187], v[168:171], v[40:43]\n"
      "s_add_u32 m0, %[m0q], 1024\n"
      "ds_read_b64_tr_b16 a[180:181], %[vb1] offset:0\n"
      "ds_read_b64_tr_b16 a[182:183], %[vb1] offset:6144\n"
      "v_mfma_f32_16x16x32_bf16 v[88:91], a[184:187], v[176:179], v[88:91]\n"
      "buffer_load_dwordx4 %[vq1], s[80:83], s72 offen nt lds\n"
      "v_mfma_f32_16x16x32_bf16 v[136:139], a[184:187], v[184:187], v[136:139]\n"
      "s_add_u32 m0, %[m0b], 100352\n"
      "ds_read_b64_tr_b16 a[184:185], %[vb2] offset:0\n"
      "ds_read_b64_tr_b16 a[186:187], %[vb2] offset:6144\n"
      "v_mfma_f32_16x16x32_bf16 v[44:47], a[188:191], v[168:171], v[44:47]\n"
      "buffer_load_dwordx4 %[vv2], s[60:63], s71 offen nt lds\n"
      "v_mfma_f32_16x16x32_bf16 v[92:95], a[188:191], v[176:179], v[92:95]\n"
      "v_exp_f32 v198, v198\n"
      "v_mfma_f32_16x16x32_bf16 v[140:143], a[188:191], v[184:187], v[140:143]\n"
      "s_waitcnt lgkmcnt(14)\n"
      "v_fma_f32 v200, v200, %[csc], v217\n"
      "s_add_u32 s71, s68, 18935808\n"
      "s_add_u32 m0, %[m0b], 110592\n"
      "s_nop 0\n"
      "buffer_load_dwordx4 %[vv0], s[60:63], s71 offen nt lds\n"
      "s_add_u32 m0, %[m0b], 111616\n"
      "s_nop 0\n"
      "buffer_load_dwordx4 %[vv1], s[60:63], s71 offen nt lds\n"
      "s_add_u32 m0, %[m0b], 112640\n"
      "s_nop 0\n"
      "buffer_load_dwordx4 %[vv2], s[60:63], s71 offen nt lds\n"
      "s_waitcnt lgkmcnt(0)\n"
      "s_add_u32 s68, s68, 18874368\n"
      "s_add_u32 s69, s69, 18874368\n"
      "s_cmp_eq_u32 s74, 0\n"
      "s_cselect_b32 s58, 75497472, 0\n"
      "s_mov_b32 s62, s58\n"
      "s_mov_b32 s66, s58\n"
      "s_mov_b32 s86, 75497472\n"
      "v_mfma_f32_16x16x32_bf16 v[144:147], a[192:195], a[72:75], 0\n"
      "v_exp_f32 v199, v199\n"
      "v_mfma_f32_16x16x32_bf16 v[152:155], a[192:195], a[96:99], 0\n"
      "v_cvt_pk_bf16_f32 v194, v196, v197\n"
      "v_fma_f32 v201, v201, %[csc], v217\n"
      "v_mfma_f32_16x16x32_bf16 v[160:163], a[192:195], a[120:123], 0\n"
      "v_exp_f32 v200, v200\n"
      "v_mfma_f32_16x16x32_bf16 v[148:151], a[196:199], a[72:75], 0\n"
      "ds_write_b64 %[ow], a[24:25] offset:0\n"
      "v_fma_f32 v202, v202, %[csc], v217\n"
      "v_mfma_f32_16x16x32_bf16 v[156:159], a[196:199], a[96:99], 0\n"
      "v_exp_f32 v201, v201\n"
      "v_mfma_f32_16x16x32_bf16 v[164:167], a[196:199], a[120:123], 0\n"
      "v_cvt_pk_bf16_f32 v195, v198, v199\n"
      "v_fma_f32 v203, v203, %[csc], v217\n"
      "v_mfma_f32_16x16x32_bf16 v[144:147], a[200:203], a[76:79], v[144:147]\n"
      "v_exp_f32 v202, v202\n"
      "v_mfma_f32_16x16x32_bf16 v[152:155], a[200:203], a[100:103], v[152:155]\n"
      "ds_write_b64 %[ow], a[26:27] offset:32\n"
      "v_fma_f32 v204, v204, %[csc], v217\n"
      "v_mfma_f32_16x16x32_bf16 v[160:163], a[200:203], a[124:127], v[160:163]\n"
      "v_exp_f32 v203, v203\n"
      "v_mfma_f32_16x16x32_bf16 v[148:151], a[204:207], a[76:79], v[148:151]\n"
      "v_cvt_pk_bf16_f32 v200, v200, v201\n"
      "v_fma_f32 v205, v205, %[csc], v217\n"
      "v_mfma_f32_16x16x32_bf16 v[156:159], a[204:207], a[100:103], v[156:159]\n"
      "v_exp_f32 v204, v204\n"
      "v_mfma_f32_16x16x32_bf16 v[164:167], a[204:207], a[124:127], v[164:167]\n"
      "ds_write_b64 %[ow], a[28:29] offset:64\n"
      "v_fma_f32 v206, v206, %[csc], v217\n"
      "v_mfma_f32_16x16x32_bf16 v[144:147], a[144:147], a[80:83], v[144:147]\n"
      "v_exp_f32 v205, v205\n"
      "v_mfma_f32_16x16x32_bf16 v[152:155], a[144:147], a[104:107], v[152:155]\n"
      "v_cvt_pk_bf16_f32 v201, v202, v203\n"
      "v_fma_f32 v207, v207, %[csc], v217\n"
      "v_mfma_f32_16x16x32_bf16 v[160:163], a[144:147], a[128:131], v[160:163]\n"
      "v_exp_f32 v206, v206\n"
      "v_mfma_f32_16x16x32_bf16 v[148:151], a[148:151], a[80:83], v[148:151]\n"
      "ds_write_b64 %[ow], a[30:31] offset:96\n"
      "v_fma_f32 v208, v208, %[csc], v218\n"
      "v_mfma_f32_16x16x32_bf16 v[156:159], a[148:151], a[104:107], v[156:159]\n"
      "v_exp_f32 v207, v207\n"
      "v_mfma_f32_16x16x32_bf16 v[164:167], a[148:151], a[128:131], v[164:167]\n"
      "v_cvt_pk_bf16_f32 v202, v204, v205\n"
      "v_fma_f32 v209, v209, %[csc], v218\n"
      "v_mfma_f32_16x16x32_bf16 v[144:147], a[152:155], a[84:87], v[144:147]\n"
      "v_exp_f32 v208, v208\n"
      "v_mfma_f32_16x16x32_bf16 v[152:155], a[152:155], a[108:111], v[152:155]\n"
      "ds_read_b128 a[232:235], %[orb] offset:0\n"
      "v_fma_f32 v210, v210, %[csc], v218\n"
      "v_mfma_f32_16x16x32_bf16 v[160:163], a[152:155], a[132:135], v[160:163]\n"
      "v_exp_f32 v209, v209\n"
      "v_mfma_f32_16x16x32_bf16 v[148:151], a[156:159], a[84:87], v[148:151]\n"
      "v_cvt_pk_bf16_f32 v203, v206, v207\n"
      "v_fma_f32 v211, v211, %[csc], v218\n"
      "v_mfma_f32_16x16x32_bf16 v[156:159], a[156:159], a[108:111], v[156:159]\n"
      "v_exp_f32 v210, v210\n"
      "v_mfma_f32_16x16x32_bf16 v[164:167], a[156:159], a[132:135], v[164:167]\n"
      "ds_read_b128 a[236:239], %[orb] offset:1152\n"
      "v_fma_f32 v212, v212, %[csc], v218\n"
      "v_mfma_f32_16x16x32_bf16 v[144:147], a[160:163], a[88:91], v[144:147]\n"
      "v_exp_f32 v211, v211\n"
      "v_mfma_f32_16x16x32_bf16 v[152:155], a[160:163], a[112:115], v[152:155]\n"
      "v_cvt_pk_bf16_f32 v208, v208, v209\n"
      "v_fma_f32 v213, v213, %[csc], v218\n"
      "v_mfma_f32_16x16x32_bf16 v[160:163], a[160:163], a[136:139], v[160:163]\n"
      "s_waitcnt lgkmcnt(1)\n"
      "v_exp_f32 v212, v212\n"
      "v_mfma_f32_16x16x32_bf16 v[148:151], a[164:167], a[88:91], v[148:151]\n"
      "s_add_u32 s73, s69, 4276099072\n"
      "buffer_store_dwordx4 a[232:235], %[ost], s[84:87], s73 offen offset:0 sc1\n"
      "v_fma_f32 v214, v214, %[csc], v218\n"
      "v_mfma_f32_16x16x32_bf16 v[156:159], a[164:167], a[112:115], v[156:159]\n"
      "v_exp_f32 v213, v213\n"
      "v_mfma_f32_16x16x32_bf16 v[164:167], a[164:167], a[136:139], v[164:167]\n"
      "v_cvt_pk_bf16_f32 v209, v210, v211\n"
      "v_fma_f32 v215, v215, %[csc], v218\n"
      "v_mfma_f32_16x16x32_bf16 v[144:147], a[168:171], a[92:95], v[144:147]\n"
      "v_exp_f32 v214, v214\n"
      "v_mfma_f32_16x16x32_bf16 v[152:155], a[168:171], a[116:119], v[152:155]\n"
      "v_exp_f32 v215, v215\n"
      "v_mfma_f32_16x16x32_bf16 v[160:163], a[168:171], a[140:143], v[160:163]\n"
      "v_cvt_pk_bf16_f32 v210, v212, v213\n"
      "v_mfma_f32_16x16x32_bf16 v[148:151], a[172:175], a[92:95], v[148:151]\n"
      "v_cvt_pk_bf16_f32 v211, v214, v215\n"
      "v_mfma_f32_16x16x32_bf16 v[156:159], a[172:175], a[116:119], v[156:159]\n"
      "ds_read_b64_tr_b16 a[188:189], %[vb3] offset:0\n"
      "ds_read_b64_tr_b16 a[190:191], %[vb3] offset:6144\n"
      "v_mfma_f32_16x16x32_bf16 v[164:167], a[172:175], a[140:143], v[164:167]\n"
      "ds_read_b64_tr_b16 a[192:193], %[vb0] offset:128\n"
      "ds_read_b64_tr_b16 a[194:195], %[vb0] offset:6272\n"
      "v_mfma_f32_16x16x32_bf16 a[208:211], a[220:223], v[192:195], a[208:211]\n"
      "ds_read_b64_tr_b16 a[196:197], %[vb1] offset:128\n"
      "ds_read_b64_tr_b16 a[198:199], %[vb1] offset:6272\n"
      "v_mfma_f32_16x16x32_bf16 a[212:215], a[220:223], v[200:203], a[212:215]\n"
      "ds_read_b64_tr_b16 a[200:201], %[vb2] offset:128\n"
      "ds_read_b64_tr_b16 a[202:203], %[vb2] offset:6272\n"
      "v_mfma_f32_16x16x32_bf16 a[216:219], a[220:223], v[208:211], a[216:219]\n"
      "s_waitcnt lgkmcnt(8)\n"
      "ds_read_b64_tr_b16 a[204:205], %[vb3] offset:128\n"
      "ds_read_b64_tr_b16 a[206:207], %[vb3] offset:6272\n"
      "v_mfma_f32_16x16x32_bf16 v[0:3], a[176:179], v[192:195], v[0:3]\n"
      "s_add_u32 s73, s69, 4276102144\n"
      "ds_read_b64_tr_b16 a[144:145], %[vb0] offset:256\n"
      "ds_read_b64_tr_b16 a[146:147], %[vb0] offset:6400\n"
      "v_mfma_f32_16x16x32_bf16 v[48:51], a[176:179], v[200:203], v[48:51]\n"
      "ds_read_b64_tr_b16 a[148:149], %[vb1] offset:256\n"
      "ds_read_b64_tr_b16 a[150:151], %[vb1] offset:6400\n"
      "v_mfma_f32_16x16x32_bf16 v[96:99], a[176:179], v[208:211], v[96:99]\n"
      "ds_read_b64_tr_b16 a[152:153], %[vb2] offset:256\n"
      "ds_read_b64_tr_b16 a[154:155], %[vb2] offset:6400\n"
      "v_mfma_f32_16x16x32_bf16 v[4:7], a[180:183], v[192:195], v[4:7]\n"
      "buffer_store_dwordx4 a[236:239], %[ost], s[84:87], s73 offen offset:0 sc1\n"
      "v_fma_f32 v144, v144, %[csc], v216\n"
      "v_mfma_f32_16x16x32_bf16 v[52:55], a[180:183], v[200:203], v[52:55]\n"
      "ds_read_b64_tr_b16 a[156:157], %[vb3] offset:256\n"
      "ds_read_b64_tr_b16 a[158:159], %[vb3] offset:6400\n"
      "v_mfma_f32_16x16x32_bf16 v[100:103], a[180:183], v[208:211], v[100:103]\n"
      "v_fma_f32 v145, v145, %[csc], v216\n"
      "v_mfma_f32_16x16x32_bf16 v[8:11], a[184:187], v[192:195], v[8:11]\n"
      "v_exp_f32 v144, v144\n"
      "v_mfma_f32_16x16x32_bf16 v[56:59], a[184:187], v[200:203], v[56:59]\n"
      "v_fma_f32 v146, v146, %[csc], v216\n"
      "v_mfma_f32_16x16x32_bf16 v[104:107], a[184:187], v[208:211], v[104:107]\n"
      "s_waitcnt lgkmcnt(10)\n"
      "v_exp_f32 v145, v145\n"
      "v_mfma_f32_16x16x32_bf16 v[12:15], a[188:191], v[192:195], v[12:15]\n"
      "v_fma_f32 v147, v147, %[csc], v216\n"
      "s_waitcnt vmcnt(20) lgkmcnt(0)\n"
      "s_barrier\n"
      "v_mfma_f32_16x16x32_bf16 v[60:63], a[188:191], v[200:203], v[60:63]\n"
      "s_add_u32 s70, s68, 86016\n"
      "ds_read_b128 a[160:163], %[kb00] offset:0\n"
      "ds_read_b128 a[164:167], %[kb00] offset:6144\n"
      "v_mfma_f32_16x16x32_bf16 v[108:111], a[188:191], v[208:211], v[108:111]\n"
      "s_add_u32 m0, %[m0b], 36864\n"
      "ds_read_b128 a[168:171], %[kb10] offset:0\n"
      "ds_read_b128 a[172:175], %[kb10] offset:6144\n"
      "v_mfma_f32_16x16x32_bf16 v[16:19], a[192:195], v[192:195], v[16:19]\n"
      "s_waitcnt vmcnt(25)\n"
      "buffer_load_dwordx4 %[vk0], s[40:43], s70 offen nt lds\n"
      "ds_read_b128 a[24:27], %[qs0] offset:2048\n"
      "v_mfma_f32_16x16x32_bf16 v[64:67], a[192:195], v[200:203], v[64:67]\n"
      "ds_read_b128 a[28:31], %[qs1] offset:2048\n"
      "ds_read_b128 a[176:179], %[kb00] offset:128\n"
      "v_mfma_f32_16x16x32_bf16 v[112:115], a[192:195], v[208:211], v[112:115]\n"
      "ds_read_b128 a[180:183], %[kb00] offset:6272\n"
      "ds_read_b128 a[184:187], %[kb10] offset:128\n"
      "v_mfma_f32_16x16x32_bf16 v[20:23], a[196:199], v[192:195], v[20:23]\n"
      "ds_read_b128 a[188:191], %[kb10] offset:6272\n"
      "ds_read_b128 a[192:195], %[kb00] offset:256\n"
      "v_mfma_f32_16x16x32_bf16 v[68:71], a[196:199], v[200:203], v[68:71]\n"
      "v_exp_f32 v146, v146\n"
      "v_mfma_f32_16x16x32_bf16 v[116:119], a[196:199], v[208:211], v[116:119]\n"
      "ds_read_b128 a[196:199], %[kb00] offset:6400\n"
      "v_fma_f32 v148, v148, %[csc], v216\n"
      "v_mfma_f32_16x16x32_bf16 v[24:27], a[200:203], v[192:195], v[24:27]\n"
      "v_exp_f32 v147, v147\n"
      "v_mfma_f32_16x16x32_bf16 v[72:75], a[200:203], v[200:203], v[72:75]\n"
      "v_cvt_pk_bf16_f32 v144, v144, v145\n"
      "v_fma_f32 v149, v149, %[csc], v216\n"
      "v_mfma_f32_16x16x32_bf16 v[120:123], a[200:203], v[208:211], v[120:123]\n"
      "s_add_u32 m0, %[m0b], 37888\n"
      "ds_read_b128 a[200:203], %[kb10] offset:256\n"
      "v_mfma_f32_16x16x32_bf16 v[28:31], a[204:207], v[192:195], v[28:31]\n"
      "buffer_load_dwordx4 %[vk1], s[40:43], s70 offen nt lds\n"
      "v_mfma_f32_16x16x32_bf16 v[76:79], a[204:207], v[200:203], v[76:79]\n"
      "v_exp_f32 v148, v148\n"
      "v_mfma_f32_16x16x32_bf16 v[124:127], a[204:207], v[208:211], v[124:127]\n"
      "ds_read_b128 a[204:207], %[kb10] offset:6400\n"
      "v_fma_f32 v150, v150, %[csc], v216\n"
      "v_mfma_f32_16x16x32_bf16 v[32:35], a[144:147], v[192:195], v[32:35]\n"
      "v_exp_f32 v149, v149\n"
      "v_mfma_f32_16x16x32_bf16 v[80:83], a[144:147], v[200:203], v[80:83]\n"
      "v_cvt_pk_bf16_f32 v145, v146, v147\n"
      "v_fma_f32 v151, v151, %[csc], v216\n"
      "v_mfma_f32_16x16x32_bf16 v[128:131], a[144:147], v[208:211], v[128:131]\n"
      "ds_read_b64_tr_b16 a[144:145], %[vb0] offset:12288\n"
      "ds_read_b64_tr_b16 a[146:147], %[vb0] offset:18432\n"
      "v_mfma_f32_16x16x32_bf16 v[36:39], a[148:151], v[192:195], v[36:39]\n"
      "v_exp_f32 v150, v150\n"
      "v_mfma_f32_16x16x32_bf16 v[84:87], a[148:151], v[200:203], v[84:87]\n"
      "v_fma_f32 v152, v152, %[csc], v217\n"
      "v_mfma_f32_16x16x32_bf16 v[132:135], a[148:151], v[208:211], v[132:135]\n"
      "ds_read_b64_tr_b16 a[148:149], %[vb1] offset:12288\n"
      "ds_read_b64_tr_b16 a[150:151], %[vb1] offset:18432\n"
      "v_mfma_f32_16x16x32_bf16 v[40:43], a[152:155], v[192:195], v[40:43]\n"
      "s_add_u32 m0, %[m0b], 38912\n"
      "v_exp_f32 v151, v151\n"
      "v_mfma_f32_16x16x32_bf16 v[88:91], a[152:155], v[200:203], v[88:91]\n"
      "buffer_load_dwordx4 %[vk2], s[40:43], s70 offen nt lds\n"
      "v_cvt_pk_bf16_f32 v146, v148, v149\n"
      "v_mfma_f32_16x16x32_bf16 v[136:139], a[152:155], v[208:211], v[136:139]\n"
      "ds_read_b64_tr_b16 a[152:153], %[vb2] offset:12288\n"
      "ds_read_b64_tr_b16 a[154:155], %[vb2] offset:18432\n"
      "v_mfma_f32_16x16x32_bf16 v[44:47], a[156:159], v[192:195], v[44:47]\n"
      "v_fma_f32 v153, v153, %[csc], v217\n"
      "v_mfma_f32_16x16x32_bf16 v[92:95], a[156:159], v[200:203], v[92:95]\n"
      "v_exp_f32 v152, v152\n"
      "v_mfma_f32_16x16x32_bf16 v[140:143], a[156:159], v[208:211], v[140:143]\n"
      "s_waitcnt lgkmcnt(15)\n"
      "v_fma_f32 v154, v154, %[csc], v217\n"
      "v_mfma_f32_16x16x32_bf16 v[168:171], a[160:163], a[72:75], 0\n"
      "s_waitcnt lgkmcnt(14)\n"
      "v_exp_f32 v153, v153\n"
      "v_mfma_f32_16x16x32_bf16 v[176:179], a[160:163], a[96:99], 0\n"
      "s_add_u32 s72, s69, 18880640\n"
      "v_cvt_pk_bf16_f32 v147, v150, v151\n"
      "v_fma_f32 v155, v155, %[csc], v217\n"
      "v_mfma_f32_16x16x32_bf16 v[184:187], a[160:163], a[120:123], 0\n"
      "s_add_u32 m0, %[m0q], 2048\n"
      "v_exp_f32 v154, v154\n"
      "v_mfma_f32_16x16x32_bf16 v[172:175], a[164:167], a[72:75], 0\n"
      "buffer_load_dwordx4 %[vq0], s[64:67], s72 offen nt lds\n"
      "v_fma_f32 v156, v156, %[csc], v217\n"
      "v_mfma_f32_16x16x32_bf16 v[180:183], a[164:167], a[96:99], 0\n"
      "s_add_u32 s71, s68, 73728\n"
      "v_exp_f32 v155, v155\n"
      "v_mfma_f32_16x16x32_bf16 v[188:191], a[164:167], a[120:123], 0\n"
      "s_add_u32 m0, %[m0b], 73728\n"
      "v_cvt_pk_bf16_f32 v152, v152, v153\n"
      "v_fma_f32 v157, v157, %[csc], v217\n"
      "v_mfma_f32_16x16x32_bf16 v[168:171], a[168:171], a[76:79], v[168:171]\n"
      "buffer_load_dwordx4 %[vv0], s[44:47], s71 offen nt lds\n"
      "v_mfma_f32_16x16x32_bf16 v[176:179], a[168:171], a[100:103], v[176:179]\n"
      "s_add_u32 m0, %[m0q], 3072\n"
      "v_exp_f32 v156, v156\n"
      "v_mfma_f32_16x16x32_bf16 v[184:187], a[168:171], a[124:127], v[184:187]\n"
      "buffer_load_dwordx4 %[vq1], s[64:67], s72 offen nt lds\n"
      "v_fma_f32 v158, v158, %[csc], v217\n"
      "v_mfma_f32_16x16x32_bf16 v[172:175], a[172:175], a[76:79], v[172:175]\n"
      "v_exp_f32 v157, v157\n"
      "v_mfma_f32_16x16x32_bf16 v[180:183], a[172:175], a[100:103], v[180:183]\n"
      "v_cvt_pk_bf16_f32 v153, v154, v155\n"
      "v_fma_f32 v159, v159, %[csc], v217\n"
      "v_mfma_f32_16x16x32_bf16 v[188:191], a[172:175], a[124:127], v[188:191]\n"
      "s_waitcnt lgkmcnt(10)\n"
      "v_exp_f32 v158, v158\n"
      "v_mfma_f32_16x16x32_bf16 v[168:171], a[176:179], a[80:83], v[168:171]\n"
      "ds_write_b64 %[ow], a[48:49] offset:0\n"
      "v_fma_f32 v160, v160, %[csc], v218\n"
      "v_mfma_f32_16x16x32_bf16 v[176:179], a[176:179], a[104:107], v[176:179]\n"
      "v_exp_f32 v159, v159\n"
      "v_mfma_f32_16x16x32_bf16 v[184:187], a[176:179], a[128:131], v[184:187]\n"
      "s_add_u32 m0, %[m0b], 74752\n"
      "v_cvt_pk_bf16_f32 v154, v156, v157\n"
      "v_fma_f32 v161, v161, %[csc], v218\n"
      "v_mfma_f32_16x16x32_bf16 v[172:175], a[180:183], a[80:83], v[172:175]\n"
      "buffer_load_dwordx4 %[vv1], s[44:47], s71 offen nt lds\n"
      "ds_write_b64 %[ow], a[50:51] offset:32\n"
      "v_mfma_f32_16x16x32_bf16 v[180:183], a[180:183], a[104:107], v[180:183]\n"
      "v_exp_f32 v160, v160\n"
      "v_mfma_f32_16x16x32_bf16 v[188:191], a[180:183], a[128:131], v[188:191]\n"
      "ds_write_b64 %[ow], a[52:53] offset:64\n"
      "v_fma_f32 v162, v162, %[csc], v218\n"
      "v_mfma_f32_16x16x32_bf16 v[168:171], a[184:187], a[84:87], v[168:171]\n"
      "v_exp_f32 v161, v161\n"
      "v_mfma_f32_16x16x32_bf16 v[176:179], a[184:187], a[108:111], v[176:179]\n"
      "v_cvt_pk_bf16_f32 v155, v158, v159\n"
      "v_fma_f32 v163, v163, %[csc], v218\n"
      "v_mfma_f32_16x16x32_bf16 v[184:187], a[184:187], a[132:135], v[184:187]\n"
      "v_exp_f32 v162, v162\n"
      "v_mfma_f32_16x16x32_bf16 v[172:175], a[188:191], a[84:87], v[172:175]\n"
      "ds_write_b64 %[ow], a[54:55] offset:96\n"
      "v_fma_f32 v164, v164, %[csc], v218\n"
      "v_mfma_f32_16x16x32_bf16 v[180:183], a[188:191], a[108:111], v[180:183]\n"
      "v_exp_f32 v163, v163\n"
      "v_mfma_f32_16x16x32_bf16 v[188:191], a[188:191], a[132:135], v[188:191]\n"
      "s_waitcnt lgkmcnt(10)\n"
      "v_cvt_pk_bf16_f32 v160, v160, v161\n"
      "v_fma_f32 v165, v165, %[csc], v218\n"
      "v_mfma_f32_16x16x32_bf16 v[168:171], a[192:195], a[88:91], v[168:171]\n"
      "s_add_u32 m0, %[m0b], 75776\n"
      "v_exp_f32 v164, v164\n"
      "v_mfma_f32_16x16x32_bf16 v[176:179], a[192:195], a[112:115], v[176:179]\n"
      "buffer_load_dwordx4 %[vv2], s[44:47], s71 offen nt lds\n"
      "v_fma_f32 v166, v166, %[csc], v218\n"
      "v_mfma_f32_16x16x32_bf16 v[184:187], a[192:195], a[136:139], v[184:187]\n"
      "v_exp_f32 v165, v165\n"
      "v_mfma_f32_16x16x32_bf16 v[172:175], a[196:199], a[88:91], v[172:175]\n"
      "v_cvt_pk_bf16_f32 v161, v162, v163\n"
      "v_fma_f32 v167, v167, %[csc], v218\n"
      "v_mfma_f32_16x16x32_bf16 v[180:183], a[196:199], a[112:115], v[180:183]\n"
      "v_exp_f32 v166, v166\n"
      "v_mfma_f32_16x16x32_bf16 v[188:191], a[196:199], a[136:139], v[188:191]\n"
      "v_exp_f32 v167, v167\n"
      "v_mfma_f32_16x16x32_bf16 v[168:171], a[200:203], a[92:95], v[168:171]\n"
      "ds_read_b128 a[224:227], %[orb] offset:0\n"
      "v_cvt_pk_bf16_f32 v162, v164, v165\n"
      "v_mfma_f32_16x16x32_bf16 v[176:179], a[200:203], a[116:119], v[176:179]\n"
      "ds_read_b128 a[228:231], %[orb] offset:1152\n"
      "v_cvt_pk_bf16_f32 v163, v166, v167\n"
      "v_mfma_f32_16x16x32_bf16 v[184:187], a[200:203], a[140:143], v[184:187]\n"
      "ds_read_b64_tr_b16 a[156:157], %[vb3] offset:12288\n"
      "ds_read_b64_tr_b16 a[158:159], %[vb3] offset:18432\n"
      "v_mfma_f32_16x16x32_bf16 v[172:175], a[204:207], a[92:95], v[172:175]\n"
      "ds_read_b64_tr_b16 a[160:161], %[vb0] offset:12416\n"
      "ds_read_b64_tr_b16 a[162:163], %[vb0] offset:18560\n"
      "v_mfma_f32_16x16x32_bf16 v[180:183], a[204:207], a[116:119], v[180:183]\n"
      "ds_read_b64_tr_b16 a[164:165], %[vb1] offset:12416\n"
      "ds_read_b64_tr_b16 a[166:167], %[vb1] offset:18560\n"
      "v_mfma_f32_16x16x32_bf16 v[188:191], a[204:207], a[140:143], v[188:191]\n"
      "ds_read_b64_tr_b16 a[168:169], %[vb2] offset:12416\n"
      "ds_read_b64_tr_b16 a[170:171], %[vb2] offset:18560\n"
      "v_mfma_f32_16x16x32_bf16 a[208:211], a[220:223], v[144:147], a[208:211]\n"
      "s_waitcnt lgkmcnt(9)\n"
      "ds_read_b64_tr_b16 a[172:173], %[vb3] offset:12416\n"
      "ds_read_b64_tr_b16 a[174:175], %[vb3] offset:18560\n"
      "v_mfma_f32_16x16x32_bf16 a[212:215], a[220:223], v[152:155], a[212:215]\n"
      "s_add_u32 s73, s69, 4276105216\n"
      "ds_read_b64_tr_b16 a[176:177], %[vb0] offset:12544\n"
      "ds_read_b64_tr_b16 a[178:179], %[vb0] offset:18688\n"
      "v_mfma_f32_16x16x32_bf16 a[216:219], a[220:223], v[160:163], a[216:219]\n"
      "ds_read_b64_tr_b16 a[180:181], %[vb1] offset:12544\n"
      "ds_read_b64_tr_b16 a[182:183], %[vb1] offset:18688\n"
      "v_mfma_f32_16x16x32_bf16 v[0:3], a[144:147], v[144:147], v[0:3]\n"
      "buffer_store_dwordx4 a[224:227], %[ost], s[84:87], s73 offen offset:0 sc1\n"
      "v_fma_f32 v168, v168, %[csc], v216\n"
      "v_mfma_f32_16x16x32_bf16 v[48:51], a[144:147], v[152:155], v[48:51]\n"
      "ds_read_b64_tr_b16 a[184:185], %[vb2] offset:12544\n"
      "ds_read_b64_tr_b16 a[186:187], %[vb2] offset:18688\n"
      "v_mfma_f32_16x16x32_bf16 v[96:99], a[144:147], v[160:163], v[96:99]\n"
      "ds_read_b64_tr_b16 a[188:189], %[vb3] offset:12544\n"
      "ds_read_b64_tr_b16 a[190:191], %[vb3] offset:18688\n"
      "v_mfma_f32_16x16x32_bf16 v[4:7], a[148:151], v[144:147], v[4:7]\n"
      "v_fma_f32 v169, v169, %[csc], v216\n"
      "v_mfma_f32_16x16x32_bf16 v[52:55], a[148:151], v[152:155], v[52:55]\n"
      "v_exp_f32 v168, v168\n"
      "v_mfma_f32_16x16x32_bf16 v[100:103], a[148:151], v[160:163], v[100:103]\n"
      "v_fma_f32 v170, v170, %[csc], v216\n"
      "v_mfma_f32_16x16x32_bf16 v[8:11], a[152:155], v[144:147], v[8:11]\n"
      "v_exp_f32 v169, v169\n"
      "v_mfma_f32_16x16x32_bf16 v[56:59], a[152:155], v[152:155], v[56:59]\n"
      "v_fma_f32 v171, v171, %[csc], v216\n"
      "s_waitcnt vmcnt(14) lgkmcnt(0)\n"
      "s_barrier\n"
      "v_mfma_f32_16x16x32_bf16 v[104:107], a[152:155], v[160:163], v[104:107]\n"
      "s_add_u32 s70, s68, 98304\n"
      "ds_read_b128 a[192:195], %[kb00] offset:12288\n"
      "ds_read_b128 a[196:199], %[kb00] offset:18432\n"
      "v_mfma_f32_16x16x32_bf16 v[12:15], a[156:159], v[144:147], v[12:15]\n"
      "s_add_u32 m0, %[m0b], 0\n"
      "ds_read_b128 a[200:203], %[kb10] offset:12288\n"
      "ds_read_b128 a[204:207], %[kb10] offset:18432\n"
      "v_mfma_f32_16x16x32_bf16 v[60:63], a[156:159], v[152:155], v[60:63]\n"
      "buffer_load_dwordx4 %[vk0], s[40:43], s70 offen nt lds\n"
      "ds_read_b128 a[144:147], %[kb00] offset:12416\n"
      "v_mfma_f32_16x16x32_bf16 v[108:111], a[156:159], v[160:163], v[108:111]\n"
      "s_add_u32 s73, s69, 4276108288\n"
      "buffer_store_dwordx4 a[228:231], %[ost], s[84:87], s73 offen offset:0 sc1\n"
      "ds_read_b128 a[148:151], %[kb00] offset:18560\n"
      "v_mfma_f32_16x16x32_bf16 v[16:19], a[160:163], v[144:147], v[16:19]\n"
      "ds_read_b128 a[152:155], %[kb10] offset:12416\n"
      "ds_read_b128 a[156:159], %[kb10] offset:18560\n"
      "v_mfma_f32_16x16x32_bf16 v[64:67], a[160:163], v[152:155], v[64:67]\n"
      "v_exp_f32 v170, v170\n"
      "v_mfma_f32_16x16x32_bf16 v[112:115], a[160:163], v[160:163], v[112:115]\n"
      "ds_read_b128 a[160:163], %[kb00] offset:12544\n"
      "v_fma_f32 v172, v172, %[csc], v216\n"
      "v_mfma_f32_16x16x32_bf16 v[20:23], a[164:167], v[144:147], v[20:23]\n"
      "v_exp_f32 v171, v171\n"
      "v_mfma_f32_16x16x32_bf16 v[68:71], a[164:167], v[152:155], v[68:71]\n"
      "v_cvt_pk_bf16_f32 v168, v168, v169\n"
      "v_fma_f32 v173, v173, %[csc], v216\n"
      "v_mfma_f32_16x16x32_bf16 v[116:119], a[164:167], v[160:163], v[116:119]\n"
      "ds_read_b128 a[164:167], %[kb00] offset:18688\n"
      "v_mfma_f32_16x16x32_bf16 v[24:27], a[168:171], v[144:147], v[24:27]\n"
      "s_add_u32 m0, %[m0b], 1024\n"
      "v_exp_f32 v172, v172\n"
      "v_mfma_f32_16x16x32_bf16 v[72:75], a[168:171], v[152:155], v[72:75]\n"
      "buffer_load_dwordx4 %[vk1], s[40:43], s70 offen nt lds\n"
      "v_fma_f32 v174, v174, %[csc], v216\n"
      "v_mfma_f32_16x16x32_bf16 v[120:123], a[168:171], v[160:163], v[120:123]\n"
      "ds_read_b128 a[168:171], %[kb10] offset:12544\n"
      "v_mfma_f32_16x16x32_bf16 v[28:31], a[172:175], v[144:147], v[28:31]\n"
      "s_waitcnt vmcnt(27)\n"
      "ds_read_b128 a[48:51], %[qs0] offset:4096\n"
      "ds_read_b128 a[52:55], %[qs1] offset:4096\n"
      "v_mfma_f32_16x16x32_bf16 v[76:79], a[172:175], v[152:155], v[76:79]\n"
      "v_exp_f32 v173, v173\n"
      "v_mfma_f32_16x16x32_bf16 v[124:127], a[172:175], v[160:163], v[124:127]\n"
      "ds_read_b128 a[172:175], %[kb10] offset:18688\n"
      "v_cvt_pk_bf16_f32 v169, v170, v171\n"
      "v_mfma_f32_16x16x32_bf16 v[32:35], a[176:179], v[144:147], v[32:35]\n"
      "v_fma_f32 v175, v175, %[csc], v216\n"
      "v_mfma_f32_16x16x32_bf16 v[80:83], a[176:179], v[152:155], v[80:83]\n"
      "v_exp_f32 v174, v174\n"
      "v_mfma_f32_16x16x32_bf16 v[128:131], a[176:179], v[160:163], v[128:131]\n"
      "ds_read_b64_tr_b16 a[176:177], %[vb0] offset:24576\n"
      "ds_read_b64_tr_b16 a[178:179], %[vb0] offset:30720\n"
      "v_mfma_f32_16x16x32_bf16 v[36:39], a[180:183], v[144:147], v[36:39]\n"
      "v_fma_f32 v176, v176, %[csc], v217\n"
      "v_mfma_f32_16x16x32_bf16 v[84:87], a[180:183], v[152:155], v[84:87]\n"
      "s_add_u32 m0, %[m0b], 2048\n"
      "v_exp_f32 v175, v175\n"
      "v_mfma_f32_16x16x32_bf16 v[132:135], a[180:183], v[160:163], v[132:135]\n"
      "buffer_load_dwordx4 %[vk2], s[40:43], s70 offen nt lds\n"
      "v_cvt_pk_bf16_f32 v170, v172, v173\n"
      "v_mfma_f32_16x16x32_bf16 v[40:43], a[184:187], v[144:147], v[40:43]\n"
      "ds_read_b64_tr_b16 a[180:181], %[vb1] offset:24576\n"
      "ds_read_b64_tr_b16 a[182:183], %[vb1] offset:30720\n"
      "v_mfma_f32_16x16x32_bf16 v[88:91], a[184:187], v[152:155], v[88:91]\n"
      "v_fma_f32 v177, v177, %[csc], v217\n"
      "v_mfma_f32_16x16x32_bf16 v[136:139], a[184:187], v[160:163], v[136:139]\n"
      "ds_read_b64_tr_b16 a[184:185], %[vb2] offset:24576\n"
      "ds_read_b64_tr_b16 a[186:187], %[vb2] offset:30720\n"
      "v_mfma_f32_16x16x32_bf16 v[44:47], a[188:191], v[144:147], v[44:47]\n"
      "v_exp_f32 v176, v176\n"
      "v_mfma_f32_16x16x32_bf16 v[92:95], a[188:191], v[152:155], v[92:95]\n"
      "v_fma_f32 v178, v178, %[csc], v217\n"
      "v_mfma_f32_16x16x32_bf16 v[140:143], a[188:191], v[160:163], v[140:143]\n"
      "s_waitcnt lgkmcnt(15)\n"
      "v_exp_f32 v177, v177\n"
      "v_mfma_f32_16x16x32_bf16 v[144:147], a[192:195], a[72:75], 0\n"
      "v_cvt_pk_bf16_f32 v171, v174, v175\n"
      "v_fma_f32 v179, v179, %[csc], v217\n"
      "v_mfma_f32_16x16x32_bf16 v[152:155], a[192:195], a[96:99], 0\n"
      "v_exp_f32 v178, v178\n"
      "v_mfma_f32_16x16x32_bf16 v[160:163], a[192:195], a[120:123], 0\n"
      "s_add_u32 s71, s68, 86016\n"
      "v_fma_f32 v180, v180, %[csc], v217\n"
      "v_mfma_f32_16x16x32_bf16 v[148:151], a[196:199], a[72:75], 0\n"
      "s_add_u32 m0, %[m0b], 86016\n"
      "v_exp_f32 v179, v179\n"
      "v_mfma_f32_16x16x32_bf16 v[156:159], a[196:199], a[96:99], 0\n"
      "buffer_load_dwordx4 %[vv0], s[44:47], s71 offen nt lds\n"
      "v_cvt_pk_bf16_f32 v176, v176, v177\n"
      "v_mfma_f32_16x16x32_bf16 v[164:167], a[196:199], a[120:123], 0\n"
      "v_fma_f32 v181, v181, %[csc], v217\n"
      "v_mfma_f32_16x16x32_bf16 v[144:147], a[200:203], a[76:79], v[144:147]\n"
      "v_exp_f32 v180, v180\n"
      "v_mfma_f32_16x16x32_bf16 v[152:155], a[200:203], a[100:103], v[152:155]\n"
      "v_fma_f32 v182, v182, %[csc], v217\n"
      "v_mfma_f32_16x16x32_bf16 v[160:163], a[200:203], a[124:127], v[160:163]\n"
      "v_exp_f32 v181, v181\n"
      "v_mfma_f32_16x16x32_bf16 v[148:151], a[204:207], a[76:79], v[148:151]\n"
      "v_cvt_pk_bf16_f32 v177, v178, v179\n"
      "v_fma_f32 v183, v183, %[csc], v217\n"
      "v_mfma_f32_16x16x32_bf16 v[156:159], a[204:207], a[100:103], v[156:159]\n"
      "s_waitcnt lgkmcnt(7)\n"
      "v_exp_f32 v182, v182\n"
      "v_mfma_f32_16x16x32_bf16 v[164:167], a[204:207], a[124:127], v[164:167]\n"
      "s_add_u32 s72, s69, 18886784\n"
      "v_fma_f32 v184, v184, %[csc], v218\n"
      "v_mfma_f32_16x16x32_bf16 v[144:147], a[144:147], a[80:83], v[144:147]\n"
      "s_add_u32 m0, %[m0b], 87040\n"
      "v_exp_f32 v183, v183\n"
      "v_mfma_f32_16x16x32_bf16 v[152:155], a[144:147], a[104:107], v[152:155]\n"
      "buffer_load_dwordx4 %[vv1], s[44:47], s71 offen nt lds\n"
      "v_cvt_pk_bf16_f32 v178, v180, v181\n"
      "v_mfma_f32_16x16x32_bf16 v[160:163], a[144:147], a[128:131], v[160:163]\n"
      "s_add_u32 m0, %[m0q], 4096\n"
      "v_fma_f32 v185, v185, %[csc], v218\n"
      "v_mfma_f32_16x16x32_bf16 v[148:151], a[148:151], a[80:83], v[148:151]\n"
      "v_exp_f32 v184, v184\n"
      "v_mfma_f32_16x16x32_bf16 v[156:159], a[148:151], a[104:107], v[156:159]\n"
      "buffer_load_dwordx4 %[vq0], s[64:67], s72 offen nt lds\n"
      "v_fma_f32 v186, v186, %[csc], v218\n"
      "v_mfma_f32_16x16x32_bf16 v[164:167], a[148:151], a[128:131], v[164:167]\n"
      "s_add_u32 m0, %[m0q], 5120\n"
      "v_exp_f32 v185, v185\n"
      "v_mfma_f32_16x16x32_bf16 v[144:147], a[152:155], a[84:87], v[144:147]\n"
      "v_cvt_pk_bf16_f32 v179, v182, v183\n"
      "v_fma_f32 v187, v187, %[csc], v218\n"
      "v_mfma_f32_16x16x32_bf16 v[152:155], a[152:155], a[108:111], v[152:155]\n"
      "v_exp_f32 v186, v186\n"
      "v_mfma_f32_16x16x32_bf16 v[160:163], a[152:155], a[132:135], v[160:163]\n"
      "buffer_load_dwordx4 %[vq1], s[64:67], s72 offen nt lds\n"
      "v_fma_f32 v188, v188, %[csc], v218\n"
      "v_mfma_f32_16x16x32_bf16 v[148:151], a[156:159], a[84:87], v[148:151]\n"
      "v_exp_f32 v187, v187\n"
      "v_mfma_f32_16x16x32_bf16 v[156:159], a[156:159], a[108:111], v[156:159]\n"
      "s_add_u32 m0, %[m0b], 88064\n"
      "v_cvt_pk_bf16_f32 v184, v184, v185\n"
      "v_fma_f32 v189, v189, %[csc], v218\n"
      "v_mfma_f32_16x16x32_bf16 v[164:167], a[156:159], a[132:135], v[164:167]\n"
      "v_exp_f32 v188, v188\n"
      "v_mfma_f32_16x16x32_bf16 v[144:147], a[160:163], a[88:91], v[144:147]\n"
      "buffer_load_dwordx4 %[vv2], s[44:47], s71 offen nt lds\n"
      "v_fma_f32 v190, v190, %[csc], v218\n"
      "v_mfma_f32_16x16x32_bf16 v[152:155], a[160:163], a[112:115], v[152:155]\n"
      "v_exp_f32 v189, v189\n"
      "v_mfma_f32_16x16x32_bf16 v[160:163], a[160:163], a[136:139], v[160:163]\n"
      "v_cvt_pk_bf16_f32 v185, v186, v187\n"
      "v_fma_f32 v191, v191, %[csc], v218\n"
      "v_mfma_f32_16x16x32_bf16 v[148:151], a[164:167], a[88:91], v[148:151]\n"
      "v_exp_f32 v190, v190\n"
      "v_mfma_f32_16x16x32_bf16 v[156:159], a[164:167], a[112:115], v[156:159]\n"
      "v_exp_f32 v191, v191\n"
      "v_mfma_f32_16x16x32_bf16 v[164:167], a[164:167], a[136:139], v[164:167]\n"
      "ds_write_b64 %[ow], a[8:9] offset:0\n"
      "v_cvt_pk_bf16_f32 v186, v188, v189\n"
      "v_mfma_f32_16x16x32_bf16 v[144:147], a[168:171], a[92:95], v[144:147]\n"
      "ds_write_b64 %[ow], a[10:11] offset:32\n"
      "v_cvt_pk_bf16_f32 v187, v190, v191\n"
      "v_mfma_f32_16x16x32_bf16 v[152:155], a[168:171], a[116:119], v[152:155]\n"
      "ds_read_b64_tr_b16 a[188:189], %[vb3] offset:24576\n"
      "ds_read_b64_tr_b16 a[190:191], %[vb3] offset:30720\n"
      "v_mfma_f32_16x16x32_bf16 v[160:163], a[168:171], a[140:143], v[160:163]\n"
      "s_waitcnt lgkmcnt(4)\n"
      "ds_read_b64_tr_b16 a[192:193], %[vb0] offset:24704\n"
      "ds_read_b64_tr_b16 a[194:195], %[vb0] offset:30848\n"
      "v_mfma_f32_16x16x32_bf16 v[148:151], a[172:175], a[92:95], v[148:151]\n"
      "ds_read_b64_tr_b16 a[196:197], %[vb1] offset:24704\n"
      "ds_read_b64_tr_b16 a[198:199], %[vb1] offset:30848\n"
      "v_mfma_f32_16x16x32_bf16 v[156:159], a[172:175], a[116:119], v[156:159]\n"
      "ds_read_b64_tr_b16 a[200:201], %[vb2] offset:24704\n"
      "ds_read_b64_tr_b16 a[202:203], %[vb2] offset:30848\n"
      "v_mfma_f32_16x16x32_bf16 v[164:167], a[172:175], a[140:143], v[164:167]\n"
      "ds_read_b64_tr_b16 a[204:205], %[vb3] offset:24704\n"
      "ds_read_b64_tr_b16 a[206:207], %[vb3] offset:30848\n"
      "v_mfma_f32_16x16x32_bf16 a[208:211], a[220:223], v[168:171], a[208:211]\n"
      "ds_read_b64_tr_b16 a[144:145], %[vb0] offset:24832\n"
      "ds_read_b64_tr_b16 a[146:147], %[vb0] offset:30976\n"
      "v_mfma_f32_16x16x32_bf16 a[212:215], a[220:223], v[176:179], a[212:215]\n"
      "ds_read_b64_tr_b16 a[148:149], %[vb1] offset:24832\n"
      "ds_read_b64_tr_b16 a[150:151], %[vb1] offset:30976\n"
      "v_mfma_f32_16x16x32_bf16 a[216:219], a[220:223], v[184:187], a[216:219]\n"
      "ds_read_b64_tr_b16 a[152:153], %[vb2] offset:24832\n"
      "ds_read_b64_tr_b16 a[154:155], %[vb2] offset:30976\n"
      "v_mfma_f32_16x16x32_bf16 v[0:3], a[176:179], v[168:171], v[0:3]\n"
      "ds_read_b64_tr_b16 a[156:157], %[vb3] offset:24832\n"
      "ds_read_b64_tr_b16 a[158:159], %[vb3] offset:30976\n"
      "v_mfma_f32_16x16x32_bf16 v[48:51], a[176:179], v[176:179], v[48:51]\n"
      "ds_write_b64 %[ow], a[12:13] offset:64\n"
      "v_fma_f32 v144, v144, %[csc], v216\n"
      "v_mfma_f32_16x16x32_bf16 v[96:99], a[176:179], v[184:187], v[96:99]\n"
      "ds_write_b64 %[ow], a[14:15] offset:96\n"
      "v_fma_f32 v145, v145, %[csc], v216\n"
      "v_mfma_f32_16x16x32_bf16 v[4:7], a[180:183], v[168:171], v[4:7]\n"
      "ds_read_b128 a[232:235], %[orb] offset:0\n"
      "v_mfma_f32_16x16x32_bf16 v[52:55], a[180:183], v[176:179], v[52:55]\n"
      "ds_read_b128 a[236:239], %[orb] offset:1152\n"
      "v_mfma_f32_16x16x32_bf16 v[100:103], a[180:183], v[184:187], v[100:103]\n"
      "v_exp_f32 v144, v144\n"
      "s_waitcnt vmcnt(20) lgkmcnt(0)\n"
      "s_barrier\n"
      "v_mfma_f32_16x16x32_bf16 v[8:11], a[184:187], v[168:171], v[8:11]\n"
      "s_add_u32 s70, s68, 110592\n"
      "ds_read_b128 a[160:163], %[kb00] offset:24576\n"
      "ds_read_b128 a[164:167], %[kb00] offset:30720\n"
      "v_mfma_f32_16x16x32_bf16 v[56:59], a[184:187], v[176:179], v[56:59]\n"
      "s_add_u32 m0, %[m0b], 12288\n"
      "ds_read_b128 a[168:171], %[kb10] offset:24576\n"
      "ds_read_b128 a[172:175], %[kb10] offset:30720\n"
      "v_mfma_f32_16x16x32_bf16 v[104:107], a[184:187], v[184:187], v[104:107]\n"
      "buffer_load_dwordx4 %[vk0], s[40:43], s70 offen nt lds\n"
      "ds_read_b128 a[176:179], %[kb00] offset:24704\n"
      "v_mfma_f32_16x16x32_bf16 v[12:15], a[188:191], v[168:171], v[12:15]\n"
      "s_add_u32 s73, s69, 4276092928\n"
      "buffer_store_dwordx4 a[232:235], %[ost], s[84:87], s73 offen offset:128 sc1\n"
      "ds_read_b128 a[180:183], %[kb00] offset:30848\n"
      "v_mfma_f32_16x16x32_bf16 v[60:63], a[188:191], v[176:179], v[60:63]\n"
      "ds_read_b128 a[184:187], %[kb10] offset:24704\n"
      "v_fma_f32 v146, v146, %[csc], v216\n"
      "v_mfma_f32_16x16x32_bf16 v[108:111], a[188:191], v[184:187], v[108:111]\n"
      "ds_read_b128 a[188:191], %[kb10] offset:30848\n"
      "v_mfma_f32_16x16x32_bf16 v[16:19], a[192:195], v[168:171], v[16:19]\n"
      "v_exp_f32 v145, v145\n"
      "v_mfma_f32_16x16x32_bf16 v[64:67], a[192:195], v[176:179], v[64:67]\n"
      "v_fma_f32 v147, v147, %[csc], v216\n"
      "v_mfma_f32_16x16x32_bf16 v[112:115], a[192:195], v[184:187], v[112:115]\n"
      "ds_read_b128 a[192:195], %[kb00] offset:24832\n"
      "v_mfma_f32_16x16x32_bf16 v[20:23], a[196:199], v[168:171], v[20:23]\n"
      "v_exp_f32 v146, v146\n"
      "v_mfma_f32_16x16x32_bf16 v[68:71], a[196:199], v[176:179], v[68:71]\n"
      "s_add_u32 m0, %[m0b], 13312\n"
      "v_fma_f32 v148, v148, %[csc], v216\n"
      "v_mfma_f32_16x16x32_bf16 v[116:119], a[196:199], v[184:187], v[116:119]\n"
      "buffer_load_dwordx4 %[vk1], s[40:43], s70 offen nt lds\n"
      "ds_read_b128 a[196:199], %[kb00] offset:30976\n"
      "v_mfma_f32_16x16x32_bf16 v[24:27], a[200:203], v[168:171], v[24:27]\n"
      "v_exp_f32 v147, v147\n"
      "v_mfma_f32_16x16x32_bf16 v[72:75], a[200:203], v[176:179], v[72:75]\n"
      "v_cvt_pk_bf16_f32 v144, v144, v145\n"
      "v_fma_f32 v149, v149, %[csc], v216\n"
      "v_mfma_f32_16x16x32_bf16 v[120:123], a[200:203], v[184:187], v[120:123]\n"
      "s_add_u32 s73, s69, 4276096000\n"
      "buffer_store_dwordx4 a[236:239], %[ost], s[84:87], s73 offen offset:128 sc1\n"
      "ds_read_b128 a[200:203], %[kb10] offset:24832\n"
      "v_mfma_f32_16x16x32_bf16 v[28:31], a[204:207], v[168:171], v[28:31]\n"
      "v_exp_f32 v148, v148\n"
      "v_mfma_f32_16x16x32_bf16 v[76:79], a[204:207], v[176:179], v[76:79]\n"
      "v_fma_f32 v150, v150, %[csc], v216\n"
      "v_mfma_f32_16x16x32_bf16 v[124:127], a[204:207], v[184:187], v[124:127]\n"
      "ds_read_b128 a[204:207], %[kb10] offset:30976\n"
      "v_mfma_f32_16x16x32_bf16 v[32:35], a[144:147], v[168:171], v[32:35]\n"
      "v_exp_f32 v149, v149\n"
      "v_mfma_f32_16x16x32_bf16 v[80:83], a[144:147], v[176:179], v[80:83]\n"
      "v_cvt_pk_bf16_f32 v145, v146, v147\n"
      "v_fma_f32 v151, v151, %[csc], v216\n"
      "v_mfma_f32_16x16x32_bf16 v[128:131], a[144:147], v[184:187], v[128:131]\n"
      "s_add_u32 m0, %[m0b], 14336\n"
      "ds_read_b64_tr_b16 a[144:145], %[vb0] offset:36864\n"
      "ds_read_b64_tr_b16 a[146:147], %[vb0] offset:43008\n"
      "v_mfma_f32_16x16x32_bf16 v[36:39], a[148:151], v[168:171], v[36:39]\n"
      "buffer_load_dwordx4 %[vk2], s[40:43], s70 offen nt lds\n"
      "v_mfma_f32_16x16x32_bf16 v[84:87], a[148:151], v[176:179], v[84:87]\n"
      "v_exp_f32 v150, v150\n"
      "v_mfma_f32_16x16x32_bf16 v[132:135], a[148:151], v[184:187], v[132:135]\n"
      "ds_read_b64_tr_b16 a[148:149], %[vb1] offset:36864\n"
      "ds_read_b64_tr_b16 a[150:151], %[vb1] offset:43008\n"
      "v_mfma_f32_16x16x32_bf16 v[40:43], a[152:155], v[168:171], v[40:43]\n"
      "v_fma_f32 v152, v152, %[csc], v217\n"
      "v_mfma_f32_16x16x32_bf16 v[88:91], a[152:155], v[176:179], v[88:91]\n"
      "s_waitcnt vmcnt(29)\n"
      "ds_read_b128 a[8:11], %[qs0] offset:0\n"
      "ds_read_b128 a[12:15], %[qs1] offset:0\n"
      "v_mfma_f32_16x16x32_bf16 v[136:139], a[152:155], v[184:187], v[136:139]\n"
      "ds_read_b64_tr_b16 a[152:153], %[vb2] offset:36864\n"
      "ds_read_b64_tr_b16 a[154:155], %[vb2] offset:43008\n"
      "v_mfma_f32_16x16x32_bf16 v[44:47], a[156:159], v[168:171], v[44:47]\n"
      "v_exp_f32 v151, v151\n"
      "v_mfma_f32_16x16x32_bf16 v[92:95], a[156:159], v[176:179], v[92:95]\n"
      "v_cvt_pk_bf16_f32 v146, v148, v149\n"
      "v_fma_f32 v153, v153, %[csc], v217\n"
      "v_mfma_f32_16x16x32_bf16 v[140:143], a[156:159], v[184:187], v[140:143]\n"
      "s_waitcnt lgkmcnt(15)\n"
      "v_exp_f32 v152, v152\n"
      "v_mfma_f32_16x16x32_bf16 v[168:171], a[160:163], a[72:75], 0\n"
      "s_add_u32 s71, s68, 98304\n"
      "v_fma_f32 v154, v154, %[csc], v217\n"
      "v_mfma_f32_16x16x32_bf16 v[176:179], a[160:163], a[96:99], 0\n"
      "s_add_u32 m0, %[m0b], 98304\n"
      "v_exp_f32 v153, v153\n"
      "v_mfma_f32_16x16x32_bf16 v[184:187], a[160:163], a[120:123], 0\n"
      "buffer_load_dwordx4 %[vv0], s[44:47], s71 offen nt lds\n"
      "v_cvt_pk_bf16_f32 v147, v150, v151\n"
      "v_mfma_f32_16x16x32_bf16 v[172:175], a[164:167], a[72:75], 0\n"
      "v_fma_f32 v155, v155, %[csc], v217\n"
      "v_mfma_f32_16x16x32_bf16 v[180:183], a[164:167], a[96:99], 0\n"
      "v_exp_f32 v154, v154\n"
      "v_mfma_f32_16x16x32_bf16 v[188:191], a[164:167], a[120:123], 0\n"
      "v_fma_f32 v156, v156, %[csc], v217\n"
      "v_mfma_f32_16x16x32_bf16 v[168:171], a[168:171], a[76:79], v[168:171]\n"
      "v_exp_f32 v155, v155\n"
      "v_mfma_f32_16x16x32_bf16 v[176:179], a[168:171], a[100:103], v[176:179]\n"
      "v_cvt_pk_bf16_f32 v152, v152, v153\n"
      "v_fma_f32 v157, v157, %[csc], v217\n"
      "v_mfma_f32_16x16x32_bf16 v[184:187], a[168:171], a[124:127], v[184:187]\n"
      "v_exp_f32 v156, v156\n"
      "v_mfma_f32_16x16x32_bf16 v[172:175], a[172:175], a[76:79], v[172:175]\n"
      "v_fma_f32 v158, v158, %[csc], v217\n"
      "v_mfma_f32_16x16x32_bf16 v[180:183], a[172:175], a[100:103], v[180:183]\n"
      "s_add_u32 m0, %[m0b], 99328\n"
      "v_exp_f32 v157, v157\n"
      "v_mfma_f32_16x16x32_bf16 v[188:191], a[172:175], a[124:127], v[188:191]\n"
      "buffer_load_dwordx4 %[vv1], s[44:47], s71 offen nt lds\n"
      "v_cvt_pk_bf16_f32 v153, v154, v155\n"
      "v_mfma_f32_16x16x32_bf16 v[168:171], a[176:179], a[80:83], v[168:171]\n"
      "v_fma_f32 v159, v159, %[csc], v217\n"
      "v_mfma_f32_16x16x32_bf16 v[176:179], a[176:179], a[104:107], v[176:179]\n"
      "v_exp_f32 v158, v158\n"
      "v_mfma_f32_16x16x32_bf16 v[184:187], a[176:179], a[128:131], v[184:187]\n"
      "s_waitcnt lgkmcnt(11)\n"
      "v_fma_f32 v160, v160, %[csc], v218\n"
      "v_mfma_f32_16x16x32_bf16 v[172:175], a[180:183], a[80:83], v[172:175]\n"
      "v_exp_f32 v159, v159\n"
      "v_mfma_f32_16x16x32_bf16 v[180:183], a[180:183], a[104:107], v[180:183]\n"
      "v_cvt_pk_bf16_f32 v154, v156, v157\n"
      "v_fma_f32 v161, v161, %[csc], v218\n"
      "v_mfma_f32_16x16x32_bf16 v[188:191], a[180:183], a[128:131], v[188:191]\n"
      "v_exp_f32 v160, v160\n"
      "v_mfma_f32_16x16x32_bf16 v[168:171], a[184:187], a[84:87], v[168:171]\n"
      "v_fma_f32 v162, v162, %[csc], v218\n"
      "v_mfma_f32_16x16x32_bf16 v[176:179], a[184:187], a[108:111], v[176:179]\n"
      "v_exp_f32 v161, v161\n"
      "v_mfma_f32_16x16x32_bf16 v[184:187], a[184:187], a[132:135], v[184:187]\n"
      "s_add_u32 m0, %[m0b], 100352\n"
      "v_cvt_pk_bf16_f32 v155, v158, v159\n"
      "v_fma_f32 v163, v163, %[csc], v218\n"
      "v_mfma_f32_16x16x32_bf16 v[172:175], a[188:191], a[84:87], v[172:175]\n"
      "s_waitcnt lgkmcnt(2)\n"
      "buffer_load_dwordx4 %[vv2], s[44:47], s71 offen nt lds\n"
      "v_mfma_f32_16x16x32_bf16 v[180:183], a[188:191], a[108:111], v[180:183]\n"
      "s_add_u32 s72, s69, 18874624\n"
      "v_exp_f32 v162, v162\n"
      "v_mfma_f32_16x16x32_bf16 v[188:191], a[188:191], a[132:135], v[188:191]\n"
      "s_add_u32 m0, %[m0q], 0\n"
      "v_fma_f32 v164, v164, %[csc], v218\n"
      "v_mfma_f32_16x16x32_bf16 v[168:171], a[192:195], a[88:91], v[168:171]\n"
      "v_exp_f32 v163, v163\n"
      "v_mfma_f32_16x16x32_bf16 v[176:179], a[192:195], a[112:115], v[176:179]\n"
      "v_cvt_pk_bf16_f32 v160, v160, v161\n"
      "v_fma_f32 v165, v165, %[csc], v218\n"
      "v_mfma_f32_16x16x32_bf16 v[184:187], a[192:195], a[136:139], v[184:187]\n"
      "v_exp_f32 v164, v164\n"
      "v_mfma_f32_16x16x32_bf16 v[172:175], a[196:199], a[88:91], v[172:175]\n"
      "buffer_load_dwordx4 %[vq0], s[64:67], s72 offen nt lds\n"
      "v_fma_f32 v166, v166, %[csc], v218\n"
      "v_mfma_f32_16x16x32_bf16 v[180:183], a[196:199], a[112:115], v[180:183]\n"
      "s_add_u32 m0, %[m0q], 1024\n"
      "v_exp_f32 v165, v165\n"
      "v_mfma_f32_16x16x32_bf16 v[188:191], a[196:199], a[136:139], v[188:191]\n"
      "v_cvt_pk_bf16_f32 v161, v162, v163\n"
      "v_fma_f32 v167, v167, %[csc], v218\n"
      "v_mfma_f32_16x16x32_bf16 v[168:171], a[200:203], a[92:95], v[168:171]\n"
      "v_exp_f32 v166, v166\n"
      "v_mfma_f32_16x16x32_bf16 v[176:179], a[200:203], a[116:119], v[176:179]\n"
      "v_exp_f32 v167, v167\n"
      "v_mfma_f32_16x16x32_bf16 v[184:187], a[200:203], a[140:143], v[184:187]\n"
      "buffer_load_dwordx4 %[vq1], s[64:67], s72 offen nt lds\n"
      "v_cvt_pk_bf16_f32 v162, v164, v165\n"
      "v_mfma_f32_16x16x32_bf16 v[172:175], a[204:207], a[92:95], v[172:175]\n"
      "ds_write_b64 %[ow], a[32:33] offset:0\n"
      "v_cvt_pk_bf16_f32 v163, v166, v167\n"
      "v_mfma_f32_16x16x32_bf16 v[180:183], a[204:207], a[116:119], v[180:183]\n"
      "ds_read_b64_tr_b16 a[156:157], %[vb3] offset:36864\n"
      "ds_read_b64_tr_b16 a[158:159], %[vb3] offset:43008\n"
      "v_mfma_f32_16x16x32_bf16 v[188:191], a[204:207], a[140:143], v[188:191]\n"
      "ds_read_b64_tr_b16 a[160:161], %[vb0] offset:36992\n"
      "ds_read_b64_tr_b16 a[162:163], %[vb0] offset:43136\n"
      "v_mfma_f32_16x16x32_bf16 a[208:211], a[220:223], v[144:147], a[208:211]\n"
      "ds_read_b64_tr_b16 a[164:165], %[vb1] offset:36992\n"
      "ds_read_b64_tr_b16 a[166:167], %[vb1] offset:43136\n"
      "v_mfma_f32_16x16x32_bf16 a[212:215], a[220:223], v[152:155], a[212:215]\n"
      "ds_read_b64_tr_b16 a[168:169], %[vb2] offset:36992\n"
      "ds_read_b64_tr_b16 a[170:171], %[vb2] offset:43136\n"
      "v_mfma_f32_16x16x32_bf16 a[216:219], a[220:223], v[160:163], a[216:219]\n"
      "ds_read_b64_tr_b16 a[172:173], %[vb3] offset:36992\n"
      "ds_read_b64_tr_b16 a[174:175], %[vb3] offset:43136\n"
      "v_mfma_f32_16x16x32_bf16 v[0:3], a[144:147], v[144:147], v[0:3]\n"
      "ds_read_b64_tr_b16 a[176:177], %[vb0] offset:37120\n"
      "ds_read_b64_tr_b16 a[178:179], %[vb0] offset:43264\n"
      "v_mfma_f32_16x16x32_bf16 v[48:51], a[144:147], v[152:155], v[48:51]\n"
      "ds_read_b64_tr_b16 a[180:181], %[vb1] offset:37120\n"
      "ds_read_b64_tr_b16 a[182:183], %[vb1] offset:43264\n"
      "v_mfma_f32_16x16x32_bf16 v[96:99], a[144:147], v[160:163], v[96:99]\n"
      "ds_read_b64_tr_b16 a[184:185], %[vb2] offset:37120\n"
      "ds_read_b64_tr_b16 a[186:187], %[vb2] offset:43264\n"
      "v_mfma_f32_16x16x32_bf16 v[4:7], a[148:151], v[144:147], v[4:7]\n"
      "ds_read_b64_tr_b16 a[188:189], %[vb3] offset:37120\n"
      "ds_read_b64_tr_b16 a[190:191], %[vb3] offset:43264\n"
      "v_mfma_f32_16x16x32_bf16 v[52:55], a[148:151], v[152:155], v[52:55]\n"
      "ds_write_b64 %[ow], a[34:35] offset:32\n"
      "v_fma_f32 v168, v168, %[csc], v216\n"
      "v_mfma_f32_16x16x32_bf16 v[100:103], a[148:151], v[160:163], v[100:103]\n"
      "s_waitcnt lgkmcnt(15)\n"
      "ds_write_b64 %[ow], a[36:37] offset:64\n"
      "v_fma_f32 v169, v169, %[csc], v216\n"
      "v_mfma_f32_16x16x32_bf16 v[8:11], a[152:155], v[144:147], v[8:11]\n"
      "ds_write_b64 %[ow], a[38:39] offset:96\n"
      "v_mfma_f32_16x16x32_bf16 v[56:59], a[152:155], v[152:155], v[56:59]\n"
      "ds_read_b128 a[224:227], %[orb] offset:0\n"
      "v_mfma_f32_16x16x32_bf16 v[104:107], a[152:155], v[160:163], v[104:107]\n"
      "ds_read_b128 a[228:231], %[orb] offset:1152\n"
      "s_waitcnt vmcnt(20) lgkmcnt(0)\n"
      "s_barrier\n"
      "v_mfma_f32_16x16x32_bf16 v[12:15], a[156:159], v[144:147], v[12:15]\n"
      "s_add_u32 s70, s68, 122880\n"
      "ds_read_b128 a[192:195], %[kb01] offset:0\n"
      "ds_read_b128 a[196:199], %[kb01] offset:6144\n"
      "v_mfma_f32_16x16x32_bf16 v[60:63], a[156:159], v[152:155], v[60:63]\n"
      "s_add_u32 m0, %[m0b], 24576\n"
      "ds_read_b128 a[200:203], %[kb11] offset:0\n"
      "ds_read_b128 a[204:207], %[kb11] offset:6144\n"
      "v_mfma_f32_16x16x32_bf16 v[108:111], a[156:159], v[160:163], v[108:111]\n"
      "buffer_load_dwordx4 %[vk0], s[40:43], s70 offen nt lds\n"
      "ds_read_b128 a[144:147], %[kb01] offset:128\n"
      "v_mfma_f32_16x16x32_bf16 v[16:19], a[160:163], v[144:147], v[16:19]\n"
      "ds_read_b128 a[148:151], %[kb01] offset:6272\n"
      "ds_read_b128 a[152:155], %[kb11] offset:128\n"
      "v_mfma_f32_16x16x32_bf16 v[64:67], a[160:163], v[152:155], v[64:67]\n"
      "ds_read_b128 a[156:159], %[kb11] offset:6272\n"
      "v_mfma_f32_16x16x32_bf16 v[112:115], a[160:163], v[160:163], v[112:115]\n"
      "ds_read_b128 a[160:163], %[kb01] offset:256\n"
      "v_mfma_f32_16x16x32_bf16 v[20:23], a[164:167], v[144:147], v[20:23]\n"
      "v_exp_f32 v168, v168\n"
      "v_mfma_f32_16x16x32_bf16 v[68:71], a[164:167], v[152:155], v[68:71]\n"
      "v_fma_f32 v170, v170, %[csc], v216\n"
      "v_mfma_f32_16x16x32_bf16 v[116:119], a[164:167], v[160:163], v[116:119]\n"
      "ds_read_b128 a[164:167], %[kb01] offset:6400\n"
      "v_mfma_f32_16x16x32_bf16 v[24:27], a[168:171], v[144:147], v[24:27]\n"
      "s_add_u32 s73, s69, 4276099072\n"
      "buffer_store_dwordx4 a[224:227], %[ost], s[84:87], s73 offen offset:128 sc1\n"
      "v_mfma_f32_16x16x32_bf16 v[72:75], a[168:171], v[152:155], v[72:75]\n"
      "s_add_u32 m0, %[m0b], 25600\n"
      "v_exp_f32 v169, v169\n"
      "v_mfma_f32_16x16x32_bf16 v[120:123], a[168:171], v[160:163], v[120:123]\n"
      "buffer_load_dwordx4 %[vk1], s[40:43], s70 offen nt lds\n"
      "ds_read_b128 a[168:171], %[kb11] offset:256\n"
      "v_mfma_f32_16x16x32_bf16 v[28:31], a[172:175], v[144:147], v[28:31]\n"
      "v_fma_f32 v171, v171, %[csc], v216\n"
      "v_mfma_f32_16x16x32_bf16 v[76:79], a[172:175], v[152:155], v[76:79]\n"
      "v_exp_f32 v170, v170\n"
      "v_mfma_f32_16x16x32_bf16 v[124:127], a[172:175], v[160:163], v[124:127]\n"
      "ds_read_b128 a[172:175], %[kb11] offset:6400\n"
      "v_fma_f32 v172, v172, %[csc], v216\n"
      "v_mfma_f32_16x16x32_bf16 v[32:35], a[176:179], v[144:147], v[32:35]\n"
      "v_exp_f32 v171, v171\n"
      "v_mfma_f32_16x16x32_bf16 v[80:83], a[176:179], v[152:155], v[80:83]\n"
      "v_cvt_pk_bf16_f32 v168, v168, v169\n"
      "v_fma_f32 v173, v173, %[csc], v216\n"
      "v_mfma_f32_16x16x32_bf16 v[128:131], a[176:179], v[160:163], v[128:131]\n"
      "ds_read_b64_tr_b16 a[176:177], %[vb0] offset:0\n"
      "ds_read_b64_tr_b16 a[178:179], %[vb0] offset:6144\n"
      "v_mfma_f32_16x16x32_bf16 v[36:39], a[180:183], v[144:147], v[36:39]\n"
      "v_exp_f32 v172, v172\n"
      "v_mfma_f32_16x16x32_bf16 v[84:87], a[180:183], v[152:155], v[84:87]\n"
      "v_fma_f32 v174, v174, %[csc], v216\n"
      "v_mfma_f32_16x16x32_bf16 v[132:135], a[180:183], v[160:163], v[132:135]\n"
      "s_add_u32 m0, %[m0b], 26624\n"
      "ds_read_b64_tr_b16 a[180:181], %[vb1] offset:0\n"
      "ds_read_b64_tr_b16 a[182:183], %[vb1] offset:6144\n"
      "v_mfma_f32_16x16x32_bf16 v[40:43], a[184:187], v[144:147], v[40:43]\n"
      "buffer_load_dwordx4 %[vk2], s[40:43], s70 offen nt lds\n"
      "v_mfma_f32_16x16x32_bf16 v[88:91], a[184:187], v[152:155], v[88:91]\n"
      "s_add_u32 s73, s69, 4276102144\n"
      "buffer_store_dwordx4 a[228:231], %[ost], s[84:87], s73 offen offset:128 sc1\n"
      "v_mfma_f32_16x16x32_bf16 v[136:139], a[184:187], v[160:163], v[136:139]\n"
      "ds_read_b64_tr_b16 a[184:185], %[vb2] offset:0\n"
      "ds_read_b64_tr_b16 a[186:187], %[vb2] offset:6144\n"
      "v_mfma_f32_16x16x32_bf16 v[44:47], a[188:191], v[144:147], v[44:47]\n"
      "v_exp_f32 v173, v173\n"
      "v_mfma_f32_16x16x32_bf16 v[92:95], a[188:191], v[152:155], v[92:95]\n"
      "v_cvt_pk_bf16_f32 v169, v170, v171\n"
      "v_fma_f32 v175, v175, %[csc], v216\n"
      "v_mfma_f32_16x16x32_bf16 v[140:143], a[188:191], v[160:163], v[140:143]\n"
      "s_waitcnt lgkmcnt(14)\n"
      "v_exp_f32 v174, v174\n"
      "v_mfma_f32_16x16x32_bf16 v[144:147], a[192:195], a[72:75], 0\n"
      "v_fma_f32 v176, v176, %[csc], v217\n"
      "v_mfma_f32_16x16x32_bf16 v[152:155], a[192:195], a[96:99], 0\n"
      "v_exp_f32 v175, v175\n"
      "v_mfma_f32_16x16x32_bf16 v[160:163], a[192:195], a[120:123], 0\n"
      "v_cvt_pk_bf16_f32 v170, v172, v173\n"
      "v_fma_f32 v177, v177, %[csc], v217\n"
      "v_mfma_f32_16x16x32_bf16 v[148:151], a[196:199], a[72:75], 0\n"
      "s_add_u32 s71, s68, 110592\n"
      "v_exp_f32 v176, v176\n"
      "v_mfma_f32_16x16x32_bf16 v[156:159], a[196:199], a[96:99], 0\n"
      "s_add_u32 m0, %[m0b], 110592\n"
      "v_fma_f32 v178, v178, %[csc], v217\n"
      "v_mfma_f32_16x16x32_bf16 v[164:167], a[196:199], a[120:123], 0\n"
      "s_waitcnt vmcnt(27)\n"
      "buffer_load_dwordx4 %[vv0], s[44:47], s71 offen nt lds\n"
      "ds_read_b128 a[32:35], %[qs0] offset:2048\n"
      "v_mfma_f32_16x16x32_bf16 v[144:147], a[200:203], a[76:79], v[144:147]\n"
      "ds_read_b128 a[36:39], %[qs1] offset:2048\n"
      "v_mfma_f32_16x16x32_bf16 v[152:155], a[200:203], a[100:103], v[152:155]\n"
      "v_exp_f32 v177, v177\n"
      "v_mfma_f32_16x16x32_bf16 v[160:163], a[200:203], a[124:127], v[160:163]\n"
      "v_cvt_pk_bf16_f32 v171, v174, v175\n"
      "v_fma_f32 v179, v179, %[csc], v217\n"
      "v_mfma_f32_16x16x32_bf16 v[148:151], a[204:207], a[76:79], v[148:151]\n"
      "v_exp_f32 v178, v178\n"
      "v_mfma_f32_16x16x32_bf16 v[156:159], a[204:207], a[100:103], v[156:159]\n"
      "v_fma_f32 v180, v180, %[csc], v217\n"
      "v_mfma_f32_16x16x32_bf16 v[164:167], a[204:207], a[124:127], v[164:167]\n"
      "s_waitcnt lgkmcnt(12)\n"
      "v_exp_f32 v179, v179\n"
      "v_mfma_f32_16x16x32_bf16 v[144:147], a[144:147], a[80:83], v[144:147]\n"
      "v_cvt_pk_bf16_f32 v176, v176, v177\n"
      "v_fma_f32 v181, v181, %[csc], v217\n"
      "v_mfma_f32_16x16x32_bf16 v[152:155], a[144:147], a[104:107], v[152:155]\n"
      "s_add_u32 m0, %[m0b], 111616\n"
      "v_exp_f32 v180, v180\n"
      "v_mfma_f32_16x16x32_bf16 v[160:163], a[144:147], a[128:131], v[160:163]\n"
      "buffer_load_dwordx4 %[vv1], s[44:47], s71 offen nt lds\n"
      "v_fma_f32 v182, v182, %[csc], v217\n"
      "v_mfma_f32_16x16x32_bf16 v[148:151], a[148:151], a[80:83], v[148:151]\n"
      "v_exp_f32 v181, v181\n"
      "v_mfma_f32_16x16x32_bf16 v[156:159], a[148:151], a[104:107], v[156:159]\n"
      "v_cvt_pk_bf16_f32 v177, v178, v179\n"
      "v_fma_f32 v183, v183, %[csc], v217\n"
      "v_mfma_f32_16x16x32_bf16 v[164:167], a[148:151], a[128:131], v[164:167]\n"
      "v_exp_f32 v182, v182\n"
      "v_mfma_f32_16x16x32_bf16 v[144:147], a[152:155], a[84:87], v[144:147]\n"
      "v_fma_f32 v184, v184, %[csc], v218\n"
      "v_mfma_f32_16x16x32_bf16 v[152:155], a[152:155], a[108:111], v[152:155]\n"
      "v_exp_f32 v183, v183\n"
      "v_mfma_f32_16x16x32_bf16 v[160:163], a[152:155], a[132:135], v[160:163]\n"
      "v_cvt_pk_bf16_f32 v178, v180, v181\n"
      "v_fma_f32 v185, v185, %[csc], v218\n"
      "v_mfma_f32_16x16x32_bf16 v[148:151], a[156:159], a[84:87], v[148:151]\n"
      "v_exp_f32 v184, v184\n"
      "v_mfma_f32_16x16x32_bf16 v[156:159], a[156:159], a[108:111], v[156:159]\n"
      "v_fma_f32 v186, v186, %[csc], v218\n"
      "v_mfma_f32_16x16x32_bf16 v[164:167], a[156:159], a[132:135], v[164:167]\n"
      "s_waitcnt lgkmcnt(8)\n"
      "v_exp_f32 v185, v185\n"
      "v_mfma_f32_16x16x32_bf16 v[144:147], a[160:163], a[88:91], v[144:147]\n"
      "s_add_u32 m0, %[m0b], 112640\n"
      "v_cvt_pk_bf16_f32 v179, v182, v183\n"
      "v_fma_f32 v187, v187, %[csc], v218\n"
      "v_mfma_f32_16x16x32_bf16 v[152:155], a[160:163], a[112:115], v[152:155]\n"
      "v_exp_f32 v186, v186\n"
      "v_mfma_f32_16x16x32_bf16 v[160:163], a[160:163], a[136:139], v[160:163]\n"
      "buffer_load_dwordx4 %[vv2], s[44:47], s71 offen nt lds\n"
      "v_fma_f32 v188, v188, %[csc], v218\n"
      "v_mfma_f32_16x16x32_bf16 v[148:151], a[164:167], a[88:91], v[148:151]\n"
      "v_exp_f32 v187, v187\n"
      "v_mfma_f32_16x16x32_bf16 v[156:159], a[164:167], a[112:115], v[156:159]\n"
      "v_cvt_pk_bf16_f32 v184, v184, v185\n"
      "v_fma_f32 v189, v189, %[csc], v218\n"
      "v_mfma_f32_16x16x32_bf16 v[164:167], a[164:167], a[136:139], v[164:167]\n"
      "v_exp_f32 v188, v188\n"
      "v_mfma_f32_16x16x32_bf16 v[144:147], a[168:171], a[92:95], v[144:147]\n"
      "s_waitcnt lgkmcnt(0)\n"
      "v_fma_f32 v190, v190, %[csc], v218\n"
      "v_mfma_f32_16x16x32_bf16 v[152:155], a[168:171], a[116:119], v[152:155]\n"
      "s_add_u32 s72, s69, 18880768\n"
      "v_exp_f32 v189, v189\n"
      "v_mfma_f32_16x16x32_bf16 v[160:163], a[168:171], a[140:143], v[160:163]\n"
      "s_add_u32 m0, %[m0q], 2048\n"
      "v_cvt_pk_bf16_f32 v185, v186, v187\n"
      "v_fma_f32 v191, v191, %[csc], v218\n"
      "v_mfma_f32_16x16x32_bf16 v[148:151], a[172:175], a[92:95], v[148:151]\n"
      "v_exp_f32 v190, v190\n"
      "v_mfma_f32_16x16x32_bf16 v[156:159], a[172:175], a[116:119], v[156:159]\n"
      "v_exp_f32 v191, v191\n"
      "v_mfma_f32_16x16x32_bf16 v[164:167], a[172:175], a[140:143], v[164:167]\n"
      "buffer_load_dwordx4 %[vq0], s[64:67], s72 offen nt lds\n"
      "v_cvt_pk_bf16_f32 v186, v188, v189\n"
      "v_cvt_pk_bf16_f32 v187, v190, v191\n"
      "v_mfma_f32_16x16x32_bf16 a[208:211], a[220:223], v[168:171], a[208:211]\n"
      "s_add_u32 m0, %[m0q], 3072\n"
      "ds_read_b64_tr_b16 a[188:189], %[vb3] offset:0\n"
      "ds_read_b64_tr_b16 a[190:191], %[vb3] offset:6144\n"
      "v_mfma_f32_16x16x32_bf16 a[212:215], a[220:223], v[176:179], a[212:215]\n"
      "ds_read_b64_tr_b16 a[192:193], %[vb0] offset:128\n"
      "ds_read_b64_tr_b16 a[194:195], %[vb0] offset:6272\n"
      "v_mfma_f32_16x16x32_bf16 a[216:219], a[220:223], v[184:187], a[216:219]\n"
      "ds_read_b64_tr_b16 a[196:197], %[vb1] offset:128\n"
      "ds_read_b64_tr_b16 a[198:199], %[vb1] offset:6272\n"
      "v_mfma_f32_16x16x32_bf16 v[0:3], a[176:179], v[168:171], v[0:3]\n"
      "ds_read_b64_tr_b16 a[200:201], %[vb2] offset:128\n"
      "ds_read_b64_tr_b16 a[202:203], %[vb2] offset:6272\n"
      "v_mfma_f32_16x16x32_bf16 v[48:51], a[176:179], v[176:179], v[48:51]\n"
      "ds_read_b64_tr_b16 a[204:205], %[vb3] offset:128\n"
      "ds_read_b64_tr_b16 a[206:207], %[vb3] offset:6272\n"
      "v_mfma_f32_16x16x32_bf16 v[96:99], a[176:179], v[184:187], v[96:99]\n"
      "ds_read_b64_tr_b16 a[144:145], %[vb0] offset:256\n"
      "ds_read_b64_tr_b16 a[146:147], %[vb0] offset:6400\n"
      "v_mfma_f32_16x16x32_bf16 v[4:7], a[180:183], v[168:171], v[4:7]\n"
      "ds_read_b64_tr_b16 a[148:149], %[vb1] offset:256\n"
      "ds_read_b64_tr_b16 a[150:151], %[vb1] offset:6400\n"
      "v_mfma_f32_16x16x32_bf16 v[52:55], a[180:183], v[176:179], v[52:55]\n"
      "ds_read_b64_tr_b16 a[152:153], %[vb2] offset:256\n"
      "ds_read_b64_tr_b16 a[154:155], %[vb2] offset:6400\n"
      "v_mfma_f32_16x16x32_bf16 v[100:103], a[180:183], v[184:187], v[100:103]\n"
      "ds_read_b64_tr_b16 a[156:157], %[vb3] offset:256\n"
      "ds_read_b64_tr_b16 a[158:159], %[vb3] offset:6400\n"
      "v_mfma_f32_16x16x32_bf16 v[8:11], a[184:187], v[168:171], v[8:11]\n"
      "buffer_load_dwordx4 %[vq1], s[64:67], s72 offen nt lds\n"
      "ds_write_b64 %[ow], a[56:57] offset:0\n"
      "v_mfma_f32_16x16x32_bf16 v[56:59], a[184:187], v[176:179], v[56:59]\n"
      "ds_write_b64 %[ow], a[58:59] offset:32\n"
      "v_fma_f32 v144, v144, %[csc], v216\n"
      "v_mfma_f32_16x16x32_bf16 v[104:107], a[184:187], v[184:187], v[104:107]\n"
      "s_waitcnt lgkmcnt(14)\n"
      "ds_write_b64 %[ow], a[60:61] offset:64\n"
      "v_fma_f32 v145, v145, %[csc], v216\n"
      "v_mfma_f32_16x16x32_bf16 v[12:15], a[188:191], v[168:171], v[12:15]\n"
      "v_exp_f32 v144, v144\n"
      "v_mfma_f32_16x16x32_bf16 v[60:63], a[188:191], v[176:179], v[60:63]\n"
      "ds_write_b64 %[ow], a[62:63] offset:96\n"
      "v_fma_f32 v146, v146, %[csc], v216\n"
      "s_waitcnt vmcnt(20) lgkmcnt(0)\n"
      "s_barrier\n"
      "v_mfma_f32_16x16x32_bf16 v[108:111], a[188:191], v[184:187], v[108:111]\n"
      "s_add_u32 s70, s68, 135168\n"
      "ds_read_b128 a[160:163], %[kb00] offset:0\n"
      "ds_read_b128 a[164:167], %[kb00] offset:6144\n"
      "v_mfma_f32_16x16x32_bf16 v[16:19], a[192:195], v[168:171], v[16:19]\n"
      "s_add_u32 m0, %[m0b], 36864\n"
      "ds_read_b128 a[168:171], %[kb10] offset:0\n"
      "ds_read_b128 a[172:175], %[kb10] offset:6144\n"
      "v_mfma_f32_16x16x32_bf16 v[64:67], a[192:195], v[176:179], v[64:67]\n"
      "buffer_load_dwordx4 %[vk0], s[40:43], s70 offen nt lds\n"
      "ds_read_b128 a[176:179], %[kb00] offset:128\n"
      "v_mfma_f32_16x16x32_bf16 v[112:115], a[192:195], v[184:187], v[112:115]\n"
      "ds_read_b128 a[180:183], %[kb00] offset:6272\n"
      "ds_read_b128 a[184:187], %[kb10] offset:128\n"
      "v_mfma_f32_16x16x32_bf16 v[20:23], a[196:199], v[168:171], v[20:23]\n"
      "ds_read_b128 a[188:191], %[kb10] offset:6272\n"
      "ds_read_b128 a[192:195], %[kb00] offset:256\n"
      "v_mfma_f32_16x16x32_bf16 v[68:71], a[196:199], v[176:179], v[68:71]\n"
      "ds_read_b128 a[232:235], %[orb] offset:0\n"
      "v_mfma_f32_16x16x32_bf16 v[116:119], a[196:199], v[184:187], v[116:119]\n"
      "ds_read_b128 a[196:199], %[kb00] offset:6400\n"
      "ds_read_b128 a[236:239], %[orb] offset:1152\n"
      "v_mfma_f32_16x16x32_bf16 v[24:27], a[200:203], v[168:171], v[24:27]\n"
      "v_exp_f32 v145, v145\n"
      "v_mfma_f32_16x16x32_bf16 v[72:75], a[200:203], v[176:179], v[72:75]\n"
      "v_fma_f32 v147, v147, %[csc], v216\n"
      "v_mfma_f32_16x16x32_bf16 v[120:123], a[200:203], v[184:187], v[120:123]\n"
      "ds_read_b128 a[200:203], %[kb10] offset:256\n"
      "v_mfma_f32_16x16x32_bf16 v[28:31], a[204:207], v[168:171], v[28:31]\n"
      "s_add_u32 m0, %[m0b], 37888\n"
      "v_exp_f32 v146, v146\n"
      "v_mfma_f32_16x16x32_bf16 v[76:79], a[204:207], v[176:179], v[76:79]\n"
      "buffer_load_dwordx4 %[vk1], s[40:43], s70 offen nt lds\n"
      "v_fma_f32 v148, v148, %[csc], v216\n"
      "v_mfma_f32_16x16x32_bf16 v[124:127], a[204:207], v[184:187], v[124:127]\n"
      "ds_read_b128 a[204:207], %[kb10] offset:6400\n"
      "v_mfma_f32_16x16x32_bf16 v[32:35], a[144:147], v[168:171], v[32:35]\n"
      "v_exp_f32 v147, v147\n"
      "v_mfma_f32_16x16x32_bf16 v[80:83], a[144:147], v[176:179], v[80:83]\n"
      "v_cvt_pk_bf16_f32 v144, v144, v145\n"
      "v_fma_f32 v149, v149, %[csc], v216\n"
      "v_mfma_f32_16x16x32_bf16 v[128:131], a[144:147], v[184:187], v[128:131]\n"
      "ds_read_b64_tr_b16 a[144:145], %[vb0] offset:12288\n"
      "ds_read_b64_tr_b16 a[146:147], %[vb0] offset:18432\n"
      "v_mfma_f32_16x16x32_bf16 v[36:39], a[148:151], v[168:171], v[36:39]\n"
      "s_waitcnt lgkmcnt(6)\n"
      "v_exp_f32 v148, v148\n"
      "v_mfma_f32_16x16x32_bf16 v[84:87], a[148:151], v[176:179], v[84:87]\n"
      "s_add_u32 s73, s69, 4276105216\n"
      "buffer_store_dwordx4 a[232:235], %[ost], s[84:87], s73 offen offset:128 sc1\n"
      "v_fma_f32 v150, v150, %[csc], v216\n"
      "v_mfma_f32_16x16x32_bf16 v[132:135], a[148:151], v[184:187], v[132:135]\n"
      "ds_read_b64_tr_b16 a[148:149], %[vb1] offset:12288\n"
      "ds_read_b64_tr_b16 a[150:151], %[vb1] offset:18432\n"
      "v_mfma_f32_16x16x32_bf16 v[40:43], a[152:155], v[168:171], v[40:43]\n"
      "v_exp_f32 v149, v149\n"
      "v_mfma_f32_16x16x32_bf16 v[88:91], a[152:155], v[176:179], v[88:91]\n"
      "s_add_u32 m0, %[m0b], 38912\n"
      "v_cvt_pk_bf16_f32 v145, v146, v147\n"
      "v_fma_f32 v151, v151, %[csc], v216\n"
      "v_mfma_f32_16x16x32_bf16 v[136:139], a[152:155], v[184:187], v[136:139]\n"
      "buffer_load_dwordx4 %[vk2], s[40:43], s70 offen nt lds\n"
      "v_mfma_f32_16x16x32_bf16 v[44:47], a[156:159], v[168:171], v[44:47]\n"
      "ds_read_b64_tr_b16 a[152:153], %[vb2] offset:12288\n"
      "ds_read_b64_tr_b16 a[154:155], %[vb2] offset:18432\n"
      "v_mfma_f32_16x16x32_bf16 v[92:95], a[156:159], v[176:179], v[92:95]\n"
      "v_exp_f32 v150, v150\n"
      "v_mfma_f32_16x16x32_bf16 v[140:143], a[156:159], v[184:187], v[140:143]\n"
      "v_fma_f32 v152, v152, %[csc], v217\n"
      "v_mfma_f32_16x16x32_bf16 v[168:171], a[160:163], a[72:75], 0\n"
      "v_exp_f32 v151, v151\n"
      "v_mfma_f32_16x16x32_bf16 v[176:179], a[160:163], a[96:99], 0\n"
      "v_cvt_pk_bf16_f32 v146, v148, v149\n"
      "v_fma_f32 v153, v153, %[csc], v217\n"
      "v_mfma_f32_16x16x32_bf16 v[184:187], a[160:163], a[120:123], 0\n"
      "v_exp_f32 v152, v152\n"
      "v_mfma_f32_16x16x32_bf16 v[172:175], a[164:167], a[72:75], 0\n"
      "s_waitcnt lgkmcnt(8)\n"
      "v_fma_f32 v154, v154, %[csc], v217\n"
      "v_mfma_f32_16x16x32_bf16 v[180:183], a[164:167], a[96:99], 0\n"
      "s_add_u32 s73, s69, 4276108288\n"
      "buffer_store_dwordx4 a[236:239], %[ost], s[84:87], s73 offen offset:128 sc1\n"
      "v_mfma_f32_16x16x32_bf16 v[188:191], a[164:167], a[120:123], 0\n"
      "s_add_u32 s71, s68, 122880\n"
      "v_exp_f32 v153, v153\n"
      "v_mfma_f32_16x16x32_bf16 v[168:171], a[168:171], a[76:79], v[168:171]\n"
      "s_add_u32 m0, %[m0b], 73728\n"
      "v_cvt_pk_bf16_f32 v147, v150, v151\n"
      "v_fma_f32 v155, v155, %[csc], v217\n"
      "v_mfma_f32_16x16x32_bf16 v[176:179], a[168:171], a[100:103], v[176:179]\n"
      "buffer_load_dwordx4 %[vv0], s[44:47], s71 offen nt lds\n"
      "v_mfma_f32_16x16x32_bf16 v[184:187], a[168:171], a[124:127], v[184:187]\n"
      "v_exp_f32 v154, v154\n"
      "v_mfma_f32_16x16x32_bf16 v[172:175], a[172:175], a[76:79], v[172:175]\n"
      "v_fma_f32 v156, v156, %[csc], v217\n"
      "v_mfma_f32_16x16x32_bf16 v[180:183], a[172:175], a[100:103], v[180:183]\n"
      "v_exp_f32 v155, v155\n"
      "v_mfma_f32_16x16x32_bf16 v[188:191], a[172:175], a[124:127], v[188:191]\n"
      "v_cvt_pk_bf16_f32 v152, v152, v153\n"
      "v_fma_f32 v157, v157, %[csc], v217\n"
      "v_mfma_f32_16x16x32_bf16 v[168:171], a[176:179], a[80:83], v[168:171]\n"
      "v_exp_f32 v156, v156\n"
      "v_mfma_f32_16x16x32_bf16 v[176:179], a[176:179], a[104:107], v[176:179]\n"
      "v_fma_f32 v158, v158, %[csc], v217\n"
      "v_mfma_f32_16x16x32_bf16 v[184:187], a[176:179], a[128:131], v[184:187]\n"
      "v_exp_f32 v157, v157\n"
      "v_mfma_f32_16x16x32_bf16 v[172:175], a[180:183], a[80:83], v[172:175]\n"
      "s_add_u32 m0, %[m0b], 74752\n"
      "v_cvt_pk_bf16_f32 v153, v154, v155\n"
      "v_fma_f32 v159, v159, %[csc], v217\n"
      "v_mfma_f32_16x16x32_bf16 v[180:183], a[180:183], a[104:107], v[180:183]\n"
      "s_waitcnt vmcnt(27)\n"
      "buffer_load_dwordx4 %[vv1], s[44:47], s71 offen nt lds\n"
      "ds_read_b128 a[56:59], %[qs0] offset:4096\n"
      "v_mfma_f32_16x16x32_bf16 v[188:191], a[180:183], a[128:131], v[188:191]\n"
      "v_exp_f32 v158, v158\n"
      "v_mfma_f32_16x16x32_bf16 v[168:171], a[184:187], a[84:87], v[168:171]\n"
      "ds_read_b128 a[60:63], %[qs1] offset:4096\n"
      "v_fma_f32 v160, v160, %[csc], v218\n"
      "v_mfma_f32_16x16x32_bf16 v[176:179], a[184:187], a[108:111], v[176:179]\n"
      "v_exp_f32 v159, v159\n"
      "v_mfma_f32_16x16x32_bf16 v[184:187], a[184:187], a[132:135], v[184:187]\n"
      "v_cvt_pk_bf16_f32 v154, v156, v157\n"
      "v_fma_f32 v161, v161, %[csc], v218\n"
      "v_mfma_f32_16x16x32_bf16 v[172:175], a[188:191], a[84:87], v[172:175]\n"
      "v_exp_f32 v160, v160\n"
      "v_mfma_f32_16x16x32_bf16 v[180:183], a[188:191], a[108:111], v[180:183]\n"
      "v_fma_f32 v162, v162, %[csc], v218\n"
      "v_mfma_f32_16x16x32_bf16 v[188:191], a[188:191], a[132:135], v[188:191]\n"
      "v_exp_f32 v161, v161\n"
      "v_mfma_f32_16x16x32_bf16 v[168:171], a[192:195], a[88:91], v[168:171]\n"
      "v_cvt_pk_bf16_f32 v155, v158, v159\n"
      "v_fma_f32 v163, v163, %[csc], v218\n"
      "v_mfma_f32_16x16x32_bf16 v[176:179], a[192:195], a[112:115], v[176:179]\n"
      "s_add_u32 m0, %[m0b], 75776\n"
      "v_exp_f32 v162, v162\n"
      "v_mfma_f32_16x16x32_bf16 v[184:187], a[192:195], a[136:139], v[184:187]\n"
      "buffer_load_dwordx4 %[vv2], s[44:47], s71 offen nt lds\n"
      "v_fma_f32 v164, v164, %[csc], v218\n"
      "v_mfma_f32_16x16x32_bf16 v[172:175], a[196:199], a[88:91], v[172:175]\n"
      "v_exp_f32 v163, v163\n"
      "v_mfma_f32_16x16x32_bf16 v[180:183], a[196:199], a[112:115], v[180:183]\n"
      "v_cvt_pk_bf16_f32 v160, v160, v161\n"
      "v_fma_f32 v165, v165, %[csc], v218\n"
      "v_mfma_f32_16x16x32_bf16 v[188:191], a[196:199], a[136:139], v[188:191]\n"
      "s_waitcnt lgkmcnt(4)\n"
      "v_exp_f32 v164, v164\n"
      "v_mfma_f32_16x16x32_bf16 v[168:171], a[200:203], a[92:95], v[168:171]\n"
      "v_fma_f32 v166, v166, %[csc], v218\n"
      "v_mfma_f32_16x16x32_bf16 v[176:179], a[200:203], a[116:119], v[176:179]\n"
      "v_exp_f32 v165, v165\n"
      "v_mfma_f32_16x16x32_bf16 v[184:187], a[200:203], a[140:143], v[184:187]\n"
      "v_cvt_pk_bf16_f32 v161, v162, v163\n"
      "v_fma_f32 v167, v167, %[csc], v218\n"
      "v_mfma_f32_16x16x32_bf16 v[172:175], a[204:207], a[92:95], v[172:175]\n"
      "v_exp_f32 v166, v166\n"
      "v_mfma_f32_16x16x32_bf16 v[180:183], a[204:207], a[116:119], v[180:183]\n"
      "v_exp_f32 v167, v167\n"
      "v_mfma_f32_16x16x32_bf16 v[188:191], a[204:207], a[140:143], v[188:191]\n"
      "v_cvt_pk_bf16_f32 v162, v164, v165\n"
      "v_cvt_pk_bf16_f32 v163, v166, v167\n"
      "v_mfma_f32_16x16x32_bf16 a[208:211], a[220:223], v[144:147], a[208:211]\n"
      "ds_read_b64_tr_b16 a[156:157], %[vb3] offset:12288\n"
      "ds_read_b64_tr_b16 a[158:159], %[vb3] offset:18432\n"
      "v_mfma_f32_16x16x32_bf16 a[212:215], a[220:223], v[152:155], a[212:215]\n"
      "ds_read_b64_tr_b16 a[160:161], %[vb0] offset:12416\n"
      "ds_read_b64_tr_b16 a[162:163], %[vb0] offset:18560\n"
      "v_mfma_f32_16x16x32_bf16 a[216:219], a[220:223], v[160:163], a[216:219]\n"
      "ds_read_b64_tr_b16 a[164:165], %[vb1] offset:12416\n"
      "ds_read_b64_tr_b16 a[166:167], %[vb1] offset:18560\n"
      "v_mfma_f32_16x16x32_bf16 v[0:3], a[144:147], v[144:147], v[0:3]\n"
      "ds_read_b64_tr_b16 a[168:169], %[vb2] offset:12416\n"
      "ds_read_b64_tr_b16 a[170:171], %[vb2] offset:18560\n"
      "v_mfma_f32_16x16x32_bf16 v[48:51], a[144:147], v[152:155], v[48:51]\n"
      "s_waitcnt lgkmcnt(8)\n"
      "ds_read_b64_tr_b16 a[172:173], %[vb3] offset:12416\n"
      "ds_read_b64_tr_b16 a[174:175], %[vb3] offset:18560\n"
      "v_mfma_f32_16x16x32_bf16 v[96:99], a[144:147], v[160:163], v[96:99]\n"
      "s_add_u32 s72, s69, 18886912\n"
      "ds_read_b64_tr_b16 a[176:177], %[vb0] offset:12544\n"
      "ds_read_b64_tr_b16 a[178:179], %[vb0] offset:18688\n"
      "v_mfma_f32_16x16x32_bf16 v[4:7], a[148:151], v[144:147], v[4:7]\n"
      "s_add_u32 m0, %[m0q], 4096\n"
      "ds_read_b64_tr_b16 a[180:181], %[vb1] offset:12544\n"
      "ds_read_b64_tr_b16 a[182:183], %[vb1] offset:18688\n"
      "v_mfma_f32_16x16x32_bf16 v[52:55], a[148:151], v[152:155], v[52:55]\n"
      "ds_read_b64_tr_b16 a[184:185], %[vb2] offset:12544\n"
      "ds_read_b64_tr_b16 a[186:187], %[vb2] offset:18688\n"
      "v_mfma_f32_16x16x32_bf16 v[100:103], a[148:151], v[160:163], v[100:103]\n"
      "ds_read_b64_tr_b16 a[188:189], %[vb3] offset:12544\n"
      "ds_read_b64_tr_b16 a[190:191], %[vb3] offset:18688\n"
      "v_mfma_f32_16x16x32_bf16 v[8:11], a[152:155], v[144:147], v[8:11]\n"
      "buffer_load_dwordx4 %[vq0], s[64:67], s72 offen nt lds\n"
      "v_fma_f32 v168, v168, %[csc], v216\n"
      "v_mfma_f32_16x16x32_bf16 v[56:59], a[152:155], v[152:155], v[56:59]\n"
      "s_add_u32 m0, %[m0q], 5120\n"
      "v_fma_f32 v169, v169, %[csc], v216\n"
      "v_mfma_f32_16x16x32_bf16 v[104:107], a[152:155], v[160:163], v[104:107]\n"
      "s_waitcnt lgkmcnt(12)\n"
      "buffer_load_dwordx4 %[vq1], s[64:67], s72 offen nt lds\n"
      "v_mfma_f32_16x16x32_bf16 v[12:15], a[156:159], v[144:147], v[12:15]\n"
      "v_exp_f32 v168, v168\n"
      "v_mfma_f32_16x16x32_bf16 v[60:63], a[156:159], v[152:155], v[60:63]\n"
      "v_fma_f32 v170, v170, %[csc], v216\n"
      "s_waitcnt vmcnt(22) lgkmcnt(0)\n"
      "s_barrier\n"
      "v_mfma_f32_16x16x32_bf16 v[108:111], a[156:159], v[160:163], v[108:111]\n"
      "s_add_u32 s70, s68, 18874368\n"
      "ds_read_b128 a[192:195], %[kb00] offset:12288\n"
      "ds_read_b128 a[196:199], %[kb00] offset:18432\n"
      "v_mfma_f32_16x16x32_bf16 v[16:19], a[160:163], v[144:147], v[16:19]\n"
      "s_add_u32 m0, %[m0b], 0\n"
      "ds_read_b128 a[200:203], %[kb10] offset:12288\n"
      "ds_read_b128 a[204:207], %[kb10] offset:18432\n"
      "v_mfma_f32_16x16x32_bf16 v[64:67], a[160:163], v[152:155], v[64:67]\n"
      "buffer_load_dwordx4 %[vk0], s[56:59], s70 offen nt lds\n"
      "ds_read_b128 a[144:147], %[kb00] offset:12416\n"
      "v_mfma_f32_16x16x32_bf16 v[112:115], a[160:163], v[160:163], v[112:115]\n"
      "ds_read_b128 a[148:151], %[kb00] offset:18560\n"
      "ds_read_b128 a[152:155], %[kb10] offset:12416\n"
      "v_mfma_f32_16x16x32_bf16 v[20:23], a[164:167], v[144:147], v[20:23]\n"
      "ds_read_b128 a[156:159], %[kb10] offset:18560\n"
      "ds_read_b128 a[160:163], %[kb00] offset:12544\n"
      "v_mfma_f32_16x16x32_bf16 v[68:71], a[164:167], v[152:155], v[68:71]\n"
      "ds_write_b64 %[ow], a[16:17] offset:0\n"
      "v_mfma_f32_16x16x32_bf16 v[116:119], a[164:167], v[160:163], v[116:119]\n"
      "ds_read_b128 a[164:167], %[kb00] offset:18688\n"
      "ds_write_b64 %[ow], a[18:19] offset:32\n"
      "v_mfma_f32_16x16x32_bf16 v[24:27], a[168:171], v[144:147], v[24:27]\n"
      "ds_write_b64 %[ow], a[20:21] offset:64\n"
      "v_mfma_f32_16x16x32_bf16 v[72:75], a[168:171], v[152:155], v[72:75]\n"
      "v_exp_f32 v169, v169\n"
      "v_mfma_f32_16x16x32_bf16 v[120:123], a[168:171], v[160:163], v[120:123]\n"
      "ds_read_b128 a[168:171], %[kb10] offset:12544\n"
      "ds_write_b64 %[ow], a[22:23] offset:96\n"
      "v_mfma_f32_16x16x32_bf16 v[28:31], a[172:175], v[144:147], v[28:31]\n"
      "s_add_u32 m0, %[m0b], 1024\n"
      "v_fma_f32 v171, v171, %[csc], v216\n"
      "v_mfma_f32_16x16x32_bf16 v[76:79], a[172:175], v[152:155], v[76:79]\n"
      "buffer_load_dwordx4 %[vk1], s[56:59], s70 offen nt lds\n"
      "v_mfma_f32_16x16x32_bf16 v[124:127], a[172:175], v[160:163], v[124:127]\n"
      "ds_read_b128 a[172:175], %[kb10] offset:18688\n"
      "v_mfma_f32_16x16x32_bf16 v[32:35], a[176:179], v[144:147], v[32:35]\n"
      "v_exp_f32 v170, v170\n"
      "v_mfma_f32_16x16x32_bf16 v[80:83], a[176:179], v[152:155], v[80:83]\n"
      "ds_read_b128 a[224:227], %[orb] offset:0\n"
      "v_fma_f32 v172, v172, %[csc], v216\n"
      "v_mfma_f32_16x16x32_bf16 v[128:131], a[176:179], v[160:163], v[128:131]\n"
      "ds_read_b64_tr_b16 a[176:177], %[vb0] offset:24576\n"
      "ds_read_b64_tr_b16 a[178:179], %[vb0] offset:30720\n"
      "v_mfma_f32_16x16x32_bf16 v[36:39], a[180:183], v[144:147], v[36:39]\n"
      "v_exp_f32 v171, v171\n"
      "v_mfma_f32_16x16x32_bf16 v[84:87], a[180:183], v[152:155], v[84:87]\n"
      "v_cvt_pk_bf16_f32 v168, v168, v169\n"
      "v_fma_f32 v173, v173, %[csc], v216\n"
      "v_mfma_f32_16x16x32_bf16 v[132:135], a[180:183], v[160:163], v[132:135]\n"
      "ds_read_b64_tr_b16 a[180:181], %[vb1] offset:24576\n"
      "ds_read_b64_tr_b16 a[182:183], %[vb1] offset:30720\n"
      "v_mfma_f32_16x16x32_bf16 v[40:43], a[184:187], v[144:147], v[40:43]\n"
      "v_exp_f32 v172, v172\n"
      "v_mfma_f32_16x16x32_bf16 v[88:91], a[184:187], v[152:155], v[88:91]\n"
      "s_add_u32 m0, %[m0b], 2048\n"
      "ds_read_b128 a[228:231], %[orb] offset:1152\n"
      "v_fma_f32 v174, v174, %[csc], v216\n"
      "v_mfma_f32_16x16x32_bf16 v[136:139], a[184:187], v[160:163], v[136:139]\n"
      "buffer_load_dwordx4 %[vk2], s[56:59], s70 offen nt lds\n"
      "v_mfma_f32_16x16x32_bf16 v[44:47], a[188:191], v[144:147], v[44:47]\n"
      "ds_read_b64_tr_b16 a[184:185], %[vb2] offset:24576\n"
      "ds_read_b64_tr_b16 a[186:187], %[vb2] offset:30720\n"
      "v_mfma_f32_16x16x32_bf16 v[92:95], a[188:191], v[152:155], v[92:95]\n"
      "v_exp_f32 v173, v173\n"
      "v_mfma_f32_16x16x32_bf16 v[140:143], a[188:191], v[160:163], v[140:143]\n"
      "s_waitcnt lgkmcnt(15)\n"
      "v_cvt_pk_bf16_f32 v169, v170, v171\n"
      "v_fma_f32 v175, v175, %[csc], v216\n"
      "v_mfma_f32_16x16x32_bf16 v[144:147], a[192:195], a[72:75], 0\n"
      "v_exp_f32 v174, v174\n"
      "v_mfma_f32_16x16x32_bf16 v[152:155], a[192:195], a[96:99], 0\n"
      "s_waitcnt lgkmcnt(7)\n"
      "v_fma_f32 v176, v176, %[csc], v217\n"
      "v_mfma_f32_16x16x32_bf16 v[160:163], a[192:195], a[120:123], 0\n"
      "s_add_u32 s73, s69, 4276092928\n"
      "buffer_store_dwordx4 a[224:227], %[ost], s[84:87], s73 offen offset:256 sc1\n"
      "v_mfma_f32_16x16x32_bf16 v[148:151], a[196:199], a[72:75], 0\n"
      "v_exp_f32 v175, v175\n"
      "v_mfma_f32_16x16x32_bf16 v[156:159], a[196:199], a[96:99], 0\n"
      "v_cvt_pk_bf16_f32 v170, v172, v173\n"
      "v_fma_f32 v177, v177, %[csc], v217\n"
      "v_mfma_f32_16x16x32_bf16 v[164:167], a[196:199], a[120:123], 0\n"
      "s_add_u32 s71, s68, 135168\n"
      "v_exp_f32 v176, v176\n"
      "v_mfma_f32_16x16x32_bf16 v[144:147], a[200:203], a[76:79], v[144:147]\n"
      "s_add_u32 m0, %[m0b], 86016\n"
      "v_fma_f32 v178, v178, %[csc], v217\n"
      "v_mfma_f32_16x16x32_bf16 v[152:155], a[200:203], a[100:103], v[152:155]\n"
      "buffer_load_dwordx4 %[vv0], s[44:47], s71 offen nt lds\n"
      "v_mfma_f32_16x16x32_bf16 v[160:163], a[200:203], a[124:127], v[160:163]\n"
      "v_exp_f32 v177, v177\n"
      "v_mfma_f32_16x16x32_bf16 v[148:151], a[204:207], a[76:79], v[148:151]\n"
      "v_cvt_pk_bf16_f32 v171, v174, v175\n"
      "v_fma_f32 v179, v179, %[csc], v217\n"
      "v_mfma_f32_16x16x32_bf16 v[156:159], a[204:207], a[100:103], v[156:159]\n"
      "v_exp_f32 v178, v178\n"
      "v_mfma_f32_16x16x32_bf16 v[164:167], a[204:207], a[124:127], v[164:167]\n"
      "v_fma_f32 v180, v180, %[csc], v217\n"
      "v_mfma_f32_16x16x32_bf16 v[144:147], a[144:147], a[80:83], v[144:147]\n"
      "v_exp_f32 v179, v179\n"
      "v_mfma_f32_16x16x32_bf16 v[152:155], a[144:147], a[104:107], v[152:155]\n"
      "s_waitcnt lgkmcnt(2)\n"
      "v_cvt_pk_bf16_f32 v176, v176, v177\n"
      "v_fma_f32 v181, v181, %[csc], v217\n"
      "v_mfma_f32_16x16x32_bf16 v[160:163], a[144:147], a[128:131], v[160:163]\n"
      "s_add_u32 s73, s69, 4276096000\n"
      "buffer_store_dwordx4 a[228:231], %[ost], s[84:87], s73 offen offset:256 sc1\n"
      "v_mfma_f32_16x16x32_bf16 v[148:151], a[148:151], a[80:83], v[148:151]\n"
      "s_add_u32 m0, %[m0b], 87040\n"
      "v_exp_f32 v180, v180\n"
      "v_mfma_f32_16x16x32_bf16 v[156:159], a[148:151], a[104:107], v[156:159]\n"
      "buffer_load_dwordx4 %[vv1], s[44:47], s71 offen nt lds\n"
      "v_fma_f32 v182, v182, %[csc], v217\n"
      "v_mfma_f32_16x16x32_bf16 v[164:167], a[148:151], a[128:131], v[164:167]\n"
      "v_exp_f32 v181, v181\n"
      "v_mfma_f32_16x16x32_bf16 v[144:147], a[152:155], a[84:87], v[144:147]\n"
      "v_cvt_pk_bf16_f32 v177, v178, v179\n"
      "v_fma_f32 v183, v183, %[csc], v217\n"
      "v_mfma_f32_16x16x32_bf16 v[152:155], a[152:155], a[108:111], v[152:155]\n"
      "v_exp_f32 v182, v182\n"
      "v_mfma_f32_16x16x32_bf16 v[160:163], a[152:155], a[132:135], v[160:163]\n"
      "v_fma_f32 v184, v184, %[csc], v218\n"
      "v_mfma_f32_16x16x32_bf16 v[148:151], a[156:159], a[84:87], v[148:151]\n"
      "v_exp_f32 v183, v183\n"
      "v_mfma_f32_16x16x32_bf16 v[156:159], a[156:159], a[108:111], v[156:159]\n"
      "v_cvt_pk_bf16_f32 v178, v180, v181\n"
      "v_fma_f32 v185, v185, %[csc], v218\n"
      "v_mfma_f32_16x16x32_bf16 v[164:167], a[156:159], a[132:135], v[164:167]\n"
      "v_exp_f32 v184, v184\n"
      "v_mfma_f32_16x16x32_bf16 v[144:147], a[160:163], a[88:91], v[144:147]\n"
      "v_fma_f32 v186, v186, %[csc], v218\n"
      "v_mfma_f32_16x16x32_bf16 v[152:155], a[160:163], a[112:115], v[152:155]\n"
      "s_add_u32 m0, %[m0b], 88064\n"
      "v_exp_f32 v185, v185\n"
      "v_mfma_f32_16x16x32_bf16 v[160:163], a[160:163], a[136:139], v[160:163]\n"
      "s_waitcnt vmcnt(27)\n"
      "v_cvt_pk_bf16_f32 v179, v182, v183\n"
      "v_fma_f32 v187, v187, %[csc], v218\n"
      "v_mfma_f32_16x16x32_bf16 v[148:151], a[164:167], a[88:91], v[148:151]\n"
      "v_exp_f32 v186, v186\n"
      "v_mfma_f32_16x16x32_bf16 v[156:159], a[164:167], a[112:115], v[156:159]\n"
      "buffer_load_dwordx4 %[vv2], s[44:47], s71 offen nt lds\n"
      "v_fma_f32 v188, v188, %[csc], v218\n"
      "v_mfma_f32_16x16x32_bf16 v[164:167], a[164:167], a[136:139], v[164:167]\n"
      "v_exp_f32 v187, v187\n"
      "v_mfma_f32_16x16x32_bf16 v[144:147], a[168:171], a[92:95], v[144:147]\n"
      "v_cvt_pk_bf16_f32 v184, v184, v185\n"
      "v_fma_f32 v189, v189, %[csc], v218\n"
      "v_mfma_f32_16x16x32_bf16 v[152:155], a[168:171], a[116:119], v[152:155]\n"
      "v_exp_f32 v188, v188\n"
      "v_mfma_f32_16x16x32_bf16 v[160:163], a[168:171], a[140:143], v[160:163]\n"
      "ds_read_b128 a[16:19], %[qs0] offset:0\n"
      "v_fma_f32 v190, v190, %[csc], v218\n"
      "v_mfma_f32_16x16x32_bf16 v[148:151], a[172:175], a[92:95], v[148:151]\n"
      "v_exp_f32 v189, v189\n"
      "v_mfma_f32_16x16x32_bf16 v[156:159], a[172:175], a[116:119], v[156:159]\n"
      "v_cvt_pk_bf16_f32 v185, v186, v187\n"
      "v_fma_f32 v191, v191, %[csc], v218\n"
      "v_mfma_f32_16x16x32_bf16 v[164:167], a[172:175], a[140:143], v[164:167]\n"
      "v_exp_f32 v190, v190\n"
      "v_exp_f32 v191, v191\n"
      "v_cvt_pk_bf16_f32 v186, v188, v189\n"
      "v_cvt_pk_bf16_f32 v187, v190, v191\n"
      "v_mfma_f32_16x16x32_bf16 a[208:211], a[220:223], v[168:171], a[208:211]\n"
      "ds_read_b64_tr_b16 a[188:189], %[vb3] offset:24576\n"
      "ds_read_b64_tr_b16 a[190:191], %[vb3] offset:30720\n"
      "v_mfma_f32_16x16x32_bf16 a[212:215], a[220:223], v[176:179], a[212:215]\n"
      "ds_read_b64_tr_b16 a[192:193], %[vb0] offset:24704\n"
      "ds_read_b64_tr_b16 a[194:195], %[vb0] offset:30848\n"
      "v_mfma_f32_16x16x32_bf16 a[216:219], a[220:223], v[184:187], a[216:219]\n"
      "ds_read_b64_tr_b16 a[196:197], %[vb1] offset:24704\n"
      "ds_read_b64_tr_b16 a[198:199], %[vb1] offset:30848\n"
      "v_mfma_f32_16x16x32_bf16 v[0:3], a[176:179], v[168:171], v[0:3]\n"
      "ds_read_b64_tr_b16 a[200:201], %[vb2] offset:24704\n"
      "ds_read_b64_tr_b16 a[202:203], %[vb2] offset:30848\n"
      "v_mfma_f32_16x16x32_bf16 v[48:51], a[176:179], v[176:179], v[48:51]\n"
      "ds_read_b128 a[20:23], %[qs1] offset:0\n"
      "v_fma_f32 v144, v144, %[csc], v216\n"
      "v_mfma_f32_16x16x32_bf16 v[96:99], a[176:179], v[184:187], v[96:99]\n"
      "ds_read_b64_tr_b16 a[204:205], %[vb3] offset:24704\n"
      "ds_read_b64_tr_b16 a[206:207], %[vb3] offset:30848\n"
      "v_mfma_f32_16x16x32_bf16 v[4:7], a[180:183], v[168:171], v[4:7]\n"
      "ds_read_b64_tr_b16 a[144:145], %[vb0] offset:24832\n"
      "ds_read_b64_tr_b16 a[146:147], %[vb0] offset:30976\n"
      "v_mfma_f32_16x16x32_bf16 v[52:55], a[180:183], v[176:179], v[52:55]\n"
      "ds_read_b64_tr_b16 a[148:149], %[vb1] offset:24832\n"
      "ds_read_b64_tr_b16 a[150:151], %[vb1] offset:30976\n"
      "v_mfma_f32_16x16x32_bf16 v[100:103], a[180:183], v[184:187], v[100:103]\n"
      "s_waitcnt lgkmcnt(15)\n"
      "ds_read_b64_tr_b16 a[152:153], %[vb2] offset:24832\n"
      "ds_read_b64_tr_b16 a[154:155], %[vb2] offset:30976\n"
      "v_mfma_f32_16x16x32_bf16 v[8:11], a[184:187], v[168:171], v[8:11]\n"
      "ds_read_b64_tr_b16 a[156:157], %[vb3] offset:24832\n"
      "ds_read_b64_tr_b16 a[158:159], %[vb3] offset:30976\n"
      "v_mfma_f32_16x16x32_bf16 v[56:59], a[184:187], v[176:179], v[56:59]\n"
      "v_fma_f32 v145, v145, %[csc], v216\n"
      "v_mfma_f32_16x16x32_bf16 v[104:107], a[184:187], v[184:187], v[104:107]\n"
      "s_waitcnt lgkmcnt(13)\n"
      "v_exp_f32 v144, v144\n"
      "v_mfma_f32_16x16x32_bf16 v[12:15], a[188:191], v[168:171], v[12:15]\n"
      "v_fma_f32 v146, v146, %[csc], v216\n"
      "v_mfma_f32_16x16x32_bf16 v[60:63], a[188:191], v[176:179], v[60:63]\n"
      "v_exp_f32 v145, v145\n"
      "v_mfma_f32_16x16x32_bf16 v[108:111], a[188:191], v[184:187], v[108:111]\n"
      "v_fma_f32 v147, v147, %[csc], v216\n"
      "s_waitcnt vmcnt(20) lgkmcnt(0)\n"
      "s_barrier\n"
      "v_mfma_f32_16x16x32_bf16 v[16:19], a[192:195], v[168:171], v[16:19]\n"
      "s_add_u32 s70, s68, 18886656\n"
      "ds_read_b128 a[160:163], %[kb00] offset:24576\n"
      "ds_read_b128 a[164:167], %[kb00] offset:30720\n"
      "v_mfma_f32_16x16x32_bf16 v[64:67], a[192:195], v[176:179], v[64:67]\n"
      "s_add_u32 m0, %[m0b], 12288\n"
      "ds_read_b128 a[168:171], %[kb10] offset:24576\n"
      "ds_read_b128 a[172:175], %[kb10] offset:30720\n"
      "v_mfma_f32_16x16x32_bf16 v[112:115], a[192:195], v[184:187], v[112:115]\n"
      "buffer_load_dwordx4 %[vk0], s[56:59], s70 offen nt lds\n"
      "ds_read_b128 a[176:179], %[kb00] offset:24704\n"
      "v_mfma_f32_16x16x32_bf16 v[20:23], a[196:199], v[168:171], v[20:23]\n"
      "ds_read_b128 a[180:183], %[kb00] offset:30848\n"
      "ds_read_b128 a[184:187], %[kb10] offset:24704\n"
      "v_mfma_f32_16x16x32_bf16 v[68:71], a[196:199], v[176:179], v[68:71]\n"
      "ds_read_b128 a[188:191], %[kb10] offset:30848\n"
      "ds_read_b128 a[192:195], %[kb00] offset:24832\n"
      "v_mfma_f32_16x16x32_bf16 v[116:119], a[196:199], v[184:187], v[116:119]\n"
      "ds_read_b128 a[196:199], %[kb00] offset:30976\n"
      "v_mfma_f32_16x16x32_bf16 v[24:27], a[200:203], v[168:171], v[24:27]\n"
      "v_exp_f32 v146, v146\n"
      "v_mfma_f32_16x16x32_bf16 v[72:75], a[200:203], v[176:179], v[72:75]\n"
      "v_fma_f32 v148, v148, %[csc], v216\n"
      "v_mfma_f32_16x16x32_bf16 v[120:123], a[200:203], v[184:187], v[120:123]\n"
      "ds_read_b128 a[200:203], %[kb10] offset:24832\n"
      "v_mfma_f32_16x16x32_bf16 v[28:31], a[204:207], v[168:171], v[28:31]\n"
      "ds_write_b64 %[ow], a[40:41] offset:0\n"
      "v_mfma_f32_16x16x32_bf16 v[76:79], a[204:207], v[176:179], v[76:79]\n"
      "s_add_u32 m0, %[m0b], 13312\n"
      "v_exp_f32 v147, v147\n"
      "v_mfma_f32_16x16x32_bf16 v[124:127], a[204:207], v[184:187], v[124:127]\n"
      "buffer_load_dwordx4 %[vk1], s[56:59], s70 offen nt lds\n"
      "ds_read_b128 a[204:207], %[kb10] offset:30976\n"
      "v_mfma_f32_16x16x32_bf16 v[32:35], a[144:147], v[168:171], v[32:35]\n"
      "ds_write_b64 %[ow], a[42:43] offset:32\n"
      "v_cvt_pk_bf16_f32 v144, v144, v145\n"
      "v_mfma_f32_16x16x32_bf16 v[80:83], a[144:147], v[176:179], v[80:83]\n"
      "v_fma_f32 v149, v149, %[csc], v216\n"
      "v_mfma_f32_16x16x32_bf16 v[128:131], a[144:147], v[184:187], v[128:131]\n"
      "ds_read_b64_tr_b16 a[144:145], %[vb0] offset:36864\n"
      "ds_read_b64_tr_b16 a[146:147], %[vb0] offset:43008\n"
      "v_mfma_f32_16x16x32_bf16 v[36:39], a[148:151], v[168:171], v[36:39]\n"
      "ds_write_b64 %[ow], a[44:45] offset:64\n"
      "v_mfma_f32_16x16x32_bf16 v[84:87], a[148:151], v[176:179], v[84:87]\n"
      "v_exp_f32 v148, v148\n"
      "v_mfma_f32_16x16x32_bf16 v[132:135], a[148:151], v[184:187], v[132:135]\n"
      "ds_read_b64_tr_b16 a[148:149], %[vb1] offset:36864\n"
      "ds_read_b64_tr_b16 a[150:151], %[vb1] offset:43008\n"
      "v_mfma_f32_16x16x32_bf16 v[40:43], a[152:155], v[168:171], v[40:43]\n"
      "ds_write_b64 %[ow], a[46:47] offset:96\n"
      "v_fma_f32 v150, v150, %[csc], v216\n"
      "v_mfma_f32_16x16x32_bf16 v[88:91], a[152:155], v[176:179], v[88:91]\n"
      "v_exp_f32 v149, v149\n"
      "v_mfma_f32_16x16x32_bf16 v[136:139], a[152:155], v[184:187], v[136:139]\n"
      "s_add_u32 m0, %[m0b], 14336\n"
      "ds_read_b64_tr_b16 a[152:153], %[vb2] offset:36864\n"
      "ds_read_b64_tr_b16 a[154:155], %[vb2] offset:43008\n"
      "v_mfma_f32_16x16x32_bf16 v[44:47], a[156:159], v[168:171], v[44:47]\n"
      "buffer_load_dwordx4 %[vk2], s[56:59], s70 offen nt lds\n"
      "v_cvt_pk_bf16_f32 v145, v146, v147\n"
      "v_mfma_f32_16x16x32_bf16 v[92:95], a[156:159], v[176:179], v[92:95]\n"
      "v_fma_f32 v151, v151, %[csc], v216\n"
      "v_mfma_f32_16x16x32_bf16 v[140:143], a[156:159], v[184:187], v[140:143]\n"
      "s_waitcnt lgkmcnt(15)\n"
      "v_exp_f32 v150, v150\n"
      "v_mfma_f32_16x16x32_bf16 v[168:171], a[160:163], a[72:75], 0\n"
      "ds_read_b128 a[232:235], %[orb] offset:0\n"
      "v_fma_f32 v152, v152, %[csc], v217\n"
      "v_mfma_f32_16x16x32_bf16 v[176:179], a[160:163], a[96:99], 0\n"
      "v_exp_f32 v151, v151\n"
      "v_mfma_f32_16x16x32_bf16 v[184:187], a[160:163], a[120:123], 0\n"
      "v_cvt_pk_bf16_f32 v146, v148, v149\n"
      "v_fma_f32 v153, v153, %[csc], v217\n"
      "v_mfma_f32_16x16x32_bf16 v[172:175], a[164:167], a[72:75], 0\n"
      "v_exp_f32 v152, v152\n"
      "v_mfma_f32_16x16x32_bf16 v[180:183], a[164:167], a[96:99], 0\n"
      "ds_read_b128 a[236:239], %[orb] offset:1152\n"
      "v_fma_f32 v154, v154, %[csc], v217\n"
      "v_mfma_f32_16x16x32_bf16 v[188:191], a[164:167], a[120:123], 0\n"
      "v_exp_f32 v153, v153\n"
      "v_mfma_f32_16x16x32_bf16 v[168:171], a[168:171], a[76:79], v[168:171]\n"
      "s_add_u32 s71, s68, 18874368\n"
      "v_cvt_pk_bf16_f32 v147, v150, v151\n"
      "v_fma_f32 v155, v155, %[csc], v217\n"
      "v_mfma_f32_16x16x32_bf16 v[176:179], a[168:171], a[100:103], v[176:179]\n"
      "s_add_u32 m0, %[m0b], 98304\n"
      "v_exp_f32 v154, v154\n"
      "v_mfma_f32_16x16x32_bf16 v[184:187], a[168:171], a[124:127], v[184:187]\n"
      "buffer_load_dwordx4 %[vv0], s[60:63], s71 offen nt lds\n"
      "v_fma_f32 v156, v156, %[csc], v217\n"
      "v_mfma_f32_16x16x32_bf16 v[172:175], a[172:175], a[76:79], v[172:175]\n"
      "v_exp_f32 v155, v155\n"
      "v_mfma_f32_16x16x32_bf16 v[180:183], a[172:175], a[100:103], v[180:183]\n"
      "v_cvt_pk_bf16_f32 v152, v152, v153\n"
      "v_fma_f32 v157, v157, %[csc], v217\n"
      "v_mfma_f32_16x16x32_bf16 v[188:191], a[172:175], a[124:127], v[188:191]\n"
      "s_waitcnt lgkmcnt(1)\n"
      "v_exp_f32 v156, v156\n"
      "v_mfma_f32_16x16x32_bf16 v[168:171], a[176:179], a[80:83], v[168:171]\n"
      "s_add_u32 s73, s69, 4276099072\n"
      "buffer_store_dwordx4 a[232:235], %[ost], s[84:87], s73 offen offset:256 sc1\n"
      "v_fma_f32 v158, v158, %[csc], v217\n"
      "v_mfma_f32_16x16x32_bf16 v[176:179], a[176:179], a[104:107], v[176:179]\n"
      "v_exp_f32 v157, v157\n"
      "v_mfma_f32_16x16x32_bf16 v[184:187], a[176:179], a[128:131], v[184:187]\n"
      "v_cvt_pk_bf16_f32 v153, v154, v155\n"
      "v_fma_f32 v159, v159, %[csc], v217\n"
      "v_mfma_f32_16x16x32_bf16 v[172:175], a[180:183], a[80:83], v[172:175]\n"
      "v_exp_f32 v158, v158\n"
      "v_mfma_f32_16x16x32_bf16 v[180:183], a[180:183], a[104:107], v[180:183]\n"
      "s_add_u32 m0, %[m0b], 99328\n"
      "v_fma_f32 v160, v160, %[csc], v218\n"
      "v_mfma_f32_16x16x32_bf16 v[188:191], a[180:183], a[128:131], v[188:191]\n"
      "buffer_load_dwordx4 %[vv1], s[60:63], s71 offen nt lds\n"
      "v_mfma_f32_16x16x32_bf16 v[168:171], a[184:187], a[84:87], v[168:171]\n"
      "v_exp_f32 v159, v159\n"
      "v_mfma_f32_16x16x32_bf16 v[176:179], a[184:187], a[108:111], v[176:179]\n"
      "v_cvt_pk_bf16_f32 v154, v156, v157\n"
      "v_fma_f32 v161, v161, %[csc], v218\n"
      "v_mfma_f32_16x16x32_bf16 v[184:187], a[184:187], a[132:135], v[184:187]\n"
      "v_exp_f32 v160, v160\n"
      "v_mfma_f32_16x16x32_bf16 v[172:175], a[188:191], a[84:87], v[172:175]\n"
      "v_fma_f32 v162, v162, %[csc], v218\n"
      "v_mfma_f32_16x16x32_bf16 v[180:183], a[188:191], a[108:111], v[180:183]\n"
      "v_exp_f32 v161, v161\n"
      "v_mfma_f32_16x16x32_bf16 v[188:191], a[188:191], a[132:135], v[188:191]\n"
      "s_waitcnt lgkmcnt(0)\n"
      "v_cvt_pk_bf16_f32 v155, v158, v159\n"
      "v_fma_f32 v163, v163, %[csc], v218\n"
      "v_mfma_f32_16x16x32_bf16 v[168:171], a[192:195], a[88:91], v[168:171]\n"
      "s_add_u32 s73, s69, 4276102144\n"
      "v_exp_f32 v162, v162\n"
      "v_mfma_f32_16x16x32_bf16 v[176:179], a[192:195], a[112:115], v[176:179]\n"
      "buffer_store_dwordx4 a[236:239], %[ost], s[84:87], s73 offen offset:256 sc1\n"
      "v_fma_f32 v164, v164, %[csc], v218\n"
      "v_mfma_f32_16x16x32_bf16 v[184:187], a[192:195], a[136:139], v[184:187]\n"
      "s_add_u32 m0, %[m0b], 100352\n"
      "v_exp_f32 v163, v163\n"
      "v_mfma_f32_16x16x32_bf16 v[172:175], a[196:199], a[88:91], v[172:175]\n"
      "v_cvt_pk_bf16_f32 v160, v160, v161\n"
      "v_fma_f32 v165, v165, %[csc], v218\n"
      "v_mfma_f32_16x16x32_bf16 v[180:183], a[196:199], a[112:115], v[180:183]\n"
      "v_exp_f32 v164, v164\n"
      "v_mfma_f32_16x16x32_bf16 v[188:191], a[196:199], a[136:139], v[188:191]\n"
      "buffer_load_dwordx4 %[vv2], s[60:63], s71 offen nt lds\n"
      "v_fma_f32 v166, v166, %[csc], v218\n"
      "v_mfma_f32_16x16x32_bf16 v[168:171], a[200:203], a[92:95], v[168:171]\n"
      "v_exp_f32 v165, v165\n"
      "v_mfma_f32_16x16x32_bf16 v[176:179], a[200:203], a[116:119], v[176:179]\n"
      "v_cvt_pk_bf16_f32 v161, v162, v163\n"
      "v_fma_f32 v167, v167, %[csc], v218\n"
      "v_mfma_f32_16x16x32_bf16 v[184:187], a[200:203], a[140:143], v[184:187]\n"
      "v_exp_f32 v166, v166\n"
      "v_mfma_f32_16x16x32_bf16 v[172:175], a[204:207], a[92:95], v[172:175]\n"
      "v_exp_f32 v167, v167\n"
      "v_mfma_f32_16x16x32_bf16 v[180:183], a[204:207], a[116:119], v[180:183]\n"
      "v_cvt_pk_bf16_f32 v162, v164, v165\n"
      "v_mfma_f32_16x16x32_bf16 v[188:191], a[204:207], a[140:143], v[188:191]\n"
      "s_waitcnt vmcnt(26)\n"
      "ds_read_b128 a[40:43], %[qs0] offset:2048\n"
      "v_cvt_pk_bf16_f32 v163, v166, v167\n"
      "v_mfma_f32_16x16x32_bf16 a[208:211], a[220:223], v[144:147], a[208:211]\n"
      "ds_read_b64_tr_b16 a[156:157], %[vb3] offset:36864\n"
      "ds_read_b64_tr_b16 a[158:159], %[vb3] offset:43008\n"
      "v_mfma_f32_16x16x32_bf16 a[212:215], a[220:223], v[152:155], a[212:215]\n"
      "ds_read_b64_tr_b16 a[160:161], %[vb0] offset:36992\n"
      "ds_read_b64_tr_b16 a[162:163], %[vb0] offset:43136\n"
      "v_mfma_f32_16x16x32_bf16 a[216:219], a[220:223], v[160:163], a[216:219]\n"
      "ds_read_b64_tr_b16 a[164:165], %[vb1] offset:36992\n"
      "ds_read_b64_tr_b16 a[166:167], %[vb1] offset:43136\n"
      "v_mfma_f32_16x16x32_bf16 v[0:3], a[144:147], v[144:147], v[0:3]\n"
      "ds_read_b64_tr_b16 a[168:169], %[vb2] offset:36992\n"
      "ds_read_b64_tr_b16 a[170:171], %[vb2] offset:43136\n"
      "v_mfma_f32_16x16x32_bf16 v[48:51], a[144:147], v[152:155], v[48:51]\n"
      "ds_read_b64_tr_b16 a[172:173], %[vb3] offset:36992\n"
      "ds_read_b64_tr_b16 a[174:175], %[vb3] offset:43136\n"
      "v_mfma_f32_16x16x32_bf16 v[96:99], a[144:147], v[160:163], v[96:99]\n"
      "ds_read_b64_tr_b16 a[176:177], %[vb0] offset:37120\n"
      "ds_read_b64_tr_b16 a[178:179], %[vb0] offset:43264\n"
      "v_mfma_f32_16x16x32_bf16 v[4:7], a[148:151], v[144:147], v[4:7]\n"
      "ds_read_b64_tr_b16 a[180:181], %[vb1] offset:37120\n"
      "ds_read_b64_tr_b16 a[182:183], %[vb1] offset:43264\n"
      "v_mfma_f32_16x16x32_bf16 v[52:55], a[148:151], v[152:155], v[52:55]\n"
      "ds_read_b128 a[44:47], %[qs1] offset:2048\n"
      "v_fma_f32 v168, v168, %[csc], v216\n"
      "v_mfma_f32_16x16x32_bf16 v[100:103], a[148:151], v[160:163], v[100:103]\n"
      "ds_read_b64_tr_b16 a[184:185], %[vb2] offset:37120\n"
      "ds_read_b64_tr_b16 a[186:187], %[vb2] offset:43264\n"
      "v_mfma_f32_16x16x32_bf16 v[8:11], a[152:155], v[144:147], v[8:11]\n"
      "ds_read_b64_tr_b16 a[188:189], %[vb3] offset:37120\n"
      "ds_read_b64_tr_b16 a[190:191], %[vb3] offset:43264\n"
      "v_mfma_f32_16x16x32_bf16 v[56:59], a[152:155], v[152:155], v[56:59]\n"
      "v_fma_f32 v169, v169, %[csc], v216\n"
      "v_mfma_f32_16x16x32_bf16 v[104:107], a[152:155], v[160:163], v[104:107]\n"
      "s_waitcnt lgkmcnt(13)\n"
      "v_exp_f32 v168, v168\n"
      "v_mfma_f32_16x16x32_bf16 v[12:15], a[156:159], v[144:147], v[12:15]\n"
      "v_fma_f32 v170, v170, %[csc], v216\n"
      "v_mfma_f32_16x16x32_bf16 v[60:63], a[156:159], v[152:155], v[60:63]\n"
      "v_exp_f32 v169, v169\n"
      "v_mfma_f32_16x16x32_bf16 v[108:111], a[156:159], v[160:163], v[108:111]\n"
      "v_fma_f32 v171, v171, %[csc], v216\n"
      "s_waitcnt vmcnt(18) lgkmcnt(0)\n"
      "s_barrier\n"
      "v_mfma_f32_16x16x32_bf16 v[16:19], a[160:163], v[144:147], v[16:19]\n"
      "s_add_u32 s70, s68, 18898944\n"
      "ds_read_b128 a[192:195], %[kb01] offset:0\n"
      "ds_read_b128 a[196:199], %[kb01] offset:6144\n"
      "v_mfma_f32_16x16x32_bf16 v[64:67], a[160:163], v[152:155], v[64:67]\n"
      "s_add_u32 m0, %[m0b], 24576\n"
      "ds_read_b128 a[200:203], %[kb11] offset:0\n"
      "ds_read_b128 a[204:207], %[kb11] offset:6144\n"
      "v_mfma_f32_16x16x32_bf16 v[112:115], a[160:163], v[160:163], v[112:115]\n"
      "buffer_load_dwordx4 %[vk0], s[56:59], s70 offen nt lds\n"
      "ds_read_b128 a[144:147], %[kb01] offset:128\n"
      "v_mfma_f32_16x16x32_bf16 v[20:23], a[164:167], v[144:147], v[20:23]\n"
      "ds_read_b128 a[148:151], %[kb01] offset:6272\n"
      "ds_read_b128 a[152:155], %[kb11] offset:128\n"
      "v_mfma_f32_16x16x32_bf16 v[68:71], a[164:167], v[152:155], v[68:71]\n"
      "ds_read_b128 a[156:159], %[kb11] offset:6272\n"
      "ds_read_b128 a[160:163], %[kb01] offset:256\n"
      "v_mfma_f32_16x16x32_bf16 v[116:119], a[164:167], v[160:163], v[116:119]\n"
      "ds_read_b128 a[164:167], %[kb01] offset:6400\n"
      "v_mfma_f32_16x16x32_bf16 v[24:27], a[168:171], v[144:147], v[24:27]\n"
      "v_exp_f32 v170, v170\n"
      "v_mfma_f32_16x16x32_bf16 v[72:75], a[168:171], v[152:155], v[72:75]\n"
      "v_fma_f32 v172, v172, %[csc], v216\n"
      "v_mfma_f32_16x16x32_bf16 v[120:123], a[168:171], v[160:163], v[120:123]\n"
      "ds_read_b128 a[168:171], %[kb11] offset:256\n"
      "v_mfma_f32_16x16x32_bf16 v[28:31], a[172:175], v[144:147], v[28:31]\n"
      "v_exp_f32 v171, v171\n"
      "v_mfma_f32_16x16x32_bf16 v[76:79], a[172:175], v[152:155], v[76:79]\n"
      "s_add_u32 m0, %[m0b], 25600\n"
      "v_cvt_pk_bf16_f32 v168, v168, v169\n"
      "v_fma_f32 v173, v173, %[csc], v216\n"
      "v_mfma_f32_16x16x32_bf16 v[124:127], a[172:175], v[160:163], v[124:127]\n"
      "buffer_load_dwordx4 %[vk1], s[56:59], s70 offen nt lds\n"
      "ds_read_b128 a[172:175], %[kb11] offset:6400\n"
      "v_mfma_f32_16x16x32_bf16 v[32:35], a[176:179], v[144:147], v[32:35]\n"
      "v_exp_f32 v172, v172\n"
      "v_mfma_f32_16x16x32_bf16 v[80:83], a[176:179], v[152:155], v[80:83]\n"
      "v_fma_f32 v174, v174, %[csc], v216\n"
      "v_mfma_f32_16x16x32_bf16 v[128:131], a[176:179], v[160:163], v[128:131]\n"
      "ds_read_b64_tr_b16 a[176:177], %[vb0] offset:0\n"
      "ds_read_b64_tr_b16 a[178:179], %[vb0] offset:6144\n"
      "v_mfma_f32_16x16x32_bf16 v[36:39], a[180:183], v[144:147], v[36:39]\n"
      "v_exp_f32 v173, v173\n"
      "v_mfma_f32_16x16x32_bf16 v[84:87], a[180:183], v[152:155], v[84:87]\n"
      "v_cvt_pk_bf16_f32 v169, v170, v171\n"
      "v_fma_f32 v175, v175, %[csc], v216\n"
      "v_mfma_f32_16x16x32_bf16 v[132:135], a[180:183], v[160:163], v[132:135]\n"
      "ds_read_b64_tr_b16 a[180:181], %[vb1] offset:0\n"
      "ds_read_b64_tr_b16 a[182:183], %[vb1] offset:6144\n"
      "v_mfma_f32_16x16x32_bf16 v[40:43], a[184:187], v[144:147], v[40:43]\n"
      "v_exp_f32 v174, v174\n"
      "v_mfma_f32_16x16x32_bf16 v[88:91], a[184:187], v[152:155], v[88:91]\n"
      "ds_write_b64 %[ow], a[64:65] offset:0\n"
      "v_fma_f32 v176, v176, %[csc], v217\n"
      "v_mfma_f32_16x16x32_bf16 v[136:139], a[184:187], v[160:163], v[136:139]\n"
      "s_add_u32 m0, %[m0b], 26624\n"
      "ds_read_b64_tr_b16 a[184:185], %[vb2] offset:0\n"
      "ds_read_b64_tr_b16 a[186:187], %[vb2] offset:6144\n"
      "v_mfma_f32_16x16x32_bf16 v[44:47], a[188:191], v[144:147], v[44:47]\n"
      "buffer_load_dwordx4 %[vk2], s[56:59], s70 offen nt lds\n"
      "v_mfma_f32_16x16x32_bf16 v[92:95], a[188:191], v[152:155], v[92:95]\n"
      "v_exp_f32 v175, v175\n"
      "v_mfma_f32_16x16x32_bf16 v[140:143], a[188:191], v[160:163], v[140:143]\n"
      "s_waitcnt lgkmcnt(15)\n"
      "v_cvt_pk_bf16_f32 v170, v172, v173\n"
      "v_fma_f32 v177, v177, %[csc], v217\n"
      "v_mfma_f32_16x16x32_bf16 v[192:195], a[192:195], a[72:75], 0\n"
      "v_exp_f32 v176, v176\n"
      "v_mfma_f32_16x16x32_bf16 v[200:203], a[192:195], a[96:99], 0\n"
      "ds_write_b64 %[ow], a[66:67] offset:32\n"
      "v_fma_f32 v178, v178, %[csc], v217\n"
      "v_mfma_f32_16x16x32_bf16 v[208:211], a[192:195], a[120:123], 0\n"
      "v_exp_f32 v177, v177\n"
      "v_mfma_f32_16x16x32_bf16 v[196:199], a[196:199], a[72:75], 0\n"
      "v_cvt_pk_bf16_f32 v171, v174, v175\n"
      "v_fma_f32 v179, v179, %[csc], v217\n"
      "v_mfma_f32_16x16x32_bf16 v[204:207], a[196:199], a[96:99], 0\n"
      "v_exp_f32 v178, v178\n"
      "v_mfma_f32_16x16x32_bf16 v[212:215], a[196:199], a[120:123], 0\n"
      "ds_write_b64 %[ow], a[68:69] offset:64\n"
      "v_fma_f32 v180, v180, %[csc], v217\n"
      "v_mfma_f32_16x16x32_bf16 v[192:195], a[200:203], a[76:79], v[192:195]\n"
      "s_add_u32 s71, s68, 18886656\n"
      "v_exp_f32 v179, v179\n"
      "v_mfma_f32_16x16x32_bf16 v[200:203], a[200:203], a[100:103], v[200:203]\n"
      "s_add_u32 m0, %[m0b], 110592\n"
      "v_cvt_pk_bf16_f32 v176, v176, v177\n"
      "v_fma_f32 v181, v181, %[csc], v217\n"
      "v_mfma_f32_16x16x32_bf16 v[208:211], a[200:203], a[124:127], v[208:211]\n"
      "buffer_load_dwordx4 %[vv0], s[60:63], s71 offen nt lds\n"
      "ds_write_b64 %[ow], a[70:71] offset:96\n"
      "v_mfma_f32_16x16x32_bf16 v[196:199], a[204:207], a[76:79], v[196:199]\n"
      "v_exp_f32 v180, v180\n"
      "v_mfma_f32_16x16x32_bf16 v[204:207], a[204:207], a[100:103], v[204:207]\n"
      "ds_read_b128 a[224:227], %[orb] offset:0\n"
      "v_fma_f32 v182, v182, %[csc], v217\n"
      "v_mfma_f32_16x16x32_bf16 v[212:215], a[204:207], a[124:127], v[212:215]\n"
      "s_waitcnt lgkmcnt(15)\n"
      "v_exp_f32 v181, v181\n"
      "v_mfma_f32_16x16x32_bf16 v[192:195], a[144:147], a[80:83], v[192:195]\n"
      "v_cvt_pk_bf16_f32 v177, v178, v179\n"
      "v_fma_f32 v183, v183, %[csc], v217\n"
      "v_mfma_f32_16x16x32_bf16 v[200:203], a[144:147], a[104:107], v[200:203]\n"
      "v_exp_f32 v182, v182\n"
      "v_mfma_f32_16x16x32_bf16 v[208:211], a[144:147], a[128:131], v[208:211]\n"
      "ds_read_b128 a[228:231], %[orb] offset:1152\n"
      "v_fma_f32 v184, v184, %[csc], v218\n"
      "v_mfma_f32_16x16x32_bf16 v[196:199], a[148:151], a[80:83], v[196:199]\n"
      "v_exp_f32 v183, v183\n"
      "v_mfma_f32_16x16x32_bf16 v[204:207], a[148:151], a[104:107], v[204:207]\n"
      "s_add_u32 m0, %[m0b], 111616\n"
      "v_cvt_pk_bf16_f32 v178, v180, v181\n"
      "v_fma_f32 v185, v185, %[csc], v218\n"
      "v_mfma_f32_16x16x32_bf16 v[212:215], a[148:151], a[128:131], v[212:215]\n"
      "buffer_load_dwordx4 %[vv1], s[60:63], s71 offen nt lds\n"
      "v_mfma_f32_16x16x32_bf16 v[192:195], a[152:155], a[84:87], v[192:195]\n"
      "v_exp_f32 v184, v184\n"
      "v_mfma_f32_16x16x32_bf16 v[200:203], a[152:155], a[108:111], v[200:203]\n"
      "v_fma_f32 v186, v186, %[csc], v218\n"
      "v_mfma_f32_16x16x32_bf16 v[208:211], a[152:155], a[132:135], v[208:211]\n"
      "v_exp_f32 v185, v185\n"
      "v_mfma_f32_16x16x32_bf16 v[196:199], a[156:159], a[84:87], v[196:199]\n"
      "s_waitcnt lgkmcnt(1)\n"
      "v_cvt_pk_bf16_f32 v179, v182, v183\n"
      "v_fma_f32 v187, v187, %[csc], v218\n"
      "v_mfma_f32_16x16x32_bf16 v[204:207], a[156:159], a[108:111], v[204:207]\n"
      "s_add_u32 s73, s69, 4276105216\n"
      "v_exp_f32 v186, v186\n"
      "v_mfma_f32_16x16x32_bf16 v[212:215], a[156:159], a[132:135], v[212:215]\n"
      "buffer_store_dwordx4 a[224:227], %[ost], s[84:87], s73 offen offset:256 sc1\n"
      "v_fma_f32 v188, v188, %[csc], v218\n"
      "v_mfma_f32_16x16x32_bf16 v[192:195], a[160:163], a[88:91], v[192:195]\n"
      "v_exp_f32 v187, v187\n"
      "v_mfma_f32_16x16x32_bf16 v[200:203], a[160:163], a[112:115], v[200:203]\n"
      "v_cvt_pk_bf16_f32 v184, v184, v185\n"
      "v_fma_f32 v189, v189, %[csc], v218\n"
      "v_mfma_f32_16x16x32_bf16 v[208:211], a[160:163], a[136:139], v[208:211]\n"
      "s_add_u32 m0, %[m0b], 112640\n"
      "v_exp_f32 v188, v188\n"
      "v_mfma_f32_16x16x32_bf16 v[196:199], a[164:167], a[88:91], v[196:199]\n"
      "buffer_load_dwordx4 %[vv2], s[60:63], s71 offen nt lds\n"
      "v_fma_f32 v190, v190, %[csc], v218\n"
      "v_mfma_f32_16x16x32_bf16 v[204:207], a[164:167], a[112:115], v[204:207]\n"
      "v_exp_f32 v189, v189\n"
      "v_mfma_f32_16x16x32_bf16 v[212:215], a[164:167], a[136:139], v[212:215]\n"
      "v_cvt_pk_bf16_f32 v185, v186, v187\n"
      "v_fma_f32 v191, v191, %[csc], v218\n"
      "v_mfma_f32_16x16x32_bf16 v[192:195], a[168:171], a[92:95], v[192:195]\n"
      "v_exp_f32 v190, v190\n"
      "v_mfma_f32_16x16x32_bf16 v[200:203], a[168:171], a[116:119], v[200:203]\n"
      "v_exp_f32 v191, v191\n"
      "v_mfma_f32_16x16x32_bf16 v[208:211], a[168:171], a[140:143], v[208:211]\n"
      "v_cvt_pk_bf16_f32 v186, v188, v189\n"
      "v_mfma_f32_16x16x32_bf16 v[196:199], a[172:175], a[92:95], v[196:199]\n"
      "s_waitcnt lgkmcnt(0)\n"
      "v_cvt_pk_bf16_f32 v187, v190, v191\n"
      "v_mfma_f32_16x16x32_bf16 v[204:207], a[172:175], a[116:119], v[204:207]\n"
      "s_add_u32 s73, s69, 4276108288\n"
      "ds_read_b64_tr_b16 a[188:189], %[vb3] offset:0\n"
      "ds_read_b64_tr_b16 a[190:191], %[vb3] offset:6144\n"
      "v_mfma_f32_16x16x32_bf16 v[212:215], a[172:175], a[140:143], v[212:215]\n"
      "ds_read_b64_tr_b16 a[192:193], %[vb0] offset:128\n"
      "ds_read_b64_tr_b16 a[194:195], %[vb0] offset:6272\n"
      "v_mfma_f32_16x16x32_bf16 a[208:211], a[220:223], v[168:171], a[208:211]\n"
      "s_add_u32 s72, s69, 37748736\n"
      "ds_read_b64_tr_b16 a[196:197], %[vb1] offset:128\n"
      "ds_read_b64_tr_b16 a[198:199], %[vb1] offset:6272\n"
      "v_mfma_f32_16x16x32_bf16 a[212:215], a[220:223], v[176:179], a[212:215]\n"
      "s_add_u32 m0, %[m0q], 0\n"
      "ds_read_b64_tr_b16 a[200:201], %[vb2] offset:128\n"
      "ds_read_b64_tr_b16 a[202:203], %[vb2] offset:6272\n"
      "v_mfma_f32_16x16x32_bf16 a[216:219], a[220:223], v[184:187], a[216:219]\n"
      "ds_read_b64_tr_b16 a[204:205], %[vb3] offset:128\n"
      "ds_read_b64_tr_b16 a[206:207], %[vb3] offset:6272\n"
      "v_mfma_f32_16x16x32_bf16 v[0:3], a[176:179], v[168:171], v[0:3]\n"
      "ds_read_b64_tr_b16 a[144:145], %[vb0] offset:256\n"
      "ds_read_b64_tr_b16 a[146:147], %[vb0] offset:6400\n"
      "v_mfma_f32_16x16x32_bf16 v[48:51], a[176:179], v[176:179], v[48:51]\n"
      "buffer_store_dwordx4 a[228:231], %[ost], s[84:87], s73 offen offset:256 sc1\n"
      "buffer_load_dwordx4 %[vq0], s[80:83], s72 offen nt lds\n"
      "v_mfma_f32_16x16x32_bf16 v[96:99], a[176:179], v[184:187], v[96:99]\n"
      "s_add_u32 m0, %[m0q], 1024\n"
      "ds_read_b64_tr_b16 a[148:149], %[vb1] offset:256\n"
      "ds_read_b64_tr_b16 a[150:151], %[vb1] offset:6400\n"
      "v_mfma_f32_16x16x32_bf16 v[4:7], a[180:183], v[168:171], v[4:7]\n"
      "ds_read_b64_tr_b16 a[152:153], %[vb2] offset:256\n"
      "ds_read_b64_tr_b16 a[154:155], %[vb2] offset:6400\n"
      "v_mfma_f32_16x16x32_bf16 v[52:55], a[180:183], v[176:179], v[52:55]\n"
      "ds_read_b64_tr_b16 a[156:157], %[vb3] offset:256\n"
      "ds_read_b64_tr_b16 a[158:159], %[vb3] offset:6400\n"
      "v_mfma_f32_16x16x32_bf16 v[100:103], a[180:183], v[184:187], v[100:103]\n"
      "buffer_load_dwordx4 %[vq1], s[80:83], s72 offen nt lds\n"
      "v_fma_f32 v192, v192, %[csc], v216\n"
      "v_mfma_f32_16x16x32_bf16 v[8:11], a[184:187], v[168:171], v[8:11]\n"
      "s_waitcnt vmcnt(26)\n"
      "ds_read_b128 a[64:67], %[qs0] offset:4096\n"
      "ds_read_b128 a[68:71], %[qs1] offset:4096\n"
      "v_mfma_f32_16x16x32_bf16 v[56:59], a[184:187], v[176:179], v[56:59]\n"
      "v_fma_f32 v193, v193, %[csc], v216\n"
      "v_mfma_f32_16x16x32_bf16 v[104:107], a[184:187], v[184:187], v[104:107]\n"
      "s_waitcnt lgkmcnt(12)\n"
      "v_exp_f32 v192, v192\n"
      "v_mfma_f32_16x16x32_bf16 v[12:15], a[188:191], v[168:171], v[12:15]\n"
      "v_fma_f32 v194, v194, %[csc], v216\n"
      "s_waitcnt vmcnt(18) lgkmcnt(0)\n"
      "s_barrier\n"
      "v_mfma_f32_16x16x32_bf16 v[60:63], a[188:191], v[176:179], v[60:63]\n"
      "s_add_u32 s70, s68, 18911232\n"
      "ds_read_b128 a[160:163], %[kb00] offset:0\n"
      "ds_read_b128 a[164:167], %[kb00] offset:6144\n"
      "v_mfma_f32_16x16x32_bf16 v[108:111], a[188:191], v[184:187], v[108:111]\n"
      "s_add_u32 m0, %[m0b], 36864\n"
      "ds_read_b128 a[168:171], %[kb10] offset:0\n"
      "ds_read_b128 a[172:175], %[kb10] offset:6144\n"
      "v_mfma_f32_16x16x32_bf16 v[16:19], a[192:195], v[168:171], v[16:19]\n"
      "buffer_load_dwordx4 %[vk0], s[56:59], s70 offen nt lds\n"
      "ds_read_b128 a[176:179], %[kb00] offset:128\n"
      "v_mfma_f32_16x16x32_bf16 v[64:67], a[192:195], v[176:179], v[64:67]\n"
      "ds_read_b128 a[180:183], %[kb00] offset:6272\n"
      "ds_read_b128 a[184:187], %[kb10] offset:128\n"
      "v_mfma_f32_16x16x32_bf16 v[112:115], a[192:195], v[184:187], v[112:115]\n"
      "ds_read_b128 a[188:191], %[kb10] offset:6272\n"
      "ds_read_b128 a[192:195], %[kb00] offset:256\n"
      "v_mfma_f32_16x16x32_bf16 v[20:23], a[196:199], v[168:171], v[20:23]\n"
      "v_exp_f32 v193, v193\n"
      "v_mfma_f32_16x16x32_bf16 v[68:71], a[196:199], v[176:179], v[68:71]\n"
      "v_fma_f32 v195, v195, %[csc], v216\n"
      "v_mfma_f32_16x16x32_bf16 v[116:119], a[196:199], v[184:187], v[116:119]\n"
      "ds_read_b128 a[196:199], %[kb00] offset:6400\n"
      "v_mfma_f32_16x16x32_bf16 v[24:27], a[200:203], v[168:171], v[24:27]\n"
      "v_exp_f32 v194, v194\n"
      "v_mfma_f32_16x16x32_bf16 v[72:75], a[200:203], v[176:179], v[72:75]\n"
      "v_fma_f32 v196, v196, %[csc], v216\n"
      "v_mfma_f32_16x16x32_bf16 v[120:123], a[200:203], v[184:187], v[120:123]\n"
      "s_add_u32 m0, %[m0b], 37888\n"
      "ds_read_b128 a[200:203], %[kb10] offset:256\n"
      "v_mfma_f32_16x16x32_bf16 v[28:31], a[204:207], v[168:171], v[28:31]\n"
      "buffer_load_dwordx4 %[vk1], s[56:59], s70 offen nt lds\n"
      "v_mfma_f32_16x16x32_bf16 v[76:79], a[204:207], v[176:179], v[76:79]\n"
      "v_exp_f32 v195, v195\n"
      "v_mfma_f32_16x16x32_bf16 v[124:127], a[204:207], v[184:187], v[124:127]\n"
      "ds_read_b128 a[204:207], %[kb10] offset:6400\n"
      "v_cvt_pk_bf16_f32 v192, v192, v193\n"
      "v_mfma_f32_16x16x32_bf16 v[32:35], a[144:147], v[168:171], v[32:35]\n"
      "v_fma_f32 v197, v197, %[csc], v216\n"
      "v_mfma_f32_16x16x32_bf16 v[80:83], a[144:147], v[176:179], v[80:83]\n"
      "v_exp_f32 v196, v196\n"
      "v_mfma_f32_16x16x32_bf16 v[128:131], a[144:147], v[184:187], v[128:131]\n"
      "ds_read_b64_tr_b16 a[144:145], %[vb0] offset:12288\n"
      "ds_read_b64_tr_b16 a[146:147], %[vb0] offset:18432\n"
      "v_mfma_f32_16x16x32_bf16 v[36:39], a[148:151], v[168:171], v[36:39]\n"
      "v_fma_f32 v198, v198, %[csc], v216\n"
      "v_mfma_f32_16x16x32_bf16 v[84:87], a[148:151], v[176:179], v[84:87]\n"
      "v_exp_f32 v197, v197\n"
      "v_mfma_f32_16x16x32_bf16 v[132:135], a[148:151], v[184:187], v[132:135]\n"
      "ds_read_b64_tr_b16 a[148:149], %[vb1] offset:12288\n"
      "ds_read_b64_tr_b16 a[150:151], %[vb1] offset:18432\n"
      "v_mfma_f32_16x16x32_bf16 v[40:43], a[152:155], v[168:171], v[40:43]\n"
      "s_add_u32 m0, %[m0b], 38912\n"
      "v_cvt_pk_bf16_f32 v193, v194, v195\n"
      "v_fma_f32 v199, v199, %[csc], v216\n"
      "v_mfma_f32_16x16x32_bf16 v[88:91], a[152:155], v[176:179], v[88:91]\n"
      "buffer_load_dwordx4 %[vk2], s[56:59], s70 offen nt lds\n"
      "v_mfma_f32_16x16x32_bf16 v[136:139], a[152:155], v[184:187], v[136:139]\n"
      "ds_read_b64_tr_b16 a[152:153], %[vb2] offset:12288\n"
      "ds_read_b64_tr_b16 a[154:155], %[vb2] offset:18432\n"
      "v_mfma_f32_16x16x32_bf16 v[44:47], a[156:159], v[168:171], v[44:47]\n"
      "v_exp_f32 v198, v198\n"
      "v_mfma_f32_16x16x32_bf16 v[92:95], a[156:159], v[176:179], v[92:95]\n"
      "v_fma_f32 v200, v200, %[csc], v217\n"
      "v_mfma_f32_16x16x32_bf16 v[140:143], a[156:159], v[184:187], v[140:143]\n"
      "s_waitcnt lgkmcnt(14)\n"
      "v_exp_f32 v199, v199\n"
      "s_cmp_lg_u32 s74, 0\n"
      "s_cbranch_scc1 L_tail%=\n"
      "v_mfma_f32_16x16x32_bf16 v[144:147], a[160:163], a[0:3], 0\n"
      "v_cvt_pk_bf16_f32 v194, v196, v197\n"
      "v_fma_f32 v201, v201, %[csc], v217\n"
      "v_mfma_f32_16x16x32_bf16 v[152:155], a[160:163], a[24:27], 0\n"
      "v_exp_f32 v200, v200\n"
      "v_mfma_f32_16x16x32_bf16 v[160:163], a[160:163], a[48:51], 0\n"
      "v_fma_f32 v202, v202, %[csc], v217\n"
      "v_mfma_f32_16x16x32_bf16 v[148:151], a[164:167], a[0:3], 0\n"
      "v_exp_f32 v201, v201\n"
      "v_mfma_f32_16x16x32_bf16 v[156:159], a[164:167], a[24:27], 0\n"
      "s_add_u32 s71, s68, 18898944\n"
      "v_cvt_pk_bf16_f32 v195, v198, v199\n"
      "v_fma_f32 v203, v203, %[csc], v217\n"
      "v_mfma_f32_16x16x32_bf16 v[164:167], a[164:167], a[48:51], 0\n"
      "s_add_u32 m0, %[m0b], 73728\n"
      "v_exp_f32 v202, v202\n"
      "v_mfma_f32_16x16x32_bf16 v[144:147], a[168:171], a[4:7], v[144:147]\n"
      "buffer_load_dwordx4 %[vv0], s[60:63], s71 offen nt lds\n"
      "v_fma_f32 v204, v204, %[csc], v217\n"
      "v_mfma_f32_16x16x32_bf16 v[152:155], a[168:171], a[28:31], v[152:155]\n"
      "v_exp_f32 v203, v203\n"
      "v_mfma_f32_16x16x32_bf16 v[160:163], a[168:171], a[52:55], v[160:163]\n"
      "v_cvt_pk_bf16_f32 v200, v200, v201\n"
      "v_fma_f32 v205, v205, %[csc], v217\n"
      "v_mfma_f32_16x16x32_bf16 v[148:151], a[172:175], a[4:7], v[148:151]\n"
      "v_exp_f32 v204, v204\n"
      "v_mfma_f32_16x16x32_bf16 v[156:159], a[172:175], a[28:31], v[156:159]\n"
      "v_fma_f32 v206, v206, %[csc], v217\n"
      "v_mfma_f32_16x16x32_bf16 v[164:167], a[172:175], a[52:55], v[164:167]\n"
      "s_waitcnt lgkmcnt(10)\n"
      "v_exp_f32 v205, v205\n"
      "v_mfma_f32_16x16x32_bf16 v[144:147], a[176:179], a[8:11], v[144:147]\n"
      "v_cvt_pk_bf16_f32 v201, v202, v203\n"
      "v_fma_f32 v207, v207, %[csc], v217\n"
      "v_mfma_f32_16x16x32_bf16 v[152:155], a[176:179], a[32:35], v[152:155]\n"
      "v_exp_f32 v206, v206\n"
      "v_mfma_f32_16x16x32_bf16 v[160:163], a[176:179], a[56:59], v[160:163]\n"
      "s_add_u32 m0, %[m0b], 74752\n"
      "v_fma_f32 v208, v208, %[csc], v218\n"
      "v_mfma_f32_16x16x32_bf16 v[148:151], a[180:183], a[8:11], v[148:151]\n"
      "buffer_load_dwordx4 %[vv1], s[60:63], s71 offen nt lds\n"
      "v_mfma_f32_16x16x32_bf16 v[156:159], a[180:183], a[32:35], v[156:159]\n"
      "v_exp_f32 v207, v207\n"
      "v_mfma_f32_16x16x32_bf16 v[164:167], a[180:183], a[56:59], v[164:167]\n"
      "v_cvt_pk_bf16_f32 v202, v204, v205\n"
      "v_fma_f32 v209, v209, %[csc], v218\n"
      "v_mfma_f32_16x16x32_bf16 v[144:147], a[184:187], a[12:15], v[144:147]\n"
      "v_exp_f32 v208, v208\n"
      "v_mfma_f32_16x16x32_bf16 v[152:155], a[184:187], a[36:39], v[152:155]\n"
      "v_fma_f32 v210, v210, %[csc], v218\n"
      "v_mfma_f32_16x16x32_bf16 v[160:163], a[184:187], a[60:63], v[160:163]\n"
      "v_exp_f32 v209, v209\n"
      "v_mfma_f32_16x16x32_bf16 v[148:151], a[188:191], a[12:15], v[148:151]\n"
      "v_cvt_pk_bf16_f32 v203, v206, v207\n"
      "v_fma_f32 v211, v211, %[csc], v218\n"
      "v_mfma_f32_16x16x32_bf16 v[156:159], a[188:191], a[36:39], v[156:159]\n"
      "v_exp_f32 v210, v210\n"
      "v_mfma_f32_16x16x32_bf16 v[164:167], a[188:191], a[60:63], v[164:167]\n"
      "s_waitcnt lgkmcnt(6)\n"
      "v_fma_f32 v212, v212, %[csc], v218\n"
      "v_mfma_f32_16x16x32_bf16 v[144:147], a[192:195], a[16:19], v[144:147]\n"
      "s_add_u32 m0, %[m0b], 75776\n"
      "v_exp_f32 v211, v211\n"
      "v_mfma_f32_16x16x32_bf16 v[152:155], a[192:195], a[40:43], v[152:155]\n"
      "v_cvt_pk_bf16_f32 v208, v208, v209\n"
      "v_fma_f32 v213, v213, %[csc], v218\n"
      "v_mfma_f32_16x16x32_bf16 v[160:163], a[192:195], a[64:67], v[160:163]\n"
      "v_exp_f32 v212, v212\n"
      "v_mfma_f32_16x16x32_bf16 v[148:151], a[196:199], a[16:19], v[148:151]\n"
      "buffer_load_dwordx4 %[vv2], s[60:63], s71 offen nt lds\n"
      "v_fma_f32 v214, v214, %[csc], v218\n"
      "v_mfma_f32_16x16x32_bf16 v[156:159], a[196:199], a[40:43], v[156:159]\n"
      "v_exp_f32 v213, v213\n"
      "v_mfma_f32_16x16x32_bf16 v[164:167], a[196:199], a[64:67], v[164:167]\n"
      "v_cvt_pk_bf16_f32 v209, v210, v211\n"
      "v_fma_f32 v215, v215, %[csc], v218\n"
      "v_mfma_f32_16x16x32_bf16 v[144:147], a[200:203], a[20:23], v[144:147]\n"
      "v_exp_f32 v214, v214\n"
      "v_mfma_f32_16x16x32_bf16 v[152:155], a[200:203], a[44:47], v[152:155]\n"
      "v_exp_f32 v215, v215\n"
      "v_mfma_f32_16x16x32_bf16 v[160:163], a[200:203], a[68:71], v[160:163]\n"
      "v_cvt_pk_bf16_f32 v210, v212, v213\n"
      "v_mfma_f32_16x16x32_bf16 v[148:151], a[204:207], a[20:23], v[148:151]\n"
      "v_cvt_pk_bf16_f32 v211, v214, v215\n"
      "v_mfma_f32_16x16x32_bf16 v[156:159], a[204:207], a[44:47], v[156:159]\n"
      "ds_read_b64_tr_b16 a[156:157], %[vb3] offset:12288\n"
      "ds_read_b64_tr_b16 a[158:159], %[vb3] offset:18432\n"
      "v_mfma_f32_16x16x32_bf16 v[164:167], a[204:207], a[68:71], v[164:167]\n"
      "ds_read_b64_tr_b16 a[160:161], %[vb0] offset:12416\n"
      "ds_read_b64_tr_b16 a[162:163], %[vb0] offset:18560\n"
      "v_mfma_f32_16x16x32_bf16 a[208:211], a[220:223], v[192:195], a[208:211]\n"
      "ds_read_b64_tr_b16 a[164:165], %[vb1] offset:12416\n"
      "ds_read_b64_tr_b16 a[166:167], %[vb1] offset:18560\n"
      "v_mfma_f32_16x16x32_bf16 a[212:215], a[220:223], v[200:203], a[212:215]\n"
      "ds_read_b64_tr_b16 a[168:169], %[vb2] offset:12416\n"
      "ds_read_b64_tr_b16 a[170:171], %[vb2] offset:18560\n"
      "v_mfma_f32_16x16x32_bf16 a[216:219], a[220:223], v[208:211], a[216:219]\n"
      "s_waitcnt lgkmcnt(8)\n"
      "ds_read_b64_tr_b16 a[172:173], %[vb3] offset:12416\n"
      "ds_read_b64_tr_b16 a[174:175], %[vb3] offset:18560\n"
      "v_mfma_f32_16x16x32_bf16 v[0:3], a[144:147], v[192:195], v[0:3]\n"
      "ds_read_b64_tr_b16 a[176:177], %[vb0] offset:12544\n"
      "ds_read_b64_tr_b16 a[178:179], %[vb0] offset:18688\n"
      "v_mfma_f32_16x16x32_bf16 v[48:51], a[144:147], v[200:203], v[48:51]\n"
      "ds_read_b64_tr_b16 a[180:181], %[vb1] offset:12544\n"
      "ds_read_b64_tr_b16 a[182:183], %[vb1] offset:18688\n"
      "v_mfma_f32_16x16x32_bf16 v[96:99], a[144:147], v[208:211], v[96:99]\n"
      "ds_read_b64_tr_b16 a[184:185], %[vb2] offset:12544\n"
      "ds_read_b64_tr_b16 a[186:187], %[vb2] offset:18688\n"
      "v_mfma_f32_16x16x32_bf16 v[4:7], a[148:151], v[192:195], v[4:7]\n"
      "ds_read_b64_tr_b16 a[188:189], %[vb3] offset:12544\n"
      "ds_read_b64_tr_b16 a[190:191], %[vb3] offset:18688\n"
      "v_mfma_f32_16x16x32_bf16 v[52:55], a[148:151], v[200:203], v[52:55]\n"
      "v_max3_f32 v222, v144, v145, v146\n"
      "v_max3_f32 v223, v147, v148, v149\n"
      "v_mfma_f32_16x16x32_bf16 v[100:103], a[148:151], v[208:211], v[100:103]\n"
      "v_max3_f32 v222, v222, v150, v151\n"
      "v_max_f32 v222, v222, v223\n"
      "v_mfma_f32_16x16x32_bf16 v[8:11], a[152:155], v[192:195], v[8:11]\n"
      "v_mov_b32 v223, v222\n"
      "v_accvgpr_read_b32 v224, a208\n"
      "v_mfma_f32_16x16x32_bf16 v[56:59], a[152:155], v[200:203], v[56:59]\n"
      "s_add_u32 s72, s69, 37754880\n"
      "v_permlane16_swap_b32 v222, v223\n"
      "v_max_f32 v222, v222, v223\n"
      "v_mfma_f32_16x16x32_bf16 v[104:107], a[152:155], v[208:211], v[104:107]\n"
      "s_waitcnt lgkmcnt(10)\n"
      "v_mov_b32 v223, v222\n"
      "v_cmp_nge_f32 vcc, 0x47800000, v224\n"
      "s_waitcnt vmcnt(13) lgkmcnt(0)\n"
      "s_barrier\n"
      "v_mfma_f32_16x16x32_bf16 v[12:15], a[156:159], v[192:195], v[12:15]\n"
      "s_add_u32 s70, s68, 18923520\n"
      "ds_read_b128 a[192:195], %[kb00] offset:12288\n"
      "ds_read_b128 a[196:199], %[kb00] offset:18432\n"
      "v_mfma_f32_16x16x32_bf16 v[60:63], a[156:159], v[200:203], v[60:63]\n"
      "s_add_u32 m0, %[m0b], 0\n"
      "ds_read_b128 a[200:203], %[kb10] offset:12288\n"
      "ds_read_b128 a[204:207], %[kb10] offset:18432\n"
      "v_mfma_f32_16x16x32_bf16 v[108:111], a[156:159], v[208:211], v[108:111]\n"
      "buffer_load_dwordx4 %[vk0], s[56:59], s70 offen nt lds\n"
      "ds_read_b128 a[144:147], %[kb00] offset:12416\n"
      "v_mfma_f32_16x16x32_bf16 v[16:19], a[160:163], v[192:195], v[16:19]\n"
      "s_add_u32 m0, %[m0q], 2048\n"
      "ds_read_b128 a[148:151], %[kb00] offset:18560\n"
      "ds_read_b128 a[152:155], %[kb10] offset:12416\n"
      "v_mfma_f32_16x16x32_bf16 v[64:67], a[160:163], v[200:203], v[64:67]\n"
      "buffer_load_dwordx4 %[vq0], s[80:83], s72 offen nt lds\n"
      "ds_read_b128 a[156:159], %[kb10] offset:18560\n"
      "v_mfma_f32_16x16x32_bf16 v[112:115], a[160:163], v[208:211], v[112:115]\n"
      "s_add_u32 m0, %[m0q], 3072\n"
      "ds_read_b128 a[160:163], %[kb00] offset:12544\n"
      "v_permlane32_swap_b32 v222, v223\n"
      "v_mfma_f32_16x16x32_bf16 v[20:23], a[164:167], v[192:195], v[20:23]\n"
      "buffer_load_dwordx4 %[vq1], s[80:83], s72 offen nt lds\n"
      "v_max_f32 v222, v222, v223\n"
      "v_mfma_f32_16x16x32_bf16 v[68:71], a[164:167], v[200:203], v[68:71]\n"
      "v_mul_f32_e64 v216, -%[csc], v222\n"
      "v_max3_f32 v222, v152, v153, v154\n"
      "v_rcp_f32 v219, v224\n"
      "s_or_b64 %[flagB], %[flagB], vcc\n"
      "v_accvgpr_read_b32 v224, a212\n"
      "v_cmp_nge_f32 vcc, 0x47800000, v224\n"
      "v_rcp_f32 v220, v224\n"
      "s_nop 2\n"
      "s_or_b64 %[flagB], %[flagB], vcc\n"
      "v_accvgpr_read_b32 v224, a216\n"
      "v_cmp_nge_f32 vcc, 0x47800000, v224\n"
      "v_rcp_f32 v221, v224\n"
      "s_nop 2\n"
      "s_or_b64 %[flagB], %[flagB], vcc\n"
      "v_mul_f32 v0, v0, v219\n"
      "v_mul_f32 v1, v1, v219\n"
      "v_mul_f32 v2, v2, v219\n"
      "v_mul_f32 v3, v3, v219\n"
      "v_mul_f32 v48, v48, v220\n"
      "v_mul_f32 v49, v49, v220\n"
      "v_cvt_pk_bf16_f32 v0, v0, v1\n"
      "v_mul_f32 v50, v50, v220\n"
      "v_cvt_pk_bf16_f32 v1, v2, v3\n"
      "v_mul_f32 v51, v51, v220\n"
      "v_mul_f32 v96, v96, v221\n"
      "v_accvgpr_write_b32 a72, v0\n"
      "v_mul_f32 v97, v97, v221\n"
      "v_cvt_pk_bf16_f32 v48, v48, v49\n"
      "v_mul_f32 v98, v98, v221\n"
      "v_cvt_pk_bf16_f32 v49, v50, v51\n"
      "v_mul_f32 v99, v99, v221\n"
      "v_accvgpr_write_b32 a73, v1\n"
      "v_mfma_f32_16x16x32_bf16 v[116:119], a[164:167], v[208:211], v[116:119]\n"
      "v_mul_f32 v4, v4, v219\n"
      "v_accvgpr_write_b32 a96, v48\n"
      "v_mfma_f32_16x16x32_bf16 v[24:27], a[168:171], v[192:195], v[24:27]\n"
      "v_mul_f32 v5, v5, v219\n"
      "v_cvt_pk_bf16_f32 v96, v96, v97\n"
      "v_mfma_f32_16x16x32_bf16 v[72:75], a[168:171], v[200:203], v[72:75]\n"
      "s_add_u32 m0, %[m0b], 1024\n"
      "v_mul_f32 v6, v6, v219\n"
      "v_cvt_pk_bf16_f32 v97, v98, v99\n"
      "v_mfma_f32_16x16x32_bf16 v[120:123], a[168:171], v[208:211], v[120:123]\n"
      "v_mul_f32 v7, v7, v219\n"
      "v_accvgpr_write_b32 a97, v49\n"
      "v_mul_f32 v52, v52, v220\n"
      "v_accvgpr_write_b32 a120, v96\n"
      "v_mul_f32 v53, v53, v220\n"
      "v_cvt_pk_bf16_f32 v4, v4, v5\n"
      "v_mul_f32 v54, v54, v220\n"
      "v_cvt_pk_bf16_f32 v5, v6, v7\n"
      "v_mul_f32 v55, v55, v220\n"
      "v_accvgpr_write_b32 a121, v97\n"
      "v_mul_f32 v100, v100, v221\n"
      "v_accvgpr_write_b32 a74, v4\n"
      "v_mul_f32 v101, v101, v221\n"
      "v_cvt_pk_bf16_f32 v52, v52, v53\n"
      "v_mul_f32 v102, v102, v221\n"
      "v_cvt_pk_bf16_f32 v53, v54, v55\n"
      "v_mul_f32 v103, v103, v221\n"
      "v_accvgpr_write_b32 a75, v5\n"
      "v_mfma_f32_16x16x32_bf16 v[28:31], a[172:175], v[192:195], v[28:31]\n"
      "buffer_load_dwordx4 %[vk1], s[56:59], s70 offen nt lds\n"
      "v_mul_f32 v8, v8, v219\n"
      "v_mfma_f32_16x16x32_bf16 v[76:79], a[172:175], v[200:203], v[76:79]\n"
      "v_accvgpr_write_b32 a98, v52\n"
      "v_mul_f32 v9, v9, v219\n"
      "v_mfma_f32_16x16x32_bf16 v[124:127], a[172:175], v[208:211], v[124:127]\n"
      "v_cvt_pk_bf16_f32 v100, v100, v101\n"
      "v_mul_f32 v10, v10, v219\n"
      "v_cvt_pk_bf16_f32 v101, v102, v103\n"
      "v_mul_f32 v11, v11, v219\n"
      "v_accvgpr_write_b32 a99, v53\n"
      "v_mul_f32 v56, v56, v220\n"
      "v_accvgpr_write_b32 a122, v100\n"
      "v_mul_f32 v57, v57, v220\n"
      "v_cvt_pk_bf16_f32 v8, v8, v9\n"
      "v_mul_f32 v58, v58, v220\n"
      "v_cvt_pk_bf16_f32 v9, v10, v11\n"
      "v_mul_f32 v59, v59, v220\n"
      "v_accvgpr_write_b32 a123, v101\n"
      "v_mul_f32 v104, v104, v221\n"
      "v_accvgpr_write_b32 a76, v8\n"
      "v_mul_f32 v105, v105, v221\n"
      "v_cvt_pk_bf16_f32 v56, v56, v57\n"
      "v_mul_f32 v106, v106, v221\n"
      "v_cvt_pk_bf16_f32 v57, v58, v59\n"
      "v_mul_f32 v107, v107, v221\n"
      "v_accvgpr_write_b32 a77, v9\n"
      "v_mfma_f32_16x16x32_bf16 v[32:35], a[176:179], v[192:195], v[32:35]\n"
      "v_mul_f32 v12, v12, v219\n"
      "v_accvgpr_write_b32 a100, v56\n"
      "v_mfma_f32_16x16x32_bf16 v[80:83], a[176:179], v[200:203], v[80:83]\n"
      "ds_read_b128 a[164:167], %[kb00] offset:18688\n"
      "v_mul_f32 v13, v13, v219\n"
      "v_mfma_f32_16x16x32_bf16 v[128:131], a[176:179], v[208:211], v[128:131]\n"
      "v_cvt_pk_bf16_f32 v104, v104, v105\n"
      "v_mul_f32 v14, v14, v219\n"
      "v_mfma_f32_16x16x32_bf16 v[36:39], a[180:183], v[192:195], v[36:39]\n"
      "v_cvt_pk_bf16_f32 v105, v106, v107\n"
      "v_mul_f32 v15, v15, v219\n"
      "v_accvgpr_write_b32 a101, v57\n"
      "v_mul_f32 v60, v60, v220\n"
      "v_accvgpr_write_b32 a124, v104\n"
      "v_mul_f32 v61, v61, v220\n"
      "v_cvt_pk_bf16_f32 v12, v12, v13\n"
      "v_mul_f32 v62, v62, v220\n"
      "v_cvt_pk_bf16_f32 v13, v14, v15\n"
      "v_mul_f32 v63, v63, v220\n"
      "v_accvgpr_write_b32 a125, v105\n"
      "v_mul_f32 v108, v108, v221\n"
      "v_accvgpr_write_b32 a78, v12\n"
      "v_mul_f32 v109, v109, v221\n"
      "v_cvt_pk_bf16_f32 v60, v60, v61\n"
      "v_mul_f32 v110, v110, v221\n"
      "v_cvt_pk_bf16_f32 v61, v62, v63\n"
      "v_mul_f32 v111, v111, v221\n"
      "v_accvgpr_write_b32 a79, v13\n"
      "v_mfma_f32_16x16x32_bf16 v[84:87], a[180:183], v[200:203], v[84:87]\n"
      "ds_read_b128 a[168:171], %[kb10] offset:12544\n"
      "ds_read_b128 a[172:175], %[kb10] offset:18688\n"
      "v_mfma_f32_16x16x32_bf16 v[132:135], a[180:183], v[208:211], v[132:135]\n"
      "s_add_u32 m0, %[m0b], 2048\n"
      "v_mul_f32 v16, v16, v219\n"
      "v_accvgpr_write_b32 a102, v60\n"
      "v_mfma_f32_16x16x32_bf16 v[40:43], a[184:187], v[192:195], v[40:43]\n"
      "buffer_load_dwordx4 %[vk2], s[56:59], s70 offen nt lds\n"
      "v_mul_f32 v17, v17, v219\n"
      "v_mfma_f32_16x16x32_bf16 v[88:91], a[184:187], v[200:203], v[88:91]\n"
      "v_cvt_pk_bf16_f32 v108, v108, v109\n"
      "v_mul_f32 v18, v18, v219\n"
      "v_mfma_f32_16x16x32_bf16 v[136:139], a[184:187], v[208:211], v[136:139]\n"
      "v_cvt_pk_bf16_f32 v109, v110, v111\n"
      "v_mul_f32 v19, v19, v219\n"
      "v_mfma_f32_16x16x32_bf16 v[44:47], a[188:191], v[192:195], v[44:47]\n"
      "ds_read_b128 a[176:179], %[kb00] offset:24576\n"
      "v_accvgpr_write_b32 a103, v61\n"
      "v_mfma_f32_16x16x32_bf16 v[92:95], a[188:191], v[200:203], v[92:95]\n"
      "ds_read_b128 a[180:183], %[kb00] offset:30720\n"
      "ds_read_b128 a[184:187], %[kb10] offset:24576\n"
      "v_mfma_f32_16x16x32_bf16 v[140:143], a[188:191], v[208:211], v[140:143]\n"
      "s_waitcnt lgkmcnt(11)\n"
      "ds_read_b128 a[188:191], %[kb10] offset:30720\n"
      "v_mul_f32 v64, v64, v220\n"
      "v_mfma_f32_16x16x32_bf16 v[168:171], a[192:195], a[0:3], 0\n"
      "v_accvgpr_write_b32 a126, v108\n"
      "v_mul_f32 v65, v65, v220\n"
      "v_mfma_f32_16x16x32_bf16 v[176:179], a[192:195], a[24:27], 0\n"
      "v_cvt_pk_bf16_f32 v16, v16, v17\n"
      "v_mul_f32 v66, v66, v220\n"
      "v_mfma_f32_16x16x32_bf16 v[184:187], a[192:195], a[48:51], 0\n"
      "ds_read_b128 a[192:195], %[kb00] offset:24704\n"
      "v_cvt_pk_bf16_f32 v17, v18, v19\n"
      "v_mfma_f32_16x16x32_bf16 v[172:175], a[196:199], a[0:3], 0\n"
      "s_add_u32 s71, s68, 18911232\n"
      "v_mul_f32 v67, v67, v220\n"
      "v_accvgpr_write_b32 a127, v109\n"
      "v_mfma_f32_16x16x32_bf16 v[180:183], a[196:199], a[24:27], 0\n"
      "s_add_u32 m0, %[m0b], 86016\n"
      "v_mul_f32 v112, v112, v221\n"
      "v_accvgpr_write_b32 a80, v16\n"
      "v_mfma_f32_16x16x32_bf16 v[188:191], a[196:199], a[48:51], 0\n"
      "buffer_load_dwordx4 %[vv0], s[60:63], s71 offen nt lds\n"
      "ds_read_b128 a[196:199], %[kb00] offset:30848\n"
      "v_mfma_f32_16x16x32_bf16 v[168:171], a[200:203], a[4:7], v[168:171]\n"
      "v_mul_f32 v113, v113, v221\n"
      "v_cvt_pk_bf16_f32 v64, v64, v65\n"
      "v_mfma_f32_16x16x32_bf16 v[176:179], a[200:203], a[28:31], v[176:179]\n"
      "v_mul_f32 v114, v114, v221\n"
      "v_cvt_pk_bf16_f32 v65, v66, v67\n"
      "v_mfma_f32_16x16x32_bf16 v[184:187], a[200:203], a[52:55], v[184:187]\n"
      "ds_read_b128 a[200:203], %[kb10] offset:24704\n"
      "v_mul_f32 v115, v115, v221\n"
      "v_mfma_f32_16x16x32_bf16 v[172:175], a[204:207], a[4:7], v[172:175]\n"
      "v_accvgpr_write_b32 a81, v17\n"
      "v_mul_f32 v20, v20, v219\n"
      "v_mfma_f32_16x16x32_bf16 v[180:183], a[204:207], a[28:31], v[180:183]\n"
      "v_accvgpr_write_b32 a104, v64\n"
      "v_mul_f32 v21, v21, v219\n"
      "v_mfma_f32_16x16x32_bf16 v[188:191], a[204:207], a[52:55], v[188:191]\n"
      "s_waitcnt lgkmcnt(11)\n"
      "ds_read_b128 a[204:207], %[kb10] offset:30848\n"
      "v_cvt_pk_bf16_f32 v112, v112, v113\n"
      "v_mfma_f32_16x16x32_bf16 v[168:171], a[144:147], a[8:11], v[168:171]\n"
      "v_mul_f32 v22, v22, v219\n"
      "v_cvt_pk_bf16_f32 v113, v114, v115\n"
      "v_mfma_f32_16x16x32_bf16 v[176:179], a[144:147], a[32:35], v[176:179]\n"
      "s_add_u32 m0, %[m0b], 87040\n"
      "v_mul_f32 v23, v23, v219\n"
      "v_accvgpr_write_b32 a105, v65\n"
      "v_mfma_f32_16x16x32_bf16 v[184:187], a[144:147], a[56:59], v[184:187]\n"
      "buffer_load_dwordx4 %[vv1], s[60:63], s71 offen nt lds\n"
      "v_mul_f32 v68, v68, v220\n"
      "v_mfma_f32_16x16x32_bf16 v[172:175], a[148:151], a[8:11], v[172:175]\n"
      "ds_read_b128 a[144:147], %[kb00] offset:24832\n"
      "v_accvgpr_write_b32 a128, v112\n"
      "v_mfma_f32_16x16x32_bf16 v[180:183], a[148:151], a[32:35], v[180:183]\n"
      "v_mul_f32 v69, v69, v220\n"
      "v_cvt_pk_bf16_f32 v20, v20, v21\n"
      "v_mfma_f32_16x16x32_bf16 v[188:191], a[148:151], a[56:59], v[188:191]\n"
      "v_mul_f32 v70, v70, v220\n"
      "v_cvt_pk_bf16_f32 v21, v22, v23\n"
      "v_mfma_f32_16x16x32_bf16 v[168:171], a[152:155], a[12:15], v[168:171]\n"
      "ds_read_b128 a[148:151], %[kb00] offset:30976\n"
      "v_mul_f32 v71, v71, v220\n"
      "v_mfma_f32_16x16x32_bf16 v[176:179], a[152:155], a[36:39], v[176:179]\n"
      "v_accvgpr_write_b32 a129, v113\n"
      "v_mul_f32 v116, v116, v221\n"
      "v_mfma_f32_16x16x32_bf16 v[184:187], a[152:155], a[60:63], v[184:187]\n"
      "ds_read_b128 a[152:155], %[kb10] offset:24832\n"
      "v_accvgpr_write_b32 a82, v20\n"
      "v_mfma_f32_16x16x32_bf16 v[172:175], a[156:159], a[12:15], v[172:175]\n"
      "v_mul_f32 v117, v117, v221\n"
      "v_cvt_pk_bf16_f32 v68, v68, v69\n"
      "v_mfma_f32_16x16x32_bf16 v[180:183], a[156:159], a[36:39], v[180:183]\n"
      "v_mul_f32 v118, v118, v221\n"
      "v_cvt_pk_bf16_f32 v69, v70, v71\n"
      "v_mfma_f32_16x16x32_bf16 v[188:191], a[156:159], a[60:63], v[188:191]\n"
      "s_waitcnt lgkmcnt(11)\n"
      "v_mul_f32 v119, v119, v221\n"
      "v_accvgpr_write_b32 a83, v21\n"
      "v_mfma_f32_16x16x32_bf16 v[168:171], a[160:163], a[16:19], v[168:171]\n"
      "s_add_u32 m0, %[m0b], 88064\n"
      "v_mul_f32 v24, v24, v219\n"
      "v_accvgpr_write_b32 a106, v68\n"
      "v_mfma_f32_16x16x32_bf16 v[176:179], a[160:163], a[40:43], v[176:179]\n"
      "buffer_load_dwordx4 %[vv2], s[60:63], s71 offen nt lds\n"
      "v_mul_f32 v25, v25, v219\n"
      "v_mfma_f32_16x16x32_bf16 v[184:187], a[160:163], a[64:67], v[184:187]\n"
      "v_cvt_pk_bf16_f32 v116, v116, v117\n"
      "v_mul_f32 v26, v26, v219\n"
      "v_mfma_f32_16x16x32_bf16 v[172:175], a[164:167], a[16:19], v[172:175]\n"
      "v_cvt_pk_bf16_f32 v117, v118, v119\n"
      "v_mul_f32 v27, v27, v219\n"
      "v_mfma_f32_16x16x32_bf16 v[180:183], a[164:167], a[40:43], v[180:183]\n"
      "v_accvgpr_write_b32 a107, v69\n"
      "v_mul_f32 v72, v72, v220\n"
      "v_mfma_f32_16x16x32_bf16 v[188:191], a[164:167], a[64:67], v[188:191]\n"
      "v_accvgpr_write_b32 a130, v116\n"
      "v_mul_f32 v73, v73, v220\n"
      "v_mfma_f32_16x16x32_bf16 v[168:171], a[168:171], a[20:23], v[168:171]\n"
      "v_cvt_pk_bf16_f32 v24, v24, v25\n"
      "v_mul_f32 v74, v74, v220\n"
      "v_cvt_pk_bf16_f32 v25, v26, v27\n"
      "v_mul_f32 v75, v75, v220\n"
      "v_accvgpr_write_b32 a131, v117\n"
      "v_mul_f32 v120, v120, v221\n"
      "v_accvgpr_write_b32 a84, v24\n"
      "v_mul_f32 v121, v121, v221\n"
      "v_cvt_pk_bf16_f32 v72, v72, v73\n"
      "v_mul_f32 v122, v122, v221\n"
      "v_cvt_pk_bf16_f32 v73, v74, v75\n"
      "v_mul_f32 v123, v123, v221\n"
      "v_accvgpr_write_b32 a85, v25\n"
      "v_mfma_f32_16x16x32_bf16 v[176:179], a[168:171], a[44:47], v[176:179]\n"
      "v_mul_f32 v28, v28, v219\n"
      "v_accvgpr_write_b32 a108, v72\n"
      "v_mfma_f32_16x16x32_bf16 v[184:187], a[168:171], a[68:71], v[184:187]\n"
      "ds_read_b128 a[156:159], %[kb10] offset:30976\n"
      "v_mul_f32 v29, v29, v219\n"
      "v_mfma_f32_16x16x32_bf16 v[172:175], a[172:175], a[20:23], v[172:175]\n"
      "v_cvt_pk_bf16_f32 v120, v120, v121\n"
      "v_mul_f32 v30, v30, v219\n"
      "v_cvt_pk_bf16_f32 v121, v122, v123\n"
      "v_mul_f32 v31, v31, v219\n"
      "v_accvgpr_write_b32 a109, v73\n"
      "v_mul_f32 v76, v76, v220\n"
      "v_accvgpr_write_b32 a132, v120\n"
      "v_mul_f32 v77, v77, v220\n"
      "v_cvt_pk_bf16_f32 v28, v28, v29\n"
      "v_mul_f32 v78, v78, v220\n"
      "v_cvt_pk_bf16_f32 v29, v30, v31\n"
      "v_mul_f32 v79, v79, v220\n"
      "v_accvgpr_write_b32 a133, v121\n"
      "v_mul_f32 v124, v124, v221\n"
      "v_accvgpr_write_b32 a86, v28\n"
      "v_mul_f32 v125, v125, v221\n"
      "v_cvt_pk_bf16_f32 v76, v76, v77\n"
      "v_mul_f32 v126, v126, v221\n"
      "v_cvt_pk_bf16_f32 v77, v78, v79\n"
      "v_mul_f32 v127, v127, v221\n"
      "v_accvgpr_write_b32 a87, v29\n"
      "v_mfma_f32_16x16x32_bf16 v[180:183], a[172:175], a[44:47], v[180:183]\n"
      "v_mul_f32 v32, v32, v219\n"
      "v_accvgpr_write_b32 a110, v76\n"
      "v_mfma_f32_16x16x32_bf16 v[188:191], a[172:175], a[68:71], v[188:191]\n"
      "s_waitcnt lgkmcnt(8)\n"
      "v_mul_f32 v33, v33, v219\n"
      "v_cvt_pk_bf16_f32 v124, v124, v125\n"
      "v_mfma_f32_16x16x32_bf16 v[192:195], a[176:179], a[0:3], 0\n"
      "v_mul_f32 v34, v34, v219\n"
      "v_cvt_pk_bf16_f32 v125, v126, v127\n"
      "v_mfma_f32_16x16x32_bf16 v[200:203], a[176:179], a[24:27], 0\n"
      "v_mul_f32 v35, v35, v219\n"
      "v_accvgpr_write_b32 a111, v77\n"
      "s_waitcnt vmcnt(17) lgkmcnt(0)\n"
      "s_barrier\n"
      "v_mfma_f32_16x16x32_bf16 v[208:211], a[176:179], a[48:51], 0\n"
      "s_add_u32 s70, s68, 18935808\n"
      "ds_read_b64_tr_b16 a[160:161], %[vb0] offset:24576\n"
      "ds_read_b64_tr_b16 a[162:163], %[vb0] offset:30720\n"
      "v_mfma_f32_16x16x32_bf16 v[196:199], a[180:183], a[0:3], 0\n"
      "s_add_u32 m0, %[m0b], 12288\n"
      "ds_read_b64_tr_b16 a[164:165], %[vb1] offset:24576\n"
      "ds_read_b64_tr_b16 a[166:167], %[vb1] offset:30720\n"
      "v_mfma_f32_16x16x32_bf16 v[204:207], a[180:183], a[24:27], 0\n"
      "buffer_load_dwordx4 %[vk0], s[56:59], s70 offen nt lds\n"
      "v_max3_f32 v223, v155, v156, v157\n"
      "v_mfma_f32_16x16x32_bf16 v[212:215], a[180:183], a[48:51], 0\n"
      "ds_read_b64_tr_b16 a[168:169], %[vb2] offset:24576\n"
      "ds_read_b64_tr_b16 a[170:171], %[vb2] offset:30720\n"
      "v_mfma_f32_16x16x32_bf16 v[192:195], a[184:187], a[4:7], v[192:195]\n"
      "v_max3_f32 v222, v222, v158, v159\n"
      "v_max_f32 v222, v222, v223\n"
      "v_mfma_f32_16x16x32_bf16 v[200:203], a[184:187], a[28:31], v[200:203]\n"
      "v_mov_b32 v223, v222\n"
      "v_mul_f32 v80, v80, v220\n"
      "v_mfma_f32_16x16x32_bf16 v[208:211], a[184:187], a[52:55], v[208:211]\n"
      "v_accvgpr_write_b32 a134, v124\n"
      "v_permlane16_swap_b32 v222, v223\n"
      "v_mfma_f32_16x16x32_bf16 v[196:199], a[188:191], a[4:7], v[196:199]\n"
      "v_max_f32 v222, v222, v223\n"
      "v_mov_b32 v223, v222\n"
      "v_mfma_f32_16x16x32_bf16 v[204:207], a[188:191], a[28:31], v[204:207]\n"
      "s_nop 0\n"
      "v_permlane32_swap_b32 v222, v223\n"
      "v_max_f32 v222, v222, v223\n"
      "v_mfma_f32_16x16x32_bf16 v[212:215], a[188:191], a[52:55], v[212:215]\n"
      "v_mul_f32_e64 v217, -%[csc], v222\n"
      "v_max3_f32 v222, v160, v161, v162\n"
      "v_mfma_f32_16x16x32_bf16 v[192:195], a[192:195], a[8:11], v[192:195]\n"
      "s_add_u32 m0, %[m0b], 13312\n"
      "v_max3_f32 v223, v163, v164, v165\n"
      "v_max3_f32 v222, v222, v166, v167\n"
      "v_mfma_f32_16x16x32_bf16 v[200:203], a[192:195], a[32:35], v[200:203]\n"
      "buffer_load_dwordx4 %[vk1], s[56:59], s70 offen nt lds\n"
      "v_max_f32 v222, v222, v223\n"
      "v_mfma_f32_16x16x32_bf16 v[208:211], a[192:195], a[56:59], v[208:211]\n"
      "v_mov_b32 v223, v222\n"
      "v_mul_f32 v81, v81, v220\n"
      "v_mfma_f32_16x16x32_bf16 v[196:199], a[196:199], a[8:11], v[196:199]\n"
      "v_cvt_pk_bf16_f32 v32, v32, v33\n"
      "v_permlane16_swap_b32 v222, v223\n"
      "v_mfma_f32_16x16x32_bf16 v[204:207], a[196:199], a[32:35], v[204:207]\n"
      "v_max_f32 v222, v222, v223\n"
      "v_mov_b32 v223, v222\n"
      "v_mfma_f32_16x16x32_bf16 v[212:215], a[196:199], a[56:59], v[212:215]\n"
      "s_nop 0\n"
      "v_permlane32_swap_b32 v222, v223\n"
      "v_max_f32 v222, v222, v223\n"
      "v_mfma_f32_16x16x32_bf16 v[192:195], a[200:203], a[12:15], v[192:195]\n"
      "v_mul_f32_e64 v218, -%[csc], v222\n"
      "v_fma_f32 v144, v144, %[csc], v216\n"
      "v_mfma_f32_16x16x32_bf16 v[200:203], a[200:203], a[36:39], v[200:203]\n"
      "v_fma_f32 v145, v145, %[csc], v216\n"
      "v_mul_f32 v82, v82, v220\n"
      "v_mfma_f32_16x16x32_bf16 v[208:211], a[200:203], a[60:63], v[208:211]\n"
      "s_add_u32 s72, s69, 37761024\n"
      "v_cvt_pk_bf16_f32 v33, v34, v35\n"
      "v_mul_f32 v83, v83, v220\n"
      "v_mfma_f32_16x16x32_bf16 v[196:199], a[204:207], a[12:15], v[196:199]\n"
      "s_add_u32 m0, %[m0q], 4096\n"
      "v_accvgpr_write_b32 a135, v125\n"
      "v_mul_f32 v128, v128, v221\n"
      "v_mfma_f32_16x16x32_bf16 v[204:207], a[204:207], a[36:39], v[204:207]\n"
      "buffer_load_dwordx4 %[vq0], s[80:83], s72 offen nt lds\n"
      "v_accvgpr_write_b32 a88, v32\n"
      "v_mfma_f32_16x16x32_bf16 v[212:215], a[204:207], a[60:63], v[212:215]\n"
      "s_add_u32 m0, %[m0b], 14336\n"
      "v_exp_f32 v144, v144\n"
      "v_mfma_f32_16x16x32_bf16 v[192:195], a[144:147], a[16:19], v[192:195]\n"
      "buffer_load_dwordx4 %[vk2], s[56:59], s70 offen nt lds\n"
      "v_fma_f32 v146, v146, %[csc], v216\n"
      "v_mfma_f32_16x16x32_bf16 v[200:203], a[144:147], a[40:43], v[200:203]\n"
      "s_add_u32 m0, %[m0q], 5120\n"
      "v_exp_f32 v145, v145\n"
      "v_mfma_f32_16x16x32_bf16 v[208:211], a[144:147], a[64:67], v[208:211]\n"
      "buffer_load_dwordx4 %[vq1], s[80:83], s72 offen nt lds\n"
      "v_fma_f32 v147, v147, %[csc], v216\n"
      "v_mfma_f32_16x16x32_bf16 v[196:199], a[148:151], a[16:19], v[196:199]\n"
      "v_exp_f32 v146, v146\n"
      "v_mfma_f32_16x16x32_bf16 v[204:207], a[148:151], a[40:43], v[204:207]\n"
      "v_fma_f32 v148, v148, %[csc], v216\n"
      "v_mul_f32 v129, v129, v221\n"
      "v_mfma_f32_16x16x32_bf16 v[212:215], a[148:151], a[64:67], v[212:215]\n"
      "v_exp_f32 v147, v147\n"
      "v_cvt_pk_bf16_f32 v80, v80, v81\n"
      "v_mul_f32 v130, v130, v221\n"
      "v_cvt_pk_bf16_f32 v81, v82, v83\n"
      "v_mul_f32 v131, v131, v221\n"
      "v_accvgpr_write_b32 a89, v33\n"
      "v_mfma_f32_16x16x32_bf16 v[192:195], a[152:155], a[20:23], v[192:195]\n"
      "v_mul_f32 v36, v36, v219\n"
      "v_cvt_pk_bf16_f32 v144, v144, v145\n"
      "v_mfma_f32_16x16x32_bf16 v[200:203], a[152:155], a[44:47], v[200:203]\n"
      "v_fma_f32 v149, v149, %[csc], v216\n"
      "v_accvgpr_write_b32 a112, v80\n"
      "v_mfma_f32_16x16x32_bf16 v[208:211], a[152:155], a[68:71], v[208:211]\n"
      "s_add_u32 s70, s68, 18948096\n"
      "v_mul_f32 v37, v37, v219\n"
      "v_cvt_pk_bf16_f32 v128, v128, v129\n"
      "v_mfma_f32_16x16x32_bf16 v[196:199], a[156:159], a[20:23], v[196:199]\n"
      "s_add_u32 m0, %[m0b], 24576\n"
      "v_mul_f32 v38, v38, v219\n"
      "v_cvt_pk_bf16_f32 v129, v130, v131\n"
      "v_mul_f32 v39, v39, v219\n"
      "v_accvgpr_write_b32 a113, v81\n"
      "v_mul_f32 v84, v84, v220\n"
      "v_accvgpr_write_b32 a136, v128\n"
      "v_mul_f32 v85, v85, v220\n"
      "v_cvt_pk_bf16_f32 v36, v36, v37\n"
      "v_mul_f32 v86, v86, v220\n"
      "v_cvt_pk_bf16_f32 v37, v38, v39\n"
      "v_mul_f32 v87, v87, v220\n"
      "v_accvgpr_write_b32 a137, v129\n"
      "v_mul_f32 v132, v132, v221\n"
      "v_accvgpr_write_b32 a90, v36\n"
      "v_mul_f32 v133, v133, v221\n"
      "v_cvt_pk_bf16_f32 v84, v84, v85\n"
      "v_mul_f32 v134, v134, v221\n"
      "v_cvt_pk_bf16_f32 v85, v86, v87\n"
      "v_mul_f32 v135, v135, v221\n"
      "v_accvgpr_write_b32 a91, v37\n"
      "v_mfma_f32_16x16x32_bf16 v[204:207], a[156:159], a[44:47], v[204:207]\n"
      "v_exp_f32 v148, v148\n"
      "v_mfma_f32_16x16x32_bf16 v[212:215], a[156:159], a[68:71], v[212:215]\n"
      "v_fma_f32 v150, v150, %[csc], v216\n"
      "v_exp_f32 v149, v149\n"
      "v_cvt_pk_bf16_f32 v145, v146, v147\n"
      "v_fma_f32 v151, v151, %[csc], v216\n"
      "v_exp_f32 v150, v150\n"
      "v_fma_f32 v152, v152, %[csc], v217\n"
      "v_exp_f32 v151, v151\n"
      "v_cvt_pk_bf16_f32 v146, v148, v149\n"
      "v_fma_f32 v153, v153, %[csc], v217\n"
      "v_exp_f32 v152, v152\n"
      "v_fma_f32 v154, v154, %[csc], v217\n"
      "v_exp_f32 v153, v153\n"
      "v_cvt_pk_bf16_f32 v147, v150, v151\n"
      "v_fma_f32 v155, v155, %[csc], v217\n"
      "v_exp_f32 v154, v154\n"
      "v_fma_f32 v156, v156, %[csc], v217\n"
      "v_exp_f32 v155, v155\n"
      "v_cvt_pk_bf16_f32 v152, v152, v153\n"
      "v_fma_f32 v157, v157, %[csc], v217\n"
      "v_exp_f32 v156, v156\n"
      "v_fma_f32 v158, v158, %[csc], v217\n"
      "v_exp_f32 v157, v157\n"
      "v_cvt_pk_bf16_f32 v153, v154, v155\n"
      "v_fma_f32 v159, v159, %[csc], v217\n"
      "v_exp_f32 v158, v158\n"
      "v_fma_f32 v160, v160, %[csc], v218\n"
      "v_exp_f32 v159, v159\n"
      "v_cvt_pk_bf16_f32 v154, v156, v157\n"
      "v_fma_f32 v161, v161, %[csc], v218\n"
      "v_exp_f32 v160, v160\n"
      "v_fma_f32 v162, v162, %[csc], v218\n"
      "v_exp_f32 v161, v161\n"
      "v_cvt_pk_bf16_f32 v155, v158, v159\n"
      "v_fma_f32 v163, v163, %[csc], v218\n"
      "v_exp_f32 v162, v162\n"
      "v_fma_f32 v164, v164, %[csc], v218\n"
      "v_exp_f32 v163, v163\n"
      "v_cvt_pk_bf16_f32 v160, v160, v161\n"
      "v_fma_f32 v165, v165, %[csc], v218\n"
      "v_exp_f32 v164, v164\n"
      "v_fma_f32 v166, v166, %[csc], v218\n"
      "v_exp_f32 v165, v165\n"
      "v_cvt_pk_bf16_f32 v161, v162, v163\n"
      "v_fma_f32 v167, v167, %[csc], v218\n"
      "v_exp_f32 v166, v166\n"
      "v_exp_f32 v167, v167\n"
      "v_cvt_pk_bf16_f32 v162, v164, v165\n"
      "v_cvt_pk_bf16_f32 v163, v166, v167\n"
      "v_mfma_f32_16x16x32_bf16 a[208:211], a[220:223], v[144:147], 0\n"
      "ds_read_b64_tr_b16 a[172:173], %[vb3] offset:24576\n"
      "ds_read_b64_tr_b16 a[174:175], %[vb3] offset:30720\n"
      "v_mfma_f32_16x16x32_bf16 a[212:215], a[220:223], v[152:155], 0\n"
      "buffer_load_dwordx4 %[vk0], s[56:59], s70 offen nt lds\n"
      "v_mul_f32 v40, v40, v219\n"
      "v_accvgpr_write_b32 a114, v84\n"
      "v_mul_f32 v41, v41, v219\n"
      "v_cvt_pk_bf16_f32 v132, v132, v133\n"
      "v_mul_f32 v42, v42, v219\n"
      "v_cvt_pk_bf16_f32 v133, v134, v135\n"
      "v_mul_f32 v43, v43, v219\n"
      "v_accvgpr_write_b32 a115, v85\n"
      "v_mul_f32 v88, v88, v220\n"
      "v_accvgpr_write_b32 a138, v132\n"
      "v_mul_f32 v89, v89, v220\n"
      "v_cvt_pk_bf16_f32 v40, v40, v41\n"
      "v_mul_f32 v90, v90, v220\n"
      "v_cvt_pk_bf16_f32 v41, v42, v43\n"
      "v_mul_f32 v91, v91, v220\n"
      "v_accvgpr_write_b32 a139, v133\n"
      "v_mul_f32 v136, v136, v221\n"
      "v_accvgpr_write_b32 a92, v40\n"
      "v_mul_f32 v137, v137, v221\n"
      "v_cvt_pk_bf16_f32 v88, v88, v89\n"
      "v_mul_f32 v138, v138, v221\n"
      "v_cvt_pk_bf16_f32 v89, v90, v91\n"
      "v_mul_f32 v139, v139, v221\n"
      "v_accvgpr_write_b32 a93, v41\n"
      "v_mfma_f32_16x16x32_bf16 a[216:219], a[220:223], v[160:163], 0\n"
      "s_waitcnt lgkmcnt(2)\n"
      "ds_read_b64_tr_b16 a[176:177], %[vb0] offset:24704\n"
      "ds_read_b64_tr_b16 a[178:179], %[vb0] offset:30848\n"
      "v_mfma_f32_16x16x32_bf16 v[0:3], a[160:163], v[144:147], 0\n"
      "v_mul_f32 v44, v44, v219\n"
      "v_accvgpr_write_b32 a116, v88\n"
      "v_mfma_f32_16x16x32_bf16 v[48:51], a[160:163], v[152:155], 0\n"
      "v_mul_f32 v45, v45, v219\n"
      "v_cvt_pk_bf16_f32 v136, v136, v137\n"
      "v_mul_f32 v46, v46, v219\n"
      "v_cvt_pk_bf16_f32 v137, v138, v139\n"
      "v_mul_f32 v47, v47, v219\n"
      "v_accvgpr_write_b32 a117, v89\n"
      "v_mul_f32 v92, v92, v220\n"
      "v_accvgpr_write_b32 a140, v136\n"
      "v_mul_f32 v93, v93, v220\n"
      "v_cvt_pk_bf16_f32 v44, v44, v45\n"
      "v_mul_f32 v94, v94, v220\n"
      "v_cvt_pk_bf16_f32 v45, v46, v47\n"
      "v_mul_f32 v95, v95, v220\n"
      "v_accvgpr_write_b32 a141, v137\n"
      "v_mul_f32 v140, v140, v221\n"
      "v_accvgpr_write_b32 a94, v44\n"
      "v_mul_f32 v141, v141, v221\n"
      "v_cvt_pk_bf16_f32 v92, v92, v93\n"
      "v_mul_f32 v142, v142, v221\n"
      "v_cvt_pk_bf16_f32 v93, v94, v95\n"
      "v_mul_f32 v143, v143, v221\n"
      "v_accvgpr_write_b32 a95, v45\n"
      "v_mfma_f32_16x16x32_bf16 v[96:99], a[160:163], v[160:163], 0\n"
      "ds_read_b64_tr_b16 a[180:181], %[vb1] offset:24704\n"
      "ds_read_b64_tr_b16 a[182:183], %[vb1] offset:30848\n"
      "v_mfma_f32_16x16x32_bf16 v[4:7], a[164:167], v[144:147], 0\n"
      "s_add_u32 m0, %[m0b], 25600\n"
      "ds_read_b64_tr_b16 a[184:185], %[vb2] offset:24704\n"
      "ds_read_b64_tr_b16 a[186:187], %[vb2] offset:30848\n"
      "v_mfma_f32_16x16x32_bf16 v[52:55], a[164:167], v[152:155], 0\n"
      "ds_read_b64_tr_b16 a[188:189], %[vb3] offset:24704\n"
      "ds_read_b64_tr_b16 a[190:191], %[vb3] offset:30848\n"
      "v_mfma_f32_16x16x32_bf16 v[100:103], a[164:167], v[160:163], 0\n"
      "ds_read_b64_tr_b16 a[192:193], %[vb0] offset:24832\n"
      "ds_read_b64_tr_b16 a[194:195], %[vb0] offset:30976\n"
      "v_mfma_f32_16x16x32_bf16 v[8:11], a[168:171], v[144:147], 0\n"
      "ds_read_b64_tr_b16 a[196:197], %[vb1] offset:24832\n"
      "ds_read_b64_tr_b16 a[198:199], %[vb1] offset:30976\n"
      "v_mfma_f32_16x16x32_bf16 v[56:59], a[168:171], v[152:155], 0\n"
      "buffer_load_dwordx4 %[vk1], s[56:59], s70 offen nt lds\n"
      "v_accvgpr_write_b32 a118, v92\n"
      "v_mfma_f32_16x16x32_bf16 v[104:107], a[168:171], v[160:163], 0\n"
      "s_waitcnt lgkmcnt(10)\n"
      "ds_read_b64_tr_b16 a[200:201], %[vb2] offset:24832\n"
      "ds_read_b64_tr_b16 a[202:203], %[vb2] offset:30976\n"
      "v_mfma_f32_16x16x32_bf16 v[12:15], a[172:175], v[144:147], 0\n"
      "ds_read_b64_tr_b16 a[204:205], %[vb3] offset:24832\n"
      "ds_read_b64_tr_b16 a[206:207], %[vb3] offset:30976\n"
      "v_mfma_f32_16x16x32_bf16 v[60:63], a[172:175], v[152:155], 0\n"
      "ds_read_b64_tr_b16 a[144:145], %[vb0] offset:36864\n"
      "ds_read_b64_tr_b16 a[146:147], %[vb0] offset:43008\n"
      "v_mfma_f32_16x16x32_bf16 v[108:111], a[172:175], v[160:163], 0\n"
      "ds_read_b64_tr_b16 a[148:149], %[vb1] offset:36864\n"
      "ds_read_b64_tr_b16 a[150:151], %[vb1] offset:43008\n"
      "v_mfma_f32_16x16x32_bf16 v[16:19], a[176:179], v[144:147], 0\n"
      "v_cvt_pk_bf16_f32 v140, v140, v141\n"
      "v_cvt_pk_bf16_f32 v141, v142, v143\n"
      "v_mfma_f32_16x16x32_bf16 v[64:67], a[176:179], v[152:155], 0\n"
      "s_add_u32 m0, %[m0b], 26624\n"
      "v_accvgpr_write_b32 a119, v93\n"
      "v_accvgpr_write_b32 a142, v140\n"
      "v_mfma_f32_16x16x32_bf16 v[112:115], a[176:179], v[160:163], 0\n"
      "s_waitcnt lgkmcnt(10)\n"
      "buffer_load_dwordx4 %[vk2], s[56:59], s70 offen nt lds\n"
      "v_accvgpr_write_b32 a143, v141\n"
      "v_mfma_f32_16x16x32_bf16 v[20:23], a[180:183], v[144:147], 0\n"
      "ds_read_b64_tr_b16 a[152:153], %[vb2] offset:36864\n"
      "ds_read_b64_tr_b16 a[154:155], %[vb2] offset:43008\n"
      "v_mfma_f32_16x16x32_bf16 v[68:71], a[180:183], v[152:155], 0\n"
      "ds_write_b64 %[ow], a[72:73] offset:0\n"
      "v_fma_f32 v168, v168, %[csc], v216\n"
      "v_mfma_f32_16x16x32_bf16 v[116:119], a[180:183], v[160:163], 0\n"
      "ds_write_b64 %[ow], a[74:75] offset:32\n"
      "v_fma_f32 v169, v169, %[csc], v216\n"
      "v_mfma_f32_16x16x32_bf16 v[24:27], a[184:187], v[144:147], 0\n"
      "ds_write_b64 %[ow], a[76:77] offset:64\n"
      "v_mfma_f32_16x16x32_bf16 v[72:75], a[184:187], v[152:155], 0\n"
      "ds_write_b64 %[ow], a[78:79] offset:96\n"
      "v_mfma_f32_16x16x32_bf16 v[120:123], a[184:187], v[160:163], 0\n"
      "v_exp_f32 v168, v168\n"
      "v_mfma_f32_16x16x32_bf16 v[28:31], a[188:191], v[144:147], 0\n"
      "ds_read_b128 a[224:227], %[orb] offset:0\n"
      "v_fma_f32 v170, v170, %[csc], v216\n"
      "v_mfma_f32_16x16x32_bf16 v[76:79], a[188:191], v[152:155], 0\n"
      "v_exp_f32 v169, v169\n"
      "v_mfma_f32_16x16x32_bf16 v[124:127], a[188:191], v[160:163], 0\n"
      "ds_read_b128 a[228:231], %[orb] offset:1152\n"
      "v_fma_f32 v171, v171, %[csc], v216\n"
      "v_mfma_f32_16x16x32_bf16 v[32:35], a[192:195], v[144:147], 0\n"
      "s_waitcnt lgkmcnt(1)\n"
      "v_exp_f32 v170, v170\n"
      "v_mfma_f32_16x16x32_bf16 v[80:83], a[192:195], v[152:155], 0\n"
      "s_add_u32 s73, s69, 0\n"
      "buffer_store_dwordx4 a[224:227], %[ost], s[52:55], s73 offen offset:0 sc1\n"
      "v_fma_f32 v172, v172, %[csc], v216\n"
      "v_mfma_f32_16x16x32_bf16 v[128:131], a[192:195], v[160:163], 0\n"
      "v_exp_f32 v171, v171\n"
      "v_mfma_f32_16x16x32_bf16 v[36:39], a[196:199], v[144:147], 0\n"
      "v_cvt_pk_bf16_f32 v168, v168, v169\n"
      "v_fma_f32 v173, v173, %[csc], v216\n"
      "v_mfma_f32_16x16x32_bf16 v[84:87], a[196:199], v[152:155], 0\n"
      "s_waitcnt lgkmcnt(0)\n"
      "v_exp_f32 v172, v172\n"
      "v_mfma_f32_16x16x32_bf16 v[132:135], a[196:199], v[160:163], 0\n"
      "s_add_u32 s73, s69, 3072\n"
      "buffer_store_dwordx4 a[228:231], %[ost], s[52:55], s73 offen offset:0 sc1\n"
      "v_fma_f32 v174, v174, %[csc], v216\n"
      "v_mfma_f32_16x16x32_bf16 v[40:43], a[200:203], v[144:147], 0\n"
      "v_exp_f32 v173, v173\n"
      "v_mfma_f32_16x16x32_bf16 v[88:91], a[200:203], v[152:155], 0\n"
      "v_cvt_pk_bf16_f32 v169, v170, v171\n"
      "v_fma_f32 v175, v175, %[csc], v216\n"
      "v_mfma_f32_16x16x32_bf16 v[136:139], a[200:203], v[160:163], 0\n"
      "v_exp_f32 v174, v174\n"
      "v_mfma_f32_16x16x32_bf16 v[44:47], a[204:207], v[144:147], 0\n"
      "v_fma_f32 v176, v176, %[csc], v217\n"
      "v_mfma_f32_16x16x32_bf16 v[92:95], a[204:207], v[152:155], 0\n"
      "v_exp_f32 v175, v175\n"
      "v_mfma_f32_16x16x32_bf16 v[140:143], a[204:207], v[160:163], 0\n"
      "v_cvt_pk_bf16_f32 v170, v172, v173\n"
      "v_fma_f32 v177, v177, %[csc], v217\n"
      "v_exp_f32 v176, v176\n"
      "v_fma_f32 v178, v178, %[csc], v217\n"
      "v_exp_f32 v177, v177\n"
      "v_cvt_pk_bf16_f32 v171, v174, v175\n"
      "v_fma_f32 v179, v179, %[csc], v217\n"
      "v_exp_f32 v178, v178\n"
      "v_fma_f32 v180, v180, %[csc], v217\n"
      "v_exp_f32 v179, v179\n"
      "v_cvt_pk_bf16_f32 v176, v176, v177\n"
      "v_fma_f32 v181, v181, %[csc], v217\n"
      "v_exp_f32 v180, v180\n"
      "v_fma_f32 v182, v182, %[csc], v217\n"
      "v_exp_f32 v181, v181\n"
      "v_cvt_pk_bf16_f32 v177, v178, v179\n"
      "v_fma_f32 v183, v183, %[csc], v217\n"
      "v_exp_f32 v182, v182\n"
      "v_fma_f32 v184, v184, %[csc], v218\n"
      "v_exp_f32 v183, v183\n"
      "v_cvt_pk_bf16_f32 v178, v180, v181\n"
      "v_fma_f32 v185, v185, %[csc], v218\n"
      "v_exp_f32 v184, v184\n"
      "v_fma_f32 v186, v186, %[csc], v218\n"
      "v_exp_f32 v185, v185\n"
      "v_cvt_pk_bf16_f32 v179, v182, v183\n"
      "v_fma_f32 v187, v187, %[csc], v218\n"
      "v_exp_f32 v186, v186\n"
      "v_fma_f32 v188, v188, %[csc], v218\n"
      "v_exp_f32 v187, v187\n"
      "v_cvt_pk_bf16_f32 v184, v184, v185\n"
      "v_fma_f32 v189, v189, %[csc], v218\n"
      "v_exp_f32 v188, v188\n"
      "v_fma_f32 v190, v190, %[csc], v218\n"
      "v_exp_f32 v189, v189\n"
      "v_cvt_pk_bf16_f32 v185, v186, v187\n"
      "v_fma_f32 v191, v191, %[csc], v218\n"
      "v_exp_f32 v190, v190\n"
      "v_exp_f32 v191, v191\n"
      "v_cvt_pk_bf16_f32 v186, v188, v189\n"
      "v_cvt_pk_bf16_f32 v187, v190, v191\n"
      "v_mfma_f32_16x16x32_bf16 a[208:211], a[220:223], v[168:171], a[208:211]\n"
      "ds_read_b64_tr_b16 a[156:157], %[vb3] offset:36864\n"
      "ds_read_b64_tr_b16 a[158:159], %[vb3] offset:43008\n"
      "v_mfma_f32_16x16x32_bf16 a[212:215], a[220:223], v[176:179], a[212:215]\n"
      "ds_read_b64_tr_b16 a[160:161], %[vb0] offset:36992\n"
      "ds_read_b64_tr_b16 a[162:163], %[vb0] offset:43136\n"
      "v_mfma_f32_16x16x32_bf16 a[216:219], a[220:223], v[184:187], a[216:219]\n"
      "ds_read_b64_tr_b16 a[164:165], %[vb1] offset:36992\n"
      "ds_read_b64_tr_b16 a[166:167], %[vb1] offset:43136\n"
      "v_mfma_f32_16x16x32_bf16 v[0:3], a[144:147], v[168:171], v[0:3]\n"
      "ds_read_b64_tr_b16 a[168:169], %[vb2] offset:36992\n"
      "ds_read_b64_tr_b16 a[170:171], %[vb2] offset:43136\n"
      "v_mfma_f32_16x16x32_bf16 v[48:51], a[144:147], v[176:179], v[48:51]\n"
      "s_waitcnt vmcnt(24)\n"
      "ds_read_b64_tr_b16 a[172:173], %[vb3] offset:36992\n"
      "ds_read_b64_tr_b16 a[174:175], %[vb3] offset:43136\n"
      "v_mfma_f32_16x16x32_bf16 v[96:99], a[144:147], v[184:187], v[96:99]\n"
      "ds_read_b64_tr_b16 a[176:177], %[vb0] offset:37120\n"
      "ds_read_b64_tr_b16 a[178:179], %[vb0] offset:43264\n"
      "v_mfma_f32_16x16x32_bf16 v[4:7], a[148:151], v[168:171], v[4:7]\n"
      "ds_read_b64_tr_b16 a[180:181], %[vb1] offset:37120\n"
      "ds_read_b64_tr_b16 a[182:183], %[vb1] offset:43264\n"
      "v_mfma_f32_16x16x32_bf16 v[52:55], a[148:151], v[176:179], v[52:55]\n"
      "ds_read_b64_tr_b16 a[184:185], %[vb2] offset:37120\n"
      "ds_read_b64_tr_b16 a[186:187], %[vb2] offset:43264\n"
      "v_mfma_f32_16x16x32_bf16 v[100:103], a[148:151], v[184:187], v[100:103]\n"
      "ds_read_b64_tr_b16 a[188:189], %[vb3] offset:37120\n"
      "ds_read_b64_tr_b16 a[190:191], %[vb3] offset:43264\n"
      "v_mfma_f32_16x16x32_bf16 v[8:11], a[152:155], v[168:171], v[8:11]\n"
      "ds_read_b128 a[72:75], %[qs0] offset:0\n"
      "ds_read_b128 a[76:79], %[qs1] offset:0\n"
      "v_mfma_f32_16x16x32_bf16 v[56:59], a[152:155], v[176:179], v[56:59]\n"
      "v_fma_f32 v192, v192, %[csc], v216\n"
      "v_fma_f32 v193, v193, %[csc], v216\n"
      "v_mfma_f32_16x16x32_bf16 v[104:107], a[152:155], v[184:187], v[104:107]\n"
      "s_waitcnt lgkmcnt(14)\n"
      "v_exp_f32 v192, v192\n"
      "v_mfma_f32_16x16x32_bf16 v[12:15], a[156:159], v[168:171], v[12:15]\n"
      "v_fma_f32 v194, v194, %[csc], v216\n"
      "v_mfma_f32_16x16x32_bf16 v[60:63], a[156:159], v[176:179], v[60:63]\n"
      "v_exp_f32 v193, v193\n"
      "s_waitcnt vmcnt(18) lgkmcnt(0)\n"
      "s_barrier\n"
      "v_mfma_f32_16x16x32_bf16 v[108:111], a[156:159], v[184:187], v[108:111]\n"
      "s_add_u32 s71, s68, 18923520\n"
      "ds_read_b128 a[192:195], %[kb01] offset:0\n"
      "ds_read_b128 a[196:199], %[kb01] offset:6144\n"
      "v_mfma_f32_16x16x32_bf16 v[16:19], a[160:163], v[168:171], v[16:19]\n"
      "s_add_u32 m0, %[m0b], 98304\n"
      "ds_read_b128 a[200:203], %[kb11] offset:0\n"
      "ds_read_b128 a[204:207], %[kb11] offset:6144\n"
      "v_mfma_f32_16x16x32_bf16 v[64:67], a[160:163], v[176:179], v[64:67]\n"
      "buffer_load_dwordx4 %[vv0], s[60:63], s71 offen nt lds\n"
      "ds_read_b128 a[144:147], %[kb01] offset:128\n"
      "v_mfma_f32_16x16x32_bf16 v[112:115], a[160:163], v[184:187], v[112:115]\n"
      "ds_read_b128 a[148:151], %[kb01] offset:6272\n"
      "ds_read_b128 a[152:155], %[kb11] offset:128\n"
      "v_mfma_f32_16x16x32_bf16 v[20:23], a[164:167], v[168:171], v[20:23]\n"
      "ds_read_b128 a[156:159], %[kb11] offset:6272\n"
      "ds_read_b128 a[160:163], %[kb01] offset:256\n"
      "v_mfma_f32_16x16x32_bf16 v[68:71], a[164:167], v[176:179], v[68:71]\n"
      "v_fma_f32 v195, v195, %[csc], v216\n"
      "v_mfma_f32_16x16x32_bf16 v[116:119], a[164:167], v[184:187], v[116:119]\n"
      "ds_read_b128 a[164:167], %[kb01] offset:6400\n"
      "v_mfma_f32_16x16x32_bf16 v[24:27], a[168:171], v[168:171], v[24:27]\n"
      "v_exp_f32 v194, v194\n"
      "v_mfma_f32_16x16x32_bf16 v[72:75], a[168:171], v[176:179], v[72:75]\n"
      "v_fma_f32 v196, v196, %[csc], v216\n"
      "v_mfma_f32_16x16x32_bf16 v[120:123], a[168:171], v[184:187], v[120:123]\n"
      "ds_read_b128 a[168:171], %[kb11] offset:256\n"
      "v_mfma_f32_16x16x32_bf16 v[28:31], a[172:175], v[168:171], v[28:31]\n"
      "s_add_u32 m0, %[m0b], 99328\n"
      "v_exp_f32 v195, v195\n"
      "v_mfma_f32_16x16x32_bf16 v[76:79], a[172:175], v[176:179], v[76:79]\n"
      "buffer_load_dwordx4 %[vv1], s[60:63], s71 offen nt lds\n"
      "v_cvt_pk_bf16_f32 v192, v192, v193\n"
      "v_mfma_f32_16x16x32_bf16 v[124:127], a[172:175], v[184:187], v[124:127]\n"
      "ds_read_b128 a[172:175], %[kb11] offset:6400\n"
      "v_fma_f32 v197, v197, %[csc], v216\n"
      "v_mfma_f32_16x16x32_bf16 v[32:35], a[176:179], v[168:171], v[32:35]\n"
      "v_exp_f32 v196, v196\n"
      "v_mfma_f32_16x16x32_bf16 v[80:83], a[176:179], v[176:179], v[80:83]\n"
      "v_fma_f32 v198, v198, %[csc], v216\n"
      "v_mfma_f32_16x16x32_bf16 v[128:131], a[176:179], v[184:187], v[128:131]\n"
      "ds_read_b64_tr_b16 a[176:177], %[vb0] offset:0\n"
      "ds_read_b64_tr_b16 a[178:179], %[vb0] offset:6144\n"
      "v_mfma_f32_16x16x32_bf16 v[36:39], a[180:183], v[168:171], v[36:39]\n"
      "s_add_u32 s72, s69, 37748864\n"
      "v_exp_f32 v197, v197\n"
      "v_mfma_f32_16x16x32_bf16 v[84:87], a[180:183], v[176:179], v[84:87]\n"
      "s_add_u32 m0, %[m0q], 0\n"
      "v_cvt_pk_bf16_f32 v193, v194, v195\n"
      "v_fma_f32 v199, v199, %[csc], v216\n"
      "v_mfma_f32_16x16x32_bf16 v[132:135], a[180:183], v[184:187], v[132:135]\n"
      "buffer_load_dwordx4 %[vq0], s[80:83], s72 offen nt lds\n"
      "v_mfma_f32_16x16x32_bf16 v[40:43], a[184:187], v[168:171], v[40:43]\n"
      "s_add_u32 m0, %[m0q], 1024\n"
      "ds_read_b64_tr_b16 a[180:181], %[vb1] offset:0\n"
      "ds_read_b64_tr_b16 a[182:183], %[vb1] offset:6144\n"
      "v_mfma_f32_16x16x32_bf16 v[88:91], a[184:187], v[176:179], v[88:91]\n"
      "buffer_load_dwordx4 %[vq1], s[80:83], s72 offen nt lds\n"
      "v_mfma_f32_16x16x32_bf16 v[136:139], a[184:187], v[184:187], v[136:139]\n"
      "s_add_u32 m0, %[m0b], 100352\n"
      "ds_read_b64_tr_b16 a[184:185], %[vb2] offset:0\n"
      "ds_read_b64_tr_b16 a[186:187], %[vb2] offset:6144\n"
      "v_mfma_f32_16x16x32_bf16 v[44:47], a[188:191], v[168:171], v[44:47]\n"
      "buffer_load_dwordx4 %[vv2], s[60:63], s71 offen nt lds\n"
      "v_mfma_f32_16x16x32_bf16 v[92:95], a[188:191], v[176:179], v[92:95]\n"
      "v_exp_f32 v198, v198\n"
      "v_mfma_f32_16x16x32_bf16 v[140:143], a[188:191], v[184:187], v[140:143]\n"
      "s_waitcnt lgkmcnt(14)\n"
      "v_fma_f32 v200, v200, %[csc], v217\n"
      "s_add_u32 s71, s68, 18935808\n"
      "s_add_u32 m0, %[m0b], 110592\n"
      "s_nop 0\n"
      "buffer_load_dwordx4 %[vv0], s[60:63], s71 offen nt lds\n"
      "s_add_u32 m0, %[m0b], 111616\n"
      "s_nop 0\n"
      "buffer_load_dwordx4 %[vv1], s[60:63], s71 offen nt lds\n"
      "s_add_u32 m0, %[m0b], 112640\n"
      "s_nop 0\n"
      "buffer_load_dwordx4 %[vv2], s[60:63], s71 offen nt lds\n"
      "s_waitcnt lgkmcnt(0)\n"
      "s_add_u32 s68, s68, 18874368\n"
      "s_add_u32 s69, s69, 18874368\n"
      "s_mov_b32 s74, 1\n"
      "s_add_u32 s75, s75, 24\n"
      "s_branch L_loop%=\n"
      "L_tail%=:\n"
      "v_cvt_pk_bf16_f32 v194, v196, v197\n"
      "v_fma_f32 v201, v201, %[csc], v217\n"
      "v_exp_f32 v200, v200\n"
      "v_fma_f32 v202, v202, %[csc], v217\n"
      "v_exp_f32 v201, v201\n"
      "v_cvt_pk_bf16_f32 v195, v198, v199\n"
      "v_fma_f32 v203, v203, %[csc], v217\n"
      "v_exp_f32 v202, v202\n"
      "v_fma_f32 v204, v204, %[csc], v217\n"
      "v_exp_f32 v203, v203\n"
      "v_cvt_pk_bf16_f32 v200, v200, v201\n"
      "v_fma_f32 v205, v205, %[csc], v217\n"
      "v_exp_f32 v204, v204\n"
      "v_fma_f32 v206, v206, %[csc], v217\n"
      "v_exp_f32 v205, v205\n"
      "v_cvt_pk_bf16_f32 v201, v202, v203\n"
      "v_fma_f32 v207, v207, %[csc], v217\n"
      "v_exp_f32 v206, v206\n"
      "v_fma_f32 v208, v208, %[csc], v218\n"
      "v_exp_f32 v207, v207\n"
      "v_cvt_pk_bf16_f32 v202, v204, v205\n"
      "v_fma_f32 v209, v209, %[csc], v218\n"
      "v_exp_f32 v208, v208\n"
      "v_fma_f32 v210, v210, %[csc], v218\n"
      "v_exp_f32 v209, v209\n"
      "v_cvt_pk_bf16_f32 v203, v206, v207\n"
      "v_fma_f32 v211, v211, %[csc], v218\n"
      "v_exp_f32 v210, v210\n"
      "v_fma_f32 v212, v212, %[csc], v218\n"
      "v_exp_f32 v211, v211\n"
      "v_cvt_pk_bf16_f32 v208, v208, v209\n"
      "v_fma_f32 v213, v213, %[csc], v218\n"
      "v_exp_f32 v212, v212\n"
      "v_fma_f32 v214, v214, %[csc], v218\n"
      "v_exp_f32 v213, v213\n"
      "v_cvt_pk_bf16_f32 v209, v210, v211\n"
      "v_fma_f32 v215, v215, %[csc], v218\n"
      "v_exp_f32 v214, v214\n"
      "v_exp_f32 v215, v215\n"
      "v_cvt_pk_bf16_f32 v210, v212, v213\n"
      "v_cvt_pk_bf16_f32 v211, v214, v215\n"
      "v_mfma_f32_16x16x32_bf16 a[208:211], a[220:223], v[192:195], a[208:211]\n"
      "ds_read_b64_tr_b16 a[156:157], %[vb3] offset:12288\n"
      "ds_read_b64_tr_b16 a[158:159], %[vb3] offset:18432\n"
      "v_mfma_f32_16x16x32_bf16 a[212:215], a[220:223], v[200:203], a[212:215]\n"
      "ds_read_b64_tr_b16 a[160:161], %[vb0] offset:12416\n"
      "ds_read_b64_tr_b16 a[162:163], %[vb0] offset:18560\n"
      "v_mfma_f32_16x16x32_bf16 a[216:219], a[220:223], v[208:211], a[216:219]\n"
      "s_waitcnt lgkmcnt(4)\n"
      "ds_read_b64_tr_b16 a[164:165], %[vb1] offset:12416\n"
      "ds_read_b64_tr_b16 a[166:167], %[vb1] offset:18560\n"
      "v_mfma_f32_16x16x32_bf16 v[0:3], a[144:147], v[192:195], v[0:3]\n"
      "ds_read_b64_tr_b16 a[168:169], %[vb2] offset:12416\n"
      "ds_read_b64_tr_b16 a[170:171], %[vb2] offset:18560\n"
      "v_mfma_f32_16x16x32_bf16 v[48:51], a[144:147], v[200:203], v[48:51]\n"
      "ds_read_b64_tr_b16 a[172:173], %[vb3] offset:12416\n"
      "ds_read_b64_tr_b16 a[174:175], %[vb3] offset:18560\n"
      "v_mfma_f32_16x16x32_bf16 v[96:99], a[144:147], v[208:211], v[96:99]\n"
      "ds_read_b64_tr_b16 a[176:177], %[vb0] offset:12544\n"
      "ds_read_b64_tr_b16 a[178:179], %[vb0] offset:18688\n"
      "v_mfma_f32_16x16x32_bf16 v[4:7], a[148:151], v[192:195], v[4:7]\n"
      "ds_read_b64_tr_b16 a[180:181], %[vb1] offset:12544\n"
      "ds_read_b64_tr_b16 a[182:183], %[vb1] offset:18688\n"
      "v_mfma_f32_16x16x32_bf16 v[52:55], a[148:151], v[200:203], v[52:55]\n"
      "ds_read_b64_tr_b16 a[184:185], %[vb2] offset:12544\n"
      "ds_read_b64_tr_b16 a[186:187], %[vb2] offset:18688\n"
      "v_mfma_f32_16x16x32_bf16 v[100:103], a[148:151], v[208:211], v[100:103]\n"
      "ds_read_b64_tr_b16 a[188:189], %[vb3] offset:12544\n"
      "ds_read_b64_tr_b16 a[190:191], %[vb3] offset:18688\n"
      "v_mfma_f32_16x16x32_bf16 v[8:11], a[152:155], v[192:195], v[8:11]\n"
      "v_accvgpr_read_b32 v224, a208\n"
      "v_cmp_nge_f32 vcc, 0x47800000, v224\n"
      "v_mfma_f32_16x16x32_bf16 v[56:59], a[152:155], v[200:203], v[56:59]\n"
      "v_rcp_f32 v219, v224\n"
      "v_mfma_f32_16x16x32_bf16 v[104:107], a[152:155], v[208:211], v[104:107]\n"
      "s_waitcnt lgkmcnt(12)\n"
      "v_mfma_f32_16x16x32_bf16 v[12:15], a[156:159], v[192:195], v[12:15]\n"
      "s_or_b64 %[flagB], %[flagB], vcc\n"
      "v_accvgpr_read_b32 v224, a212\n"
      "v_cmp_nge_f32 vcc, 0x47800000, v224\n"
      "v_mfma_f32_16x16x32_bf16 v[60:63], a[156:159], v[200:203], v[60:63]\n"
      "v_rcp_f32 v220, v224\n"
      "v_mfma_f32_16x16x32_bf16 v[108:111], a[156:159], v[208:211], v[108:111]\n"
      "s_nop 0\n"
      "s_or_b64 %[flagB], %[flagB], vcc\n"
      "v_accvgpr_read_b32 v224, a216\n"
      "v_cmp_nge_f32 vcc, 0x47800000, v224\n"
      "v_mfma_f32_16x16x32_bf16 v[16:19], a[160:163], v[192:195], v[16:19]\n"
      "v_rcp_f32 v221, v224\n"
      "v_mfma_f32_16x16x32_bf16 v[64:67], a[160:163], v[200:203], v[64:67]\n"
      "s_nop 0\n"
      "s_or_b64 %[flagB], %[flagB], vcc\n"
      "v_mul_f32 v0, v0, v219\n"
      "v_mul_f32 v1, v1, v219\n"
      "v_mfma_f32_16x16x32_bf16 v[112:115], a[160:163], v[208:211], v[112:115]\n"
      "v_mul_f32 v2, v2, v219\n"
      "v_mul_f32 v3, v3, v219\n"
      "v_mfma_f32_16x16x32_bf16 v[20:23], a[164:167], v[192:195], v[20:23]\n"
      "v_mul_f32 v4, v4, v219\n"
      "v_mul_f32 v5, v5, v219\n"
      "v_mfma_f32_16x16x32_bf16 v[68:71], a[164:167], v[200:203], v[68:71]\n"
      "v_cvt_pk_bf16_f32 v0, v0, v1\n"
      "v_mul_f32 v6, v6, v219\n"
      "v_mfma_f32_16x16x32_bf16 v[116:119], a[164:167], v[208:211], v[116:119]\n"
      "s_waitcnt lgkmcnt(4)\n"
      "v_cvt_pk_bf16_f32 v1, v2, v3\n"
      "v_mul_f32 v7, v7, v219\n"
      "v_mfma_f32_16x16x32_bf16 v[24:27], a[168:171], v[192:195], v[24:27]\n"
      "v_mul_f32 v8, v8, v219\n"
      "v_mul_f32 v9, v9, v219\n"
      "v_mfma_f32_16x16x32_bf16 v[72:75], a[168:171], v[200:203], v[72:75]\n"
      "v_cvt_pk_bf16_f32 v4, v4, v5\n"
      "v_mul_f32 v10, v10, v219\n"
      "v_mfma_f32_16x16x32_bf16 v[120:123], a[168:171], v[208:211], v[120:123]\n"
      "v_cvt_pk_bf16_f32 v5, v6, v7\n"
      "v_mul_f32 v11, v11, v219\n"
      "v_mfma_f32_16x16x32_bf16 v[28:31], a[172:175], v[192:195], v[28:31]\n"
      "ds_write_b64 %[ow], v[0:1] offset:0\n"
      "v_mul_f32 v12, v12, v219\n"
      "v_mfma_f32_16x16x32_bf16 v[76:79], a[172:175], v[200:203], v[76:79]\n"
      "v_mul_f32 v13, v13, v219\n"
      "v_cvt_pk_bf16_f32 v8, v8, v9\n"
      "v_mfma_f32_16x16x32_bf16 v[124:127], a[172:175], v[208:211], v[124:127]\n"
      "v_mul_f32 v14, v14, v219\n"
      "v_cvt_pk_bf16_f32 v9, v10, v11\n"
      "v_mfma_f32_16x16x32_bf16 v[32:35], a[176:179], v[192:195], v[32:35]\n"
      "v_mul_f32 v15, v15, v219\n"
      "v_mfma_f32_16x16x32_bf16 v[80:83], a[176:179], v[200:203], v[80:83]\n"
      "ds_write_b64 %[ow], v[4:5] offset:32\n"
      "v_cvt_pk_bf16_f32 v12, v12, v13\n"
      "v_mfma_f32_16x16x32_bf16 v[128:131], a[176:179], v[208:211], v[128:131]\n"
      "v_cvt_pk_bf16_f32 v13, v14, v15\n"
      "v_mfma_f32_16x16x32_bf16 v[36:39], a[180:183], v[192:195], v[36:39]\n"
      "ds_write_b64 %[ow], v[8:9] offset:64\n"
      "v_mfma_f32_16x16x32_bf16 v[84:87], a[180:183], v[200:203], v[84:87]\n"
      "ds_write_b64 %[ow], v[12:13] offset:96\n"
      "ds_read_b128 a[224:227], %[orb] offset:0\n"
      "v_mfma_f32_16x16x32_bf16 v[132:135], a[180:183], v[208:211], v[132:135]\n"
      "s_waitcnt lgkmcnt(5)\n"
      "ds_read_b128 a[228:231], %[orb] offset:1152\n"
      "v_mul_f32 v48, v48, v220\n"
      "v_mfma_f32_16x16x32_bf16 v[40:43], a[184:187], v[192:195], v[40:43]\n"
      "v_mul_f32 v49, v49, v220\n"
      "v_mul_f32 v50, v50, v220\n"
      "v_mfma_f32_16x16x32_bf16 v[88:91], a[184:187], v[200:203], v[88:91]\n"
      "v_mul_f32 v51, v51, v220\n"
      "v_mul_f32 v52, v52, v220\n"
      "v_mfma_f32_16x16x32_bf16 v[136:139], a[184:187], v[208:211], v[136:139]\n"
      "v_mul_f32 v53, v53, v220\n"
      "v_cvt_pk_bf16_f32 v48, v48, v49\n"
      "v_mfma_f32_16x16x32_bf16 v[44:47], a[188:191], v[192:195], v[44:47]\n"
      "v_mul_f32 v54, v54, v220\n"
      "v_cvt_pk_bf16_f32 v49, v50, v51\n"
      "v_mfma_f32_16x16x32_bf16 v[92:95], a[188:191], v[200:203], v[92:95]\n"
      "v_mul_f32 v55, v55, v220\n"
      "v_mul_f32 v56, v56, v220\n"
      "v_mfma_f32_16x16x32_bf16 v[140:143], a[188:191], v[208:211], v[140:143]\n"
      "v_mul_f32 v57, v57, v220\n"
      "v_cvt_pk_bf16_f32 v52, v52, v53\n"
      "s_nop 7\n"
      "ds_write_b64 %[ow], v[48:49] offset:0\n"
      "v_mul_f32 v58, v58, v220\n"
      "v_cvt_pk_bf16_f32 v53, v54, v55\n"
      "ds_write_b64 %[ow], v[52:53] offset:32\n"
      "v_mul_f32 v59, v59, v220\n"
      "v_mul_f32 v60, v60, v220\n"
      "v_mul_f32 v61, v61, v220\n"
      "v_cvt_pk_bf16_f32 v56, v56, v57\n"
      "v_mul_f32 v62, v62, v220\n"
      "v_cvt_pk_bf16_f32 v57, v58, v59\n"
      "v_mul_f32 v63, v63, v220\n"
      "s_waitcnt lgkmcnt(3)\n"
      "s_add_u32 s73, s69, 0\n"
      "buffer_store_dwordx4 a[224:227], %[ost], s[52:55], s73 offen offset:0 sc1\n"
      "s_waitcnt lgkmcnt(2)\n"
      "s_add_u32 s73, s69, 3072\n"
      "buffer_store_dwordx4 a[228:231], %[ost], s[52:55], s73 offen offset:0 sc1\n"
      "ds_write_b64 %[ow], v[56:57] offset:64\n"
      "v_cvt_pk_bf16_f32 v60, v60, v61\n"
      "v_cvt_pk_bf16_f32 v61, v62, v63\n"
      "ds_write_b64 %[ow], v[60:61] offset:96\n"
      "ds_read_b128 a[232:235], %[orb] offset:0\n"
      "ds_read_b128 a[236:239], %[orb] offset:1152\n"
      "v_mul_f32 v96, v96, v221\n"
      "v_mul_f32 v97, v97, v221\n"
      "v_mul_f32 v98, v98, v221\n"
      "v_mul_f32 v99, v99, v221\n"
      "v_mul_f32 v100, v100, v221\n"
      "v_mul_f32 v101, v101, v221\n"
      "v_cvt_pk_bf16_f32 v96, v96, v97\n"
      "v_mul_f32 v102, v102, v221\n"
      "v_cvt_pk_bf16_f32 v97, v98, v99\n"
      "ds_write_b64 %[ow], v[96:97] offset:0\n"
      "v_mul_f32 v103, v103, v221\n"
      "v_mul_f32 v104, v104, v221\n"
      "v_mul_f32 v105, v105, v221\n"
      "v_cvt_pk_bf16_f32 v100, v100, v101\n"
      "v_mul_f32 v106, v106, v221\n"
      "v_cvt_pk_bf16_f32 v101, v102, v103\n"
      "ds_write_b64 %[ow], v[100:101] offset:32\n"
      "v_mul_f32 v107, v107, v221\n"
      "v_mul_f32 v108, v108, v221\n"
      "v_mul_f32 v109, v109, v221\n"
      "v_cvt_pk_bf16_f32 v104, v104, v105\n"
      "v_mul_f32 v110, v110, v221\n"
      "v_cvt_pk_bf16_f32 v105, v106, v107\n"
      "v_mul_f32 v111, v111, v221\n"
      "s_waitcnt lgkmcnt(3)\n"
      "s_add_u32 s73, s69, 6144\n"
      "buffer_store_dwordx4 a[232:235], %[ost], s[52:55], s73 offen offset:0 sc1\n"
      "s_waitcnt lgkmcnt(2)\n"
      "s_add_u32 s73, s69, 9216\n"
      "buffer_store_dwordx4 a[236:239], %[ost], s[52:55], s73 offen offset:0 sc1\n"
      "ds_write_b64 %[ow], v[104:105] offset:64\n"
      "v_cvt_pk_bf16_f32 v108, v108, v109\n"
      "v_cvt_pk_bf16_f32 v109, v110, v111\n"
      "ds_write_b64 %[ow], v[108:109] offset:96\n"
      "ds_read_b128 a[224:227], %[orb] offset:0\n"
      "ds_read_b128 a[228:231], %[orb] offset:1152\n"
      "v_mul_f32 v16, v16, v219\n"
      "v_mul_f32 v17, v17, v219\n"
      "v_mul_f32 v18, v18, v219\n"
      "v_mul_f32 v19, v19, v219\n"
      "v_mul_f32 v20, v20, v219\n"
      "v_mul_f32 v21, v21, v219\n"
      "v_cvt_pk_bf16_f32 v16, v16, v17\n"
      "v_mul_f32 v22, v22, v219\n"
      "v_cvt_pk_bf16_f32 v17, v18, v19\n"
      "ds_write_b64 %[ow], v[16:17] offset:0\n"
      "v_mul_f32 v23, v23, v219\n"
      "v_mul_f32 v24, v24, v219\n"
      "v_mul_f32 v25, v25, v219\n"
      "v_cvt_pk_bf16_f32 v20, v20, v21\n"
      "v_mul_f32 v26, v26, v219\n"
      "v_cvt_pk_bf16_f32 v21, v22, v23\n"
      "ds_write_b64 %[ow], v[20:21] offset:32\n"
      "v_mul_f32 v27, v27, v219\n"
      "v_mul_f32 v28, v28, v219\n"
      "v_mul_f32 v29, v29, v219\n"
      "v_cvt_pk_bf16_f32 v24, v24, v25\n"
      "v_mul_f32 v30, v30, v219\n"
      "v_cvt_pk_bf16_f32 v25, v26, v27\n"
      "v_mul_f32 v31, v31, v219\n"
      "s_waitcnt lgkmcnt(3)\n"
      "s_add_u32 s73, s69, 12288\n"
      "buffer_store_dwordx4 a[224:227], %[ost], s[52:55], s73 offen offset:0 sc1\n"
      "s_waitcnt lgkmcnt(2)\n"
      "s_add_u32 s73, s69, 15360\n"
      "buffer_store_dwordx4 a[228:231], %[ost], s[52:55], s73 offen offset:0 sc1\n"
      "ds_write_b64 %[ow], v[24:25] offset:64\n"
      "v_cvt_pk_bf16_f32 v28, v28, v29\n"
      "v_cvt_pk_bf16_f32 v29, v30, v31\n"
      "ds_write_b64 %[ow], v[28:29] offset:96\n"
      "ds_read_b128 a[232:235], %[orb] offset:0\n"
      "ds_read_b128 a[236:239], %[orb] offset:1152\n"
      "v_mul_f32 v64, v64, v220\n"
      "v_mul_f32 v65, v65, v220\n"
      "v_mul_f32 v66, v66, v220\n"
      "v_mul_f32 v67, v67, v220\n"
      "v_mul_f32 v68, v68, v220\n"
      "v_mul_f32 v69, v69, v220\n"
      "v_cvt_pk_bf16_f32 v64, v64, v65\n"
      "v_mul_f32 v70, v70, v220\n"
      "v_cvt_pk_bf16_f32 v65, v66, v67\n"
      "ds_write_b64 %[ow], v[64:65] offset:0\n"
      "v_mul_f32 v71, v71, v220\n"
      "v_mul_f32 v72, v72, v220\n"
      "v_mul_f32 v73, v73, v220\n"
      "v_cvt_pk_bf16_f32 v68, v68, v69\n"
      "v_mul_f32 v74, v74, v220\n"
      "v_cvt_pk_bf16_f32 v69, v70, v71\n"
      "ds_write_b64 %[ow], v[68:69] offset:32\n"
      "v_mul_f32 v75, v75, v220\n"
      "v_mul_f32 v76, v76, v220\n"
      "v_mul_f32 v77, v77, v220\n"
      "v_cvt_pk_bf16_f32 v72, v72, v73\n"
      "v_mul_f32 v78, v78, v220\n"
      "v_cvt_pk_bf16_f32 v73, v74, v75\n"
      "v_mul_f32 v79, v79, v220\n"
      "s_waitcnt lgkmcnt(3)\n"
      "s_add_u32 s73, s69, 0\n"
      "buffer_store_dwordx4 a[232:235], %[ost], s[52:55], s73 offen offset:128 sc1\n"
      "s_waitcnt lgkmcnt(2)\n"
      "s_add_u32 s73, s69, 3072\n"
      "buffer_store_dwordx4 a[236:239], %[ost], s[52:55], s73 offen offset:128 sc1\n"
      "ds_write_b64 %[ow], v[72:73] offset:64\n"
      "v_cvt_pk_bf16_f32 v76, v76, v77\n"
      "v_cvt_pk_bf16_f32 v77, v78, v79\n"
      "ds_write_b64 %[ow], v[76:77] offset:96\n"
      "ds_read_b128 a[224:227], %[orb] offset:0\n"
      "ds_read_b128 a[228:231], %[orb] offset:1152\n"
      "v_mul_f32 v112, v112, v221\n"
      "v_mul_f32 v113, v113, v221\n"
      "v_mul_f32 v114, v114, v221\n"
      "v_mul_f32 v115, v115, v221\n"
      "v_mul_f32 v116, v116, v221\n"
      "v_mul_f32 v117, v117, v221\n"
      "v_cvt_pk_bf16_f32 v112, v112, v113\n"
      "v_mul_f32 v118, v118, v221\n"
      "v_cvt_pk_bf16_f32 v113, v114, v115\n"
      "ds_write_b64 %[ow], v[112:113] offset:0\n"
      "v_mul_f32 v119, v119, v221\n"
      "v_mul_f32 v120, v120, v221\n"
      "v_mul_f32 v121, v121, v221\n"
      "v_cvt_pk_bf16_f32 v116, v116, v117\n"
      "v_mul_f32 v122, v122, v221\n"
      "v_cvt_pk_bf16_f32 v117, v118, v119\n"
      "ds_write_b64 %[ow], v[116:117] offset:32\n"
      "v_mul_f32 v123, v123, v221\n"
      "v_mul_f32 v124, v124, v221\n"
      "v_mul_f32 v125, v125, v221\n"
      "v_cvt_pk_bf16_f32 v120, v120, v121\n"
      "v_mul_f32 v126, v126, v221\n"
      "v_cvt_pk_bf16_f32 v121, v122, v123\n"
      "v_mul_f32 v127, v127, v221\n"
      "s_waitcnt lgkmcnt(3)\n"
      "s_add_u32 s73, s69, 6144\n"
      "buffer_store_dwordx4 a[224:227], %[ost], s[52:55], s73 offen offset:128 sc1\n"
      "s_waitcnt lgkmcnt(2)\n"
      "s_add_u32 s73, s69, 9216\n"
      "buffer_store_dwordx4 a[228:231], %[ost], s[52:55], s73 offen offset:128 sc1\n"
      "ds_write_b64 %[ow], v[120:121] offset:64\n"
      "v_cvt_pk_bf16_f32 v124, v124, v125\n"
      "v_cvt_pk_bf16_f32 v125, v126, v127\n"
      "ds_write_b64 %[ow], v[124:125] offset:96\n"
      "ds_read_b128 a[232:235], %[orb] offset:0\n"
      "ds_read_b128 a[236:239], %[orb] offset:1152\n"
      "v_mul_f32 v32, v32, v219\n"
      "v_mul_f32 v33, v33, v219\n"
      "v_mul_f32 v34, v34, v219\n"
      "v_mul_f32 v35, v35, v219\n"
      "v_mul_f32 v36, v36, v219\n"
      "v_mul_f32 v37, v37, v219\n"
      "v_cvt_pk_bf16_f32 v32, v32, v33\n"
      "v_mul_f32 v38, v38, v219\n"
      "v_cvt_pk_bf16_f32 v33, v34, v35\n"
      "ds_write_b64 %[ow], v[32:33] offset:0\n"
      "v_mul_f32 v39, v39, v219\n"
      "v_mul_f32 v40, v40, v219\n"
      "v_mul_f32 v41, v41, v219\n"
      "v_cvt_pk_bf16_f32 v36, v36, v37\n"
      "v_mul_f32 v42, v42, v219\n"
      "v_cvt_pk_bf16_f32 v37, v38, v39\n"
      "ds_write_b64 %[ow], v[36:37] offset:32\n"
      "v_mul_f32 v43, v43, v219\n"
      "v_mul_f32 v44, v44, v219\n"
      "v_mul_f32 v45, v45, v219\n"
      "v_cvt_pk_bf16_f32 v40, v40, v41\n"
      "v_mul_f32 v46, v46, v219\n"
      "v_cvt_pk_bf16_f32 v41, v42, v43\n"
      "v_mul_f32 v47, v47, v219\n"
      "s_waitcnt lgkmcnt(3)\n"
      "s_add_u32 s73, s69, 12288\n"
      "buffer_store_dwordx4 a[232:235], %[ost], s[52:55], s73 offen offset:128 sc1\n"
      "s_waitcnt lgkmcnt(2)\n"
      "s_add_u32 s73, s69, 15360\n"
      "buffer_store_dwordx4 a[236:239], %[ost], s[52:55], s73 offen offset:128 sc1\n"
      "ds_write_b64 %[ow], v[40:41] offset:64\n"
      "v_cvt_pk_bf16_f32 v44, v44, v45\n"
      "v_cvt_pk_bf16_f32 v45, v46, v47\n"
      "ds_write_b64 %[ow], v[44:45] offset:96\n"
      "ds_read_b128 a[224:227], %[orb] offset:0\n"
      "ds_read_b128 a[228:231], %[orb] offset:1152\n"
      "v_mul_f32 v80, v80, v220\n"
      "v_mul_f32 v81, v81, v220\n"
      "v_mul_f32 v82, v82, v220\n"
      "v_mul_f32 v83, v83, v220\n"
      "v_mul_f32 v84, v84, v220\n"
      "v_mul_f32 v85, v85, v220\n"
      "v_cvt_pk_bf16_f32 v80, v80, v81\n"
      "v_mul_f32 v86, v86, v220\n"
      "v_cvt_pk_bf16_f32 v81, v82, v83\n"
      "ds_write_b64 %[ow], v[80:81] offset:0\n"
      "v_mul_f32 v87, v87, v220\n"
      "v_mul_f32 v88, v88, v220\n"
      "v_mul_f32 v89, v89, v220\n"
      "v_cvt_pk_bf16_f32 v84, v84, v85\n"
      "v_mul_f32 v90, v90, v220\n"
      "v_cvt_pk_bf16_f32 v85, v86, v87\n"
      "ds_write_b64 %[ow], v[84:85] offset:32\n"
      "v_mul_f32 v91, v91, v220\n"
      "v_mul_f32 v92, v92, v220\n"
      "v_mul_f32 v93, v93, v220\n"
      "v_cvt_pk_bf16_f32 v88, v88, v89\n"
      "v_mul_f32 v94, v94, v220\n"
      "v_cvt_pk_bf16_f32 v89, v90, v91\n"
      "v_mul_f32 v95, v95, v220\n"
      "s_waitcnt lgkmcnt(3)\n"
      "s_add_u32 s73, s69, 0\n"
      "buffer_store_dwordx4 a[224:227], %[ost], s[52:55], s73 offen offset:256 sc1\n"
      "s_waitcnt lgkmcnt(2)\n"
      "s_add_u32 s73, s69, 3072\n"
      "buffer_store_dwordx4 a[228:231], %[ost], s[52:55], s73 offen offset:256 sc1\n"
      "ds_write_b64 %[ow], v[88:89] offset:64\n"
      "v_cvt_pk_bf16_f32 v92, v92, v93\n"
      "v_cvt_pk_bf16_f32 v93, v94, v95\n"
      "ds_write_b64 %[ow], v[92:93] offset:96\n"
      "ds_read_b128 a[232:235], %[orb] offset:0\n"
      "ds_read_b128 a[236:239], %[orb] offset:1152\n"
      "v_mul_f32 v128, v128, v221\n"
      "v_mul_f32 v129, v129, v221\n"
      "v_mul_f32 v130, v130, v221\n"
      "v_mul_f32 v131, v131, v221\n"
      "v_mul_f32 v132, v132, v221\n"
      "v_mul_f32 v133, v133, v221\n"
      "v_cvt_pk_bf16_f32 v128, v128, v129\n"
      "v_mul_f32 v134, v134, v221\n"
      "v_cvt_pk_bf16_f32 v129, v130, v131\n"
      "ds_write_b64 %[ow], v[128:129] offset:0\n"
      "v_mul_f32 v135, v135, v221\n"
      "v_mul_f32 v136, v136, v221\n"
      "v_mul_f32 v137, v137, v221\n"
      "v_cvt_pk_bf16_f32 v132, v132, v133\n"
      "v_mul_f32 v138, v138, v221\n"
      "v_cvt_pk_bf16_f32 v133, v134, v135\n"
      "ds_write_b64 %[ow], v[132:133] offset:32\n"
      "v_mul_f32 v139, v139, v221\n"
      "v_mul_f32 v140, v140, v221\n"
      "v_mul_f32 v141, v141, v221\n"
      "v_cvt_pk_bf16_f32 v136, v136, v137\n"
      "v_mul_f32 v142, v142, v221\n"
      "v_cvt_pk_bf16_f32 v137, v138, v139\n"
      "v_mul_f32 v143, v143, v221\n"
      "s_waitcnt lgkmcnt(3)\n"
      "s_add_u32 s73, s69, 6144\n"
      "buffer_store_dwordx4 a[232:235], %[ost], s[52:55], s73 offen offset:256 sc1\n"
      "s_waitcnt lgkmcnt(2)\n"
      "s_add_u32 s73, s69, 9216\n"
      "buffer_store_dwordx4 a[236:239], %[ost], s[52:55], s73 offen offset:256 sc1\n"
      "ds_write_b64 %[ow], v[136:137] offset:64\n"
      "v_cvt_pk_bf16_f32 v140, v140, v141\n"
      "v_cvt_pk_bf16_f32 v141, v142, v143\n"
      "ds_write_b64 %[ow], v[140:141] offset:96\n"
      "ds_read_b128 a[224:227], %[orb] offset:0\n"
      "ds_read_b128 a[228:231], %[orb] offset:1152\n"
      "s_waitcnt lgkmcnt(1)\n"
      "s_add_u32 s73, s69, 12288\n"
      "buffer_store_dwordx4 a[224:227], %[ost], s[52:55], s73 offen offset:256 sc1\n"
      "s_waitcnt lgkmcnt(0)\n"
      "s_add_u32 s73, s69, 15360\n"
      "buffer_store_dwordx4 a[228:231], %[ost], s[52:55], s73 offen offset:256 sc1\n"
      "s_waitcnt vmcnt(0)\n"
      "L_end%=:\n"
      : [flagA] "=&s"(flagA), [flagB] "=&s"(flagB)
      : [kb00] "v"(kb[0][0]), [kb10] "v"(kb[1][0]), [kb01] "v"(kb[0][1]), [kb11] "v"(kb[1][1]),
        [vb0] "v"(vb[0]), [vb1] "v"(vb[1]), [vb2] "v"(vb[2]), [vb3] "v"(vb[3]),
        [vk0] "v"(vk[0]), [vk1] "v"(vk[1]), [vk2] "v"(vk[2]), [vv0] "v"(vv[0]), [vv1] "v"(vv[1]), [vv2] "v"(vv[2]),
        [vq0] "v"(vq[0]), [vq1] "v"(vq[1]),
        [qs0] "v"(qs0), [qs1] "v"(qs1), [ow] "v"(ow), [orb] "v"(orb), [ost] "v"(ost),
        [ka] "s"(__builtin_amdgcn_kernarg_segment_ptr()),  [m0b] "s"(m0b), [m0q] "s"(m0q), [m0p] "s"(m0p), [spq] "s"(spq),
        [hb] "s"(hb), [qb0] "s"(qb0), [csc] "s"(CSC)
      : "memory", "scc", "vcc", "m0", "v0", "v1", "v2", "v3", "v4", "v5", "v6", "v7", "v8", "v9", "v10", "v11", "v12", "v13", "v14", "v15", "v16", "v17", "v18", "v19", "v20", "v21", "v22", "v23", "v24", "v25", "v26", "v27", "v28", "v29", "v30", "v31", "v32", "v33", "v34", "v35", "v36", "v37", "v38", "v39", "v40", "v41", "v42", "v43", "v44", "v45", "v46", "v47", "v48", "v49", "v50", "v51", "v52", "v53", "v54", "v55", "v56", "v57", "v58", "v59", "v60", "v61", "v62", "v63", "v64", "v65", "v66", "v67", "v68", "v69", "v70", "v71", "v72", "v73", "v74", "v75", "v76", "v77", "v78", "v79", "v80", "v81", "v82", "v83", "v84", "v85", "v86", "v87", "v88", "v89", "v90", "v91", "v92", "v93", "v94", "v95", "v96", "v97", "v98", "v99", "v100", "v101", "v102", "v103", "v104", "v105", "v106", "v107", "v108", "v109", "v110", "v111", "v112", "v113", "v114", "v115", "v116", "v117", "v118", "v119", "v120", "v121", "v122", "v123", "v124", "v125", "v126", "v127", "v128", "v129", "v130", "v131", "v132", "v133", "v134", "v135", "v136", "v137", "v138", "v139", "v140", "v141", "v142", "v143", "v144", "v145", "v146", "v147", "v148", "v149", "v150", "v151", "v152", "v153", "v154", "v155", "v156", "v157", "v158", "v159", "v160", "v161", "v162", "v163", "v164", "v165", "v166", "v167", "v168", "v169", "v170", "v171", "v172", "v173", "v174", "v175", "v176", "v177", "v178", "v179", "v180", "v181", "v182", "v183", "v184", "v185", "v186", "v187", "v188", "v189", "v190", "v191", "v192", "v193", "v194", "v195", "v196", "v197", "v198", "v199", "v200", "v201", "v202", "v203", "v204", "v205", "v206", "v207", "v208", "v209", "v210", "v211", "v212", "v213", "v214", "v215", "v216", "v217", "v218", "v219", "v220", "v221", "v222", "v223", "v224", "v225", "v226", "a0", "a1", "a2", "a3", "a4", "a5", "a6", "a7", "a8", "a9", "a10", "a11", "a12", "a13", "a14", "a15", "a16", "a17", "a18", "a19", "a20", "a21", "a22", "a23", "a24", "a25", "a26", "a27", "a28", "a29", "a30", "a31", "a32", "a33", "a34", "a35", "a36", "a37", "a38", "a39", "a40", "a41", "a42", "a43", "a44", "a45", "a46", "a47", "a48", "a49", "a50", "a51", "a52", "a53", "a54", "a55", "a56", "a57", "a58", "a59", "a60", "a61", "a62", "a63", "a64", "a65", "a66", "a67", "a68", "a69", "a70", "a71", "a72", "a73", "a74", "a75", "a76", "a77", "a78", "a79", "a80", "a81", "a82", "a83", "a84", "a85", "a86", "a87", "a88", "a89", "a90", "a91", "a92", "a93", "a94", "a95", "a96", "a97", "a98", "a99", "a100", "a101", "a102", "a103", "a104", "a105", "a106", "a107", "a108", "a109", "a110", "a111", "a112", "a113", "a114", "a115", "a116", "a117", "a118", "a119", "a120", "a121", "a122", "a123", "a124", "a125", "a126", "a127", "a128", "a129", "a130", "a131", "a132", "a133", "a134", "a135", "a136", "a137", "a138", "a139", "a140", "a141", "a142", "a143", "a144", "a145", "a146", "a147", "a148", "a149", "a150", "a151", "a152", "a153", "a154", "a155", "a156", "a157", "a158", "a159", "a160", "a161", "a162", "a163", "a164", "a165", "a166", "a167", "a168", "a169", "a170", "a171", "a172", "a173", "a174", "a175", "a176", "a177", "a178", "a179", "a180", "a181", "a182", "a183", "a184", "a185", "a186", "a187", "a188", "a189", "a190", "a191", "a192", "a193", "a194", "a195", "a196", "a197", "a198", "a199", "a200", "a201", "a202", "a203", "a204", "a205", "a206", "a207", "a208", "a209", "a210", "a211", "a212", "a213", "a214", "a215", "a216", "a217", "a218", "a219", "a220", "a221", "a222", "a223", "a224", "a225", "a226", "a227", "a228", "a229", "a230", "a231", "a232", "a233", "a234", "a235", "a236", "a237", "a238", "a239", "s40", "s41", "s42", "s43", "s44", "s45", "s46", "s47", "s48", "s49", "s50", "s51", "s52", "s53", "s54", "s55", "s56", "s57", "s58", "s59", "s60", "s61", "s62", "s63", "s64", "s65", "s66", "s67", "s68", "s69", "s70", "s71", "s72", "s73", "s74", "s75", "s76", "s77", "s78", "s79", "s80", "s81", "s82", "s83", "s84", "s85", "s86", "s87");
  if (__builtin_expect((flagA | flagB) != 0ull, 0)) {
    unsigned l2; asm volatile("v_mbcnt_lo_u32_b32 %0, -1, 0\n\tv_mbcnt_hi_u32_b32 %0, -1, %0" : "=v"(l2));
    for (int r = 0; r < 4; ++r) {
      const unsigned long long f = (r & 1) ? flagB : flagA;
      if (f == 0ull) continue;
      const size_t hoff = (size_t)(head0 + 128u * r) * SK * D;
      for (int part = 0; part < 2; ++part) {
        const unsigned row = half * 192u + wid * 48u + 16u * part + (l2 & 31u);
        slow_block_impl(Q + hoff + (size_t)row * D, K + hoff, V + hoff, O + hoff + (size_t)row * D, (int)l2);
      }
    }
  }
}

extern "C" void kernel_launch(void* const* d_in, const int* in_sizes, int n_in, void* d_out, int out_size, void* d_ws, size_t ws_size, hipStream_t stream) {
  (void)in_sizes; (void)n_in; (void)out_size; (void)d_ws; (void)ws_size;
  (void)hipFuncSetAttribute((const void*)gqa_softmax_attn_kernel, hipFuncAttributeMaxDynamicSharedMemorySize, (int)LDSB);
  hipLaunchKernelGGL(gqa_softmax_attn_kernel, dim3(256), dim3(256), LDSB, stream,
                     (const __bf16*)d_in[0], (const __bf16*)d_in[1], (const __bf16*)d_in[2], (__bf16*)d_out);
}
```
